```python
import torch

def _softmax(x, axis=-1):
    return torch.softmax(x, dim=axis)
def _gelu(x):
    return torch.nn.functional.gelu(x, approximate="tanh")
def _silu(x):
    return torch.nn.functional.silu(x)
def _sigmoid(x):
    return torch.sigmoid(x)
def _layernorm(x, axis=-1, eps=1e-5):
    m = x.mean(dim=axis, keepdim=True)
    v = x.var(dim=axis, keepdim=True, unbiased=False)
    return (x - m) / torch.sqrt(v + eps)
def _scatter_add(tgt, idx, src, axis=0):
    return tgt.clone().index_add_(axis, idx, src)
def _segment_sum(src, seg, n):
    out = torch.zeros((n,) + src.shape[1:], dtype=src.dtype)
    return out.index_add_(0, seg, src)
def _pad(x, pw):
    flat = tuple(v for pair in reversed(pw) for v in pair)
    return torch.nn.functional.pad(x, flat)

kernel_name = 'concat_heads__mha_block_cached__gqa_block_cached__near_ridge'

def setup_inputs():

    _w = (lambda a: (lambda t: t.to(torch.bfloat16) if t.is_floating_point() else t)(torch.as_tensor(a)))
    def _A_setup():
        B, Sq, D = 16, 96, 1024
        H, Sk, Dh = 32, 512, 128
        return {
            "x": _w((lambda *s: torch.randn(*s, dtype=torch.bfloat16) * 0.8)(B, Sq, D)),
            "k_cache": _w((lambda *s: torch.randn(*s, dtype=torch.bfloat16) * 0.8)(B, H, Sk, Dh)),
            "v_cache": _w((lambda *s: torch.randn(*s, dtype=torch.bfloat16) * 0.05)(B, H, Sk, Dh)),
            "wq": _w((lambda *s: torch.randn(*s, dtype=torch.bfloat16) * 0.8)(D, H * Dh) * (D ** -0.5)),
            "wo": _w((lambda *s: torch.randn(*s, dtype=torch.bfloat16) * 0.05)(H * Dh, D) * ((H * Dh) ** -0.5)),
        }
    _A_in = _A_setup()

    def _B_setup():
        B, Sq, D = 16, 96, 1024
        Hq, Hkv, Sk, Dh = 32, 32, 512, 128
        assert Hq % Hkv == 0, (Hq, Hkv)
        return {
            "x": _w((lambda *s: torch.randn(*s, dtype=torch.bfloat16) * 0.8)(B, Sq, D)),
            "k_cache": _w((lambda *s: torch.randn(*s, dtype=torch.bfloat16) * 0.8)(B, Hkv, Sk, Dh)),
            "v_cache": _w((lambda *s: torch.randn(*s, dtype=torch.bfloat16) * 0.05)(B, Hkv, Sk, Dh)),
            "wq": _w((lambda *s: torch.randn(*s, dtype=torch.bfloat16) * 0.8)(D, Hq * Dh) * (D ** -0.5)),
            "wo": _w((lambda *s: torch.randn(*s, dtype=torch.bfloat16) * 0.05)(Hq * Dh, D) * ((Hq * Dh) ** -0.5)),
        }
    _B_in = _B_setup()

    return {
        "A_x": _A_in["x"],
        "A_k_cache": _A_in["k_cache"],
        "A_v_cache": _A_in["v_cache"],
        "A_wq": _A_in["wq"],
        "A_wo": _A_in["wo"],
        "B_x": _B_in["x"],
        "B_k_cache": _B_in["k_cache"],
        "B_v_cache": _B_in["v_cache"],
        "B_wq": _B_in["wq"],
        "B_wo": _B_in["wo"],
    }

def reference(A_x, A_k_cache, A_v_cache, A_wq, A_wo, B_x, B_k_cache, B_v_cache, B_wq, B_wo):
    def _A_ref(x, k_cache, v_cache, wq, wo):
        B, Sq, D = x.shape
        H, Dh = 32, 128
        q = (lambda x, *d: x.permute(*d))((x @ wq).reshape(B, Sq, H, Dh), 0, 2, 1, 3)
        scores = torch.einsum("bhqd,bhkd->bhqk", q, k_cache) * (Dh ** -0.5)
        p = _softmax(scores, axis=-1)
        o = torch.einsum("bhqk,bhkd->bhqd", p, v_cache)
        return (lambda x, *d: x.permute(*d))(o, 0, 2, 1, 3).reshape(B, Sq, H * Dh) @ wo

    def _B_ref(x, k_cache, v_cache, wq, wo):
        B, Sq, D = x.shape
        Hq, Dh = 32, 128
        Hkv = k_cache.shape[1]
        rep = Hq // Hkv
        q = (lambda x, *d: x.permute(*d))((x @ wq).reshape(B, Sq, Hq, Dh), 0, 2, 1, 3)
        k = (lambda x, n, axis: torch.repeat_interleave(x, n, dim=axis))(k_cache, rep, axis=1)
        v = (lambda x, n, axis: torch.repeat_interleave(x, n, dim=axis))(v_cache, rep, axis=1)
        scores = torch.einsum("bhqd,bhkd->bhqk", q, k) * (Dh ** -0.5)
        p = _softmax(scores, axis=-1)
        o = torch.einsum("bhqk,bhkd->bhqd", p, v)
        return (lambda x, *d: x.permute(*d))(o, 0, 2, 1, 3).reshape(B, Sq, Hq * Dh) @ wo

    a = _A_ref(A_x, A_k_cache, A_v_cache, A_wq, A_wo)
    b = _B_ref(B_x, B_k_cache, B_v_cache, B_wq, B_wo)
    return torch.concatenate([a, b], axis=-1)
```

```cpp
#include <hip/hip_runtime.h>
#include <hip/hip_bf16.h>
typedef const char* cptr;

__global__ __launch_bounds__(256, 1) void fused_kernel(cptr xA, cptr kA, cptr vA, cptr wqA, cptr woA, cptr xB, cptr kB, cptr vB, cptr wqB, cptr woB,
                                                        char* ws, char* out) {
  __shared__ __attribute__((aligned(16))) char smem[163840];
  const unsigned long long ka = (unsigned long long)__builtin_amdgcn_kernarg_segment_ptr();
  const unsigned w = __builtin_amdgcn_readfirstlane(threadIdx.x >> 6);
  asm volatile(
R"ASM(
.macro MM d, a, b ; D(vgpr) += A(agpr) * B(agpr)
v_mfma_f32_16x16x32_bf16 v[\d:\d+3], a[\a:\a+3], a[\b:\b+3], v[\d:\d+3]
.endm
.macro MS d, a, b ; D(vgpr) += A(agpr) * B(vgpr)
v_mfma_f32_16x16x32_bf16 v[\d:\d+3], a[\a:\a+3], v[\b:\b+3], v[\d:\d+3]
.endm
.macro MP d, a, b ; D(agpr) += A(agpr) * B(vgpr)
v_mfma_f32_16x16x32_bf16 a[\d:\d+3], a[\a:\a+3], v[\b:\b+3], a[\d:\d+3]
.endm
.macro RT d, v, o ; transposed LDS read
ds_read_b64_tr_b16 a[\d:\d+1], v\v offset:\o
.endm
.macro RB d, v, o ; 16-byte LDS read
ds_read_b128 a[\d:\d+3], v\v offset:\o
.endm
.macro DM v, r, s, o ; global -> LDS copy of 1 KB
buffer_load_dwordx4 v\v, s[\r:\r+3], s\s offen offset:\o lds
.endm
.macro EX r
v_exp_f32 v\r, v\r
.endm
.macro CV d, a, b
v_cvt_pk_bf16_f32 v\d, v\a, v\b
.endm
)ASM"
R"ASM(s_bfe_u32 s71, %[bid], 0x10002
s_mul_i32 s80, s71, 40
s_load_dwordx8 s[52:59], %[ka], s80
s_load_dwordx2 s[60:61], %[ka], s80 offset:32
s_load_dwordx4 s[64:67], %[ka], 80
s_mov_b32 s70, %[w]
s_and_b32 s72, %[bid], 3
s_bfe_u32 s73, %[bid], 0x30003
s_lshr_b32 s74, %[bid], 6
s_lshr_b32 s75, s73, 2
s_add_u32 s80, s73, s74
s_and_b32 s80, s80, 3
s_mov_b32 s78, 0x3213214
s_cmp_eq_u32 s80, 1
s_cselect_b32 s78, 0x13203250, s78
s_cmp_eq_u32 s80, 2
s_cselect_b32 s78, 0x23103610, s78
s_cmp_eq_u32 s80, 3
s_cselect_b32 s78, 0x32107210, s78
v_mbcnt_lo_u32_b32 v192, -1, 0
v_mbcnt_hi_u32_b32 v192, -1, v192
v_and_b32 v193, 7, v192
v_lshrrev_b32 v194, 4, v192
v_xor_b32 v193, v193, v194
v_lshrrev_b32 v194, 3, v192
v_lshlrev_b32 v194, 11, v194
s_mul_i32 s80, s70, 3
s_add_u32 s81, s80, 0
s_and_b32 s82, s81, 1
s_lshl_b32 s82, s82, 2
v_xor_b32 v195, s82, v193
v_lshl_add_u32 v195, v195, 4, v194
s_lshl_b32 s81, s81, 14
s_sub_u32 s81, s81, 0
v_add_u32 v212, s81, v195
s_add_u32 s81, s80, 1
s_and_b32 s82, s81, 1
s_lshl_b32 s82, s82, 2
v_xor_b32 v195, s82, v193
v_lshl_add_u32 v195, v195, 4, v194
s_lshl_b32 s81, s81, 14
s_sub_u32 s81, s81, 1024
v_add_u32 v213, s81, v195
s_add_u32 s81, s80, 2
s_and_b32 s82, s81, 1
s_lshl_b32 s82, s82, 2
v_xor_b32 v195, s82, v193
v_lshl_add_u32 v195, v195, 4, v194
s_lshl_b32 s81, s81, 14
s_sub_u32 s81, s81, 2048
v_add_u32 v214, s81, v195
s_mul_i32 s85, s70, 3072
s_add_u32 s85, s85, 98304
s_and_b32 s80, s70, 1
s_lshl_b32 s80, s80, 17
s_lshr_b32 s82, s70, 1
s_lshl_b32 s82, s82, 8
s_add_u32 s80, s80, s82
s_lshr_b32 s86, s70, 1
s_mul_i32 s86, s86, 40960
s_lshl_b32 s81, s70, 12
s_add_u32 s86, s86, s81
s_lshl_b32 s87, s75, 18
s_xor_b32 s88, s87, 262144
v_lshrrev_b32 v194, 4, v192
v_lshlrev_b32 v195, 2, v194
v_and_b32 v193, 15, v192
v_xor_b32 v193, v193, v195
v_lshlrev_b32 v193, 4, v193
v_lshl_add_u32 v193, v194, 13, v193
s_add_u32 s81, s80, 0
v_add_u32 v216, s81, v193
s_add_u32 s81, s80, 31744
v_add_u32 v217, s81, v193
v_xor_b32 v193, 32, v193
s_add_u32 s81, s80, 63488
v_add_u32 v218, s81, v193
s_add_u32 s81, s80, 95232
v_add_u32 v219, s81, v193
s_mul_i32 s80, s73, 393216
s_lshl2_add_u32 s82, s74, s72
s_lshl_b32 s81, s82, 9
s_sub_u32 s95, 8388608, s81
s_waitcnt lgkmcnt(0)
s_add_u32 s8, s52, s80
s_addc_u32 s9, s53, 0
s_and_b32 s9, s9, 0xffff
s_mov_b32 s10, 393216
s_mov_b32 s11, 0x20000
s_add_u32 m0, s85, 0
s_bfe_u32 s83, s78, 0x4001c
s_mul_i32 s83, s83, 7
s_and_b32 s83, s83, 15
s_lshl_b32 s83, s83, 7
DM 212,8,83,0
DM 213,8,83,1024
DM 214,8,83,2048
s_add_u32 m0, s85, 12288
s_bfe_u32 s83, s78, 0x4001c
s_mul_i32 s83, s83, 7
s_and_b32 s83, s83, 15
s_lshl_b32 s83, s83, 7
s_add_u32 s83, s83, 196608
DM 212,8,83,0
DM 213,8,83,1024
DM 214,8,83,2048
s_add_u32 s12, s58, s81
s_addc_u32 s13, s59, 0
s_and_b32 s13, s13, 0xffff
s_mov_b32 s14, s95
s_mov_b32 s15, 0x20000
s_add_u32 m0, s86, 0
s_bfe_u32 s84, s78, 0x4001c
s_mul_i32 s84, s84, 7
s_and_b32 s84, s84, 15
s_lshl_b32 s84, s84, 19
s_add_u32 s84, s84, s87
DM 216,12,84,0
DM 217,12,84,1024
DM 218,12,84,2048
DM 219,12,84,3072
s_add_u32 m0, s86, 8192
s_bfe_u32 s84, s78, 0x4001c
s_mul_i32 s84, s84, 7
s_and_b32 s84, s84, 15
s_lshl_b32 s84, s84, 19
s_add_u32 s84, s84, s88
DM 216,12,84,0
DM 217,12,84,1024
DM 218,12,84,2048
DM 219,12,84,3072
s_lshl3_add_u32 s80, s71, s73
s_lshl_b32 s81, s80, 12
s_add_u32 s81, s81, 41943040
s_add_u32 s40, s64, s81
s_addc_u32 s41, s65, 0
s_and_b32 s41, s41, 0xffff
s_mov_b32 s42, 4096
s_mov_b32 s43, 0x20000
s_lshl2_add_u32 s95, s72, s74
s_lshl2_add_u32 s77, s95, s70
s_lshl_b32 s77, s77, 2
buffer_load_dword v197, off, s[40:43], s77 offset:2816 sc1
s_lshr_b32 s81, %[bid], 3
s_lshl2_add_u32 s81, s81, s70
s_lshl_b32 s81, s81, 8
v_lshl_add_u32 v211, v192, 7, s81
s_getpc_b64 s[62:63]
L_w1:
s_add_u32 s62, s62, L_c0-L_w1
s_addc_u32 s63, s63, 0
v_min_u32 v196, L_q2-L_c0, v211
s_mov_b64 exec, 3
global_load_dword v215, v196, s[62:63]
s_mov_b64 exec, -1
s_getpc_b64 s[62:63]
L_w2:
s_add_u32 s62, s62, L_q2-L_w2
s_addc_u32 s63, s63, 0
v_min_u32 v196, L_c1-L_q2, v211
s_mov_b64 exec, 3
global_load_dword v215, v196, s[62:63]
s_mov_b64 exec, -1
L_c0: ; ---- set-up (descriptors, lane addresses), then Q projection, schedule variant 0
s_cmp_lg_u32 s77, 0
s_cbranch_scc1 L_nz
v_mov_b32 v193, 0
v_lshlrev_b32 v194, 7, v192
s_mov_b64 exec, 0x3ffff
buffer_store_dword v193, v194, s[40:43], 0 offen sc1
s_mov_b64 exec, -1
L_nz:
s_and_b32 s93, s70, 1
s_lshl1_add_u32 s93, s73, s93
s_lshr_b32 s94, s70, 1
s_lshl1_add_u32 s94, s82, s94
s_mul_i32 s76, s93, 1
s_mul_i32 s81, s94, 1
s_add_u32 s76, s76, s81
s_mul_i32 s81, s71, 0
s_add_u32 s76, s76, s81
s_and_b32 s76, s76, 15
s_lshl_b32 s93, s93, 5
s_add_u32 s93, s93, s94
s_lshl_b32 s81, s93, 17
s_add_u32 s16, s54, s81
s_addc_u32 s17, s55, 0
s_and_b32 s17, s17, 0xffff
s_mov_b32 s18, 131072
s_mov_b32 s19, 0x20000
s_add_u32 s20, s56, s81
s_addc_u32 s21, s57, 0
s_and_b32 s21, s21, 0xffff
s_mov_b32 s22, 131072
s_mov_b32 s23, 0x20000
s_lshl_b32 s81, s71, 9
s_add_u32 s93, s93, s81
s_mul_i32 s81, s93, 24576
s_add_u32 s24, s64, s81
s_addc_u32 s25, s65, 0
s_and_b32 s25, s25, 0xffff
s_mov_b32 s26, 24576
s_mov_b32 s27, 0x20000
s_mul_i32 s81, s80, 1572864
s_add_u32 s28, s64, s81
s_addc_u32 s29, s65, 0
s_and_b32 s29, s29, 0xffff
s_mov_b32 s30, 1572864
s_mov_b32 s31, 0x20000
s_lshr_b32 s93, s72, 1
s_lshl2_add_u32 s93, s93, s74
s_lshl_b32 s93, s93, 8
s_sub_u32 s94, 8388608, s93
s_add_u32 s32, s60, s93
s_addc_u32 s33, s61, 0
s_and_b32 s33, s33, 0xffff
s_mov_b32 s34, s94
s_mov_b32 s35, 0x20000
s_lshl_b32 s94, s71, 11
s_add_u32 s93, s93, s94
s_and_b32 s94, s72, 1
s_lshl1_add_u32 s94, s73, s94
s_mul_i32 s94, s94, 393216
s_add_u32 s94, s94, s93
s_sub_u32 s93, 393216, s93
s_add_u32 s36, s66, s94
s_addc_u32 s37, s67, 0
s_and_b32 s37, s37, 0xffff
s_mov_b32 s38, s93
s_mov_b32 s39, 0x20000
s_lshr_b32 s56, s70, 1
s_and_b32 s55, s70, 1
s_lshl4_add_u32 s93, s80, s95
s_lshl1_add_u32 s94, s93, s55
s_mul_i32 s94, s94, 24576
s_add_u32 s94, s94, 46137344
s_mul_i32 s81, s56, 24576
s_add_u32 s44, s64, s94
s_addc_u32 s45, s65, 0
s_and_b32 s45, s45, 0xffff
s_mov_b32 s46, s81
s_mov_b32 s47, 0x20000
s_xor_b32 s93, s93, 4
s_lshl1_add_u32 s94, s93, s55
s_mul_i32 s94, s94, 24576
s_mul_i32 s81, s56, 6144
s_add_u32 s94, s94, s81
s_add_u32 s94, s94, 46137344
s_add_u32 s48, s64, s94
s_addc_u32 s49, s65, 0
s_and_b32 s49, s49, 0xffff
s_mov_b32 s50, 12288
s_mov_b32 s51, 0x20000
s_add_u32 s58, s95, 2
s_lshl_b32 s58, s58, 7
s_xor_b32 s57, s95, 4
s_add_u32 s57, s57, 2
s_lshl_b32 s57, s57, 7
s_and_b32 s93, s72, 1
s_mul_i32 s98, s93, 56
s_add_u32 s98, s98, s75
s_mul_i32 s99, s56, 8
s_add_u32 s99, s99, 112
s_add_u32 s99, s99, s75
s_mul_i32 s94, s55, 3072
s_mul_i32 s101, s93, 786432
s_add_u32 s101, s101, s94
s_xor_b32 s93, s93, s56
s_mul_i32 s100, s93, 786432
s_add_u32 s100, s100, s94
s_lshl_b32 s96, s70, 11
s_mul_i32 s97, s56, 81920
s_lshl_b32 s94, s55, 12
s_add_u32 s97, s97, s94
v_and_b32 v193, 15, v192
v_lshlrev_b32 v194, 7, v193
s_mul_i32 s80, s55, 12288
s_add_u32 s80, s80, 98304
v_add_u32 v194, s80, v194
v_lshrrev_b32 v193, 1, v193
v_lshrrev_b32 v195, 4, v192
v_xor_b32 v193, v193, v195
s_lshl_b32 s80, s75, 2
v_xor_b32 v193, s80, v193
v_xor_b32 v195, 0, v193
v_lshl_add_u32 v208, v195, 4, v194
v_xor_b32 v195, 4, v193
v_lshl_add_u32 v209, v195, 4, v194
s_mul_i32 s80, s56, 49152
v_lshrrev_b32 v193, 2, v192
v_and_b32 v193, 3, v193
v_lshrrev_b32 v194, 4, v192
v_lshl_add_u32 v195, v194, 3, v193
v_lshlrev_b32 v195, 8, v195
v_and_b32 v194, 1, v194
v_lshl_add_u32 v193, v193, 1, v194
v_xor_b32 v193, 0, v193
v_and_b32 v194, 3, v192
v_lshl_add_u32 v195, v194, 3, v195
v_add_u32 v195, s80, v195
v_xor_b32 v194, 0, v193
v_lshl_add_u32 v200, v194, 5, v195
v_xor_b32 v194, 1, v193
v_lshl_add_u32 v201, v194, 5, v195
v_xor_b32 v194, 2, v193
v_lshl_add_u32 v202, v194, 5, v195
v_xor_b32 v194, 3, v193
v_lshl_add_u32 v203, v194, 5, v195
v_xor_b32 v194, 4, v193
v_lshl_add_u32 v204, v194, 5, v195
v_xor_b32 v194, 5, v193
v_lshl_add_u32 v205, v194, 5, v195
v_xor_b32 v194, 6, v193
v_lshl_add_u32 v206, v194, 5, v195
v_xor_b32 v194, 7, v193
v_lshl_add_u32 v207, v194, 5, v195
s_mul_i32 s89, s70, 40960
v_and_b32 v193, 15, v192
v_lshlrev_b32 v194, 8, v193
v_add_u32 v194, s89, v194
v_lshrrev_b32 v195, 5, v192
v_xor_b32 v193, v193, v195
v_lshrrev_b32 v195, 3, v192
v_and_b32 v195, 2, v195
v_xor_b32 v193, v193, v195
v_xor_b32 v195, 0, v193
v_lshl_add_u32 v220, v195, 4, v194
v_xor_b32 v195, 4, v193
v_lshl_add_u32 v221, v195, 4, v194
v_xor_b32 v195, 8, v193
v_lshl_add_u32 v222, v195, 4, v194
v_xor_b32 v195, 12, v193
v_lshl_add_u32 v223, v195, 4, v194
v_lshrrev_b32 v193, 2, v192
v_and_b32 v193, 3, v193
v_lshrrev_b32 v194, 4, v192
v_lshl_add_u32 v195, v194, 2, v193
v_lshlrev_b32 v195, 8, v195
v_and_b32 v194, 1, v194
v_lshl_add_u32 v193, v194, 2, v193
v_xor_b32 v193, 0, v193
v_and_b32 v194, 3, v192
v_lshl_add_u32 v195, v194, 3, v195
v_add_u32 v195, s89, v195
v_xor_b32 v194, 0, v193
v_lshl_add_u32 v224, v194, 5, v195
v_xor_b32 v194, 1, v193
v_lshl_add_u32 v225, v194, 5, v195
v_xor_b32 v194, 2, v193
v_lshl_add_u32 v226, v194, 5, v195
v_xor_b32 v194, 3, v193
v_lshl_add_u32 v227, v194, 5, v195
v_xor_b32 v194, 4, v193
v_lshl_add_u32 v228, v194, 5, v195
v_xor_b32 v194, 5, v193
v_lshl_add_u32 v229, v194, 5, v195
v_xor_b32 v194, 6, v193
v_lshl_add_u32 v230, v194, 5, v195
v_xor_b32 v194, 7, v193
v_lshl_add_u32 v231, v194, 5, v195
v_lshrrev_b32 v196, 4, v192
v_bfe_u32 v195, v196, 0, 1
v_lshlrev_b32 v194, 8, v195
v_bfe_u32 v195, v196, 1, 1
v_lshl_add_u32 v194, v195, 9, v194
v_and_b32 v193, 15, v192
v_xor_b32 v193, v193, v196
v_xor_b32 v195, 0, v193
v_lshl_add_u32 v195, v195, 4, v194
v_add_u32 v232, 0, v195
v_xor_b32 v195, 4, v193
v_lshl_add_u32 v195, v195, 4, v194
v_add_u32 v233, 7168, v195
v_xor_b32 v195, 8, v193
v_lshl_add_u32 v195, v195, 4, v194
v_add_u32 v234, 14336, v195
v_xor_b32 v195, 12, v193
v_lshl_add_u32 v195, v195, 4, v194
v_add_u32 v235, 21504, v195
v_lshlrev_b32 v196, 1, v196
v_and_b32 v193, 15, v192
v_xor_b32 v193, v193, v196
v_xor_b32 v195, 0, v193
v_lshl_add_u32 v195, v195, 4, v194
v_add_u32 v236, 0, v195
v_xor_b32 v195, 8, v193
v_lshl_add_u32 v195, v195, 4, v194
v_add_u32 v199, 7168, v195
v_xor_b32 v195, 0, v193
v_lshl_add_u32 v195, v195, 4, v194
v_add_u32 v238, 14336, v195
v_xor_b32 v195, 8, v193
v_lshl_add_u32 v195, v195, 4, v194
v_add_u32 v239, 21504, v195
v_lshlrev_b32 v237, 4, v192
v_add_u32 v198, s89, v237
s_mov_b32 s59, 0xffff63ff
s_bfe_u32 s80, s76, 0x10000
s_lshl_b32 s80, s80, 10
s_or_b32 s59, s59, s80
s_bfe_u32 s80, s76, 0x10001
s_lshl_b32 s80, s80, 11
s_or_b32 s59, s59, s80
s_bfe_u32 s80, s76, 0x10002
s_lshl_b32 s80, s80, 12
s_or_b32 s59, s59, s80
s_bfe_u32 s80, s76, 0x10003
s_lshl_b32 s80, s80, 15
s_or_b32 s59, s59, s80
s_mov_b32 s91, 0x41000000
s_mov_b32 s68, 0x3e0293ee
s_mov_b32 s69, 0x3e0293ee
s_waitcnt vmcnt(7)
s_barrier
RT 176,200,0
RT 178,200,1024
RB 192,208,0
RB 196,208,2048
RB 200,208,4096
RB 204,208,6144
RB 208,208,8192
RB 212,208,10240
RT 180,201,0
RT 182,201,1024
RT 184,202,0
RT 186,202,1024
RT 188,203,0
RT 190,203,1024
s_waitcnt lgkmcnt(6)
s_bitcmp1_b32 s73, 0
s_cbranch_scc1 L_q2
v_mfma_f32_16x16x32_bf16 v[0:3], a[176:179], a[192:195], 0
s_add_u32 m0, s85, 24576
s_bfe_u32 s83, s78, 0x40000
s_mul_i32 s83, s83, 7
v_mfma_f32_16x16x32_bf16 v[32:35], a[176:179], a[196:199], 0
s_and_b32 s83, s83, 15
s_lshl_b32 s83, s83, 7
RB 216,209,0
v_mfma_f32_16x16x32_bf16 v[64:67], a[176:179], a[200:203], 0
RB 220,209,2048
RB 224,209,4096
v_mfma_f32_16x16x32_bf16 v[96:99], a[176:179], a[204:207], 0
RB 228,209,6144
RB 232,209,8192
v_mfma_f32_16x16x32_bf16 v[128:131], a[176:179], a[208:211], 0
DM 212,8,83,0
v_mfma_f32_16x16x32_bf16 v[160:163], a[176:179], a[212:215], 0
s_waitcnt lgkmcnt(9)
RT 176,204,0
RT 178,204,1024
v_mfma_f32_16x16x32_bf16 v[164:167], a[180:183], a[212:215], 0
RB 236,209,10240
v_mfma_f32_16x16x32_bf16 v[132:135], a[180:183], a[208:211], 0
v_mfma_f32_16x16x32_bf16 v[100:103], a[180:183], a[204:207], 0
v_mfma_f32_16x16x32_bf16 v[68:71], a[180:183], a[200:203], 0
DM 213,8,83,1024
v_mfma_f32_16x16x32_bf16 v[36:39], a[180:183], a[196:199], 0
v_mfma_f32_16x16x32_bf16 v[4:7], a[180:183], a[192:195], 0
s_waitcnt lgkmcnt(10)
RT 180,205,0
RT 182,205,1024
v_mfma_f32_16x16x32_bf16 v[8:11], a[184:187], a[192:195], 0
v_mfma_f32_16x16x32_bf16 v[40:43], a[184:187], a[196:199], 0
v_mfma_f32_16x16x32_bf16 v[72:75], a[184:187], a[200:203], 0
DM 214,8,83,2048
v_mfma_f32_16x16x32_bf16 v[104:107], a[184:187], a[204:207], 0
s_add_u32 m0, s85, 36864
s_bfe_u32 s83, s78, 0x40000
s_mul_i32 s83, s83, 7
v_mfma_f32_16x16x32_bf16 v[136:139], a[184:187], a[208:211], 0
s_and_b32 s83, s83, 15
s_lshl_b32 s83, s83, 7
s_add_u32 s83, s83, 196608
v_mfma_f32_16x16x32_bf16 v[168:171], a[184:187], a[212:215], 0
s_waitcnt lgkmcnt(10)
RT 184,206,0
RT 186,206,1024
v_mfma_f32_16x16x32_bf16 v[172:175], a[188:191], a[212:215], 0
v_mfma_f32_16x16x32_bf16 v[140:143], a[188:191], a[208:211], 0
DM 212,8,83,0
v_mfma_f32_16x16x32_bf16 v[108:111], a[188:191], a[204:207], 0
s_waitcnt vmcnt(7)
s_barrier
v_mfma_f32_16x16x32_bf16 v[76:79], a[188:191], a[200:203], 0
v_mfma_f32_16x16x32_bf16 v[44:47], a[188:191], a[196:199], 0
v_mfma_f32_16x16x32_bf16 v[12:15], a[188:191], a[192:195], 0
s_waitcnt lgkmcnt(5)
RT 188,207,0
RT 190,207,1024
v_mfma_f32_16x16x32_bf16 v[16:19], a[176:179], a[192:195], 0
DM 213,8,83,1024
v_mfma_f32_16x16x32_bf16 v[48:51], a[176:179], a[196:199], 0
s_bfe_u32 s84, s78, 0x40000
s_mul_i32 s84, s84, 7
s_and_b32 s84, s84, 15
v_mfma_f32_16x16x32_bf16 v[80:83], a[176:179], a[200:203], 0
s_lshl_b32 s84, s84, 19
s_add_u32 s84, s84, s87
v_mfma_f32_16x16x32_bf16 v[112:115], a[176:179], a[204:207], 0
v_mfma_f32_16x16x32_bf16 v[144:147], a[176:179], a[208:211], 0
v_mfma_f32_16x16x32_bf16 v[176:179], a[176:179], a[212:215], 0
DM 214,8,83,2048
s_waitcnt lgkmcnt(4)
v_mfma_f32_16x16x32_bf16 v[180:183], a[180:183], a[212:215], 0
s_add_u32 m0, s86, 16384
RT 176,200,8192
RT 178,200,9216
v_mfma_f32_16x16x32_bf16 v[148:151], a[180:183], a[208:211], 0
v_mfma_f32_16x16x32_bf16 v[116:119], a[180:183], a[204:207], 0
v_mfma_f32_16x16x32_bf16 v[84:87], a[180:183], a[200:203], 0
v_mfma_f32_16x16x32_bf16 v[52:55], a[180:183], a[196:199], 0
DM 216,12,84,0
v_mfma_f32_16x16x32_bf16 v[20:23], a[180:183], a[192:195], 0
s_waitcnt lgkmcnt(4)
RT 180,201,8192
RT 182,201,9216
v_mfma_f32_16x16x32_bf16 v[24:27], a[184:187], a[192:195], 0
v_mfma_f32_16x16x32_bf16 v[56:59], a[184:187], a[196:199], 0
v_mfma_f32_16x16x32_bf16 v[88:91], a[184:187], a[200:203], 0
v_mfma_f32_16x16x32_bf16 v[120:123], a[184:187], a[204:207], 0
DM 217,12,84,1024
v_mfma_f32_16x16x32_bf16 v[152:155], a[184:187], a[208:211], 0
v_mfma_f32_16x16x32_bf16 v[184:187], a[184:187], a[212:215], 0
s_waitcnt lgkmcnt(4)
RT 184,202,8192
RT 186,202,9216
v_mfma_f32_16x16x32_bf16 v[188:191], a[188:191], a[212:215], 0
v_mfma_f32_16x16x32_bf16 v[156:159], a[188:191], a[208:211], 0
v_mfma_f32_16x16x32_bf16 v[124:127], a[188:191], a[204:207], 0
DM 218,12,84,2048
v_mfma_f32_16x16x32_bf16 v[92:95], a[188:191], a[200:203], 0
v_mfma_f32_16x16x32_bf16 v[60:63], a[188:191], a[196:199], 0
v_mfma_f32_16x16x32_bf16 v[28:31], a[188:191], a[192:195], 0
s_waitcnt lgkmcnt(4)
RT 188,203,8192
RT 190,203,9216
MM 0,176,216
MM 32,176,220
s_barrier
MM 64,176,224
DM 219,12,84,3072
MM 96,176,228
s_add_u32 m0, s86, 24576
s_bfe_u32 s84, s78, 0x40000
s_mul_i32 s84, s84, 7
MM 128,176,232
s_and_b32 s84, s84, 15
s_lshl_b32 s84, s84, 19
s_add_u32 s84, s84, s88
MM 160,176,236
s_waitcnt lgkmcnt(4)
RT 176,204,8192
RT 178,204,9216
MM 164,180,236
MM 132,180,232
DM 216,12,84,0
MM 100,180,228
MM 68,180,224
MM 36,180,220
MM 4,180,216
s_waitcnt lgkmcnt(4)
RT 180,205,8192
RT 182,205,9216
MM 8,184,216
DM 217,12,84,1024
MM 40,184,220
MM 72,184,224
MM 104,184,228
MM 136,184,232
MM 168,184,236
DM 218,12,84,2048
s_waitcnt lgkmcnt(4)
MM 172,188,236
s_bfe_u32 s83, s78, 0x40004
s_mul_i32 s83, s83, 7
s_and_b32 s83, s83, 15
MM 140,188,232
s_lshl_b32 s83, s83, 7
RT 184,206,8192
RT 186,206,9216
MM 108,188,228
MM 76,188,224
MM 44,188,220
DM 219,12,84,3072
MM 12,188,216
s_waitcnt lgkmcnt(4)
s_add_u32 m0, s85, 0
RT 188,207,8192
MM 16,176,216
RT 190,207,9216
MM 48,176,220
MM 80,176,224
MM 112,176,228
DM 212,8,83,0
MM 144,176,232
MM 176,176,236
s_waitcnt lgkmcnt(4)
MM 180,180,236
MM 148,180,232
MM 116,180,228
DM 213,8,83,1024
MM 84,180,224
MM 52,180,220
MM 20,180,216
s_waitcnt lgkmcnt(2)
MM 24,184,216
s_waitcnt vmcnt(2)
s_barrier
MM 56,184,220
RT 176,200,16384
RT 178,200,17408
MM 88,184,224
RT 180,200,24576
RT 182,200,25600
MM 120,184,228
RB 240,208,34816
RB 244,209,34816
MM 152,184,232
RB 248,209,32768
RB 252,208,32768
MM 184,184,236
DM 214,8,83,2048
s_waitcnt lgkmcnt(8)
MM 188,188,236
s_add_u32 m0, s85, 12288
s_bfe_u32 s83, s78, 0x40004
s_mul_i32 s83, s83, 7
MM 156,188,232
s_and_b32 s83, s83, 15
s_lshl_b32 s83, s83, 7
s_add_u32 s83, s83, 196608
MM 124,188,228
RB 212,208,30720
RB 208,209,30720
MM 92,188,224
RB 204,209,28672
RB 200,208,28672
MM 60,188,220
DM 212,8,83,0
MM 28,188,216
s_waitcnt lgkmcnt(4)
RB 196,208,26624
RB 192,209,26624
MM 160,176,240
RB 236,209,24576
RB 232,208,24576
MM 160,180,244
RT 184,201,16384
RT 186,201,17408
MM 128,180,248
RT 188,201,24576
RT 190,201,25600
MM 128,176,252
s_waitcnt lgkmcnt(8)
v_add_u32 v197, 1, v197
MM 96,176,212
DM 213,8,83,1024
MM 96,180,208
s_bfe_u32 s84, s78, 0x40004
s_mul_i32 s84, s84, 7
s_and_b32 s84, s84, 15
MM 64,180,204
s_lshl_b32 s84, s84, 19
s_add_u32 s84, s84, s87
MM 64,176,200
s_waitcnt lgkmcnt(4)
MM 32,176,196
MM 32,180,192
s_barrier
MM 0,180,236
DM 214,8,83,2048
MM 0,176,232
s_waitcnt lgkmcnt(0)
s_add_u32 m0, s86, 32768
RT 180,202,16384
MM 4,184,232
RT 182,202,17408
RT 176,202,24576
MM 4,188,236
RT 178,202,25600
MM 36,188,192
MM 36,184,196
DM 216,12,84,0
MM 68,184,200
buffer_store_dword v197, off, s[40:43], s77 offset:2560 sc1
MM 68,188,204
MM 100,188,208
MM 100,184,212
MM 132,184,252
DM 217,12,84,1024
MM 132,188,248
MM 164,188,244
RT 188,203,16384
RT 190,203,17408
MM 164,184,240
s_waitcnt lgkmcnt(2)
RT 184,203,24576
RT 186,203,25600
MM 168,180,240
MM 168,176,244
DM 218,12,84,2048
MM 136,176,248
MM 136,180,252
MM 104,180,212
MM 104,176,208
MM 72,176,204
DM 219,12,84,3072
MM 72,180,200
s_add_u32 m0, s86, 40960
s_bfe_u32 s84, s78, 0x40004
s_mul_i32 s84, s84, 7
MM 40,180,196
s_and_b32 s84, s84, 15
s_lshl_b32 s84, s84, 19
s_add_u32 s84, s84, s88
MM 40,176,192
MM 8,176,236
RT 176,204,16384
RT 178,204,17408
MM 8,180,232
s_waitcnt lgkmcnt(2)
RT 180,204,24576
RT 182,204,25600
MM 12,188,232
DM 216,12,84,0
MM 12,184,236
MM 44,184,192
MM 44,188,196
MM 76,188,200
MM 76,184,204
DM 217,12,84,1024
MM 108,184,208
MM 108,188,212
MM 140,188,252
MM 140,184,248
MM 172,184,244
DM 218,12,84,2048
MM 172,188,240
s_waitcnt lgkmcnt(0)
RT 184,205,16384
RT 186,205,17408
MM 176,176,240
s_bfe_u32 s83, s78, 0x40008
RT 188,205,24576
RT 190,205,25600
MM 176,180,244
s_mul_i32 s83, s83, 7
s_and_b32 s83, s83, 15
s_lshl_b32 s83, s83, 7
MM 144,180,248
MM 144,176,252
DM 219,12,84,3072
MM 112,176,212
s_add_u32 m0, s85, 24576
MM 112,180,208
MM 80,180,204
MM 80,176,200
MM 48,176,196
DM 212,8,83,0
MM 48,180,192
MM 16,180,236
RT 180,206,16384
RT 182,206,17408
MM 16,176,232
s_waitcnt lgkmcnt(2)
RT 176,206,24576
RT 178,206,25600
MM 20,184,232
MM 20,188,236
DM 213,8,83,1024
MM 52,188,192
MM 52,184,196
MM 84,184,200
MM 84,188,204
MM 116,188,208
DM 214,8,83,2048
MM 116,184,212
s_add_u32 m0, s85, 36864
s_bfe_u32 s83, s78, 0x40008
s_mul_i32 s83, s83, 7
MM 148,184,252
s_and_b32 s83, s83, 15
s_lshl_b32 s83, s83, 7
s_add_u32 s83, s83, 196608
MM 148,188,248
MM 180,188,244
RT 188,207,16384
RT 190,207,17408
MM 180,184,240
s_waitcnt lgkmcnt(2)
RT 184,207,24576
RT 186,207,25600
MM 184,180,240
DM 212,8,83,0
MM 184,176,244
MM 152,176,248
MM 152,180,252
MM 120,180,212
MM 120,176,208
DM 213,8,83,1024
MM 88,176,204
MM 88,180,200
s_bfe_u32 s84, s78, 0x40008
s_mul_i32 s84, s84, 7
s_and_b32 s84, s84, 15
MM 56,180,196
s_lshl_b32 s84, s84, 19
s_add_u32 s84, s84, s87
MM 56,176,192
MM 24,176,236
DM 214,8,83,2048
MM 24,180,232
s_waitcnt lgkmcnt(0)
s_add_u32 m0, s86, 0
MM 28,188,232
s_waitcnt vmcnt(6) lgkmcnt(0)
s_barrier
MM 28,184,236
RT 176,200,32768
RT 178,200,33792
MM 60,184,192
RT 180,200,40960
RT 182,200,41984
MM 60,188,196
RB 228,208,0
RB 224,209,0
MM 92,188,200
RB 220,209,2048
RB 216,208,2048
MM 92,184,204
DM 216,12,84,0
MM 124,184,208
RB 232,208,4096
RB 236,209,4096
MM 124,188,212
RB 192,209,6144
RB 196,208,6144
MM 156,188,252
RB 200,208,8192
RB 204,209,8192
MM 156,184,248
RB 208,209,10240
RB 212,208,10240
MM 188,184,244
DM 217,12,84,1024
MM 188,188,240
s_waitcnt lgkmcnt(8)
RT 184,201,32768
RT 186,201,33792
MM 0,176,228
RT 188,201,40960
RT 190,201,41984
MM 0,180,224
MM 32,180,220
MM 32,176,216
s_waitcnt lgkmcnt(8)
MM 64,176,232
DM 218,12,84,2048
MM 64,180,236
MM 96,180,192
MM 96,176,196
s_waitcnt lgkmcnt(4)
MM 128,176,200
DM 219,12,84,3072
MM 128,180,204
s_barrier
s_add_u32 m0, s86, 8192
s_bfe_u32 s84, s78, 0x40008
MM 160,180,208
s_mul_i32 s84, s84, 7
s_and_b32 s84, s84, 15
s_lshl_b32 s84, s84, 19
MM 160,176,212
s_waitcnt lgkmcnt(0)
s_add_u32 s84, s84, s88
RT 180,202,32768
MM 164,184,212
RT 182,202,33792
RT 176,202,40960
MM 164,188,208
DM 216,12,84,0
MM 132,188,204
RT 178,202,41984
MM 132,184,200
MM 100,184,196
MM 100,188,192
DM 217,12,84,1024
MM 68,188,236
MM 68,184,232
MM 36,184,216
s_bfe_u32 s83, s78, 0x4000c
s_mul_i32 s83, s83, 7
s_and_b32 s83, s83, 15
MM 36,188,220
DM 218,12,84,2048
MM 4,188,224
s_lshl_b32 s83, s83, 7
RT 188,203,32768
RT 190,203,33792
MM 4,184,228
s_waitcnt lgkmcnt(2)
RT 184,203,40960
RT 186,203,41984
MM 8,180,228
MM 8,176,224
DM 219,12,84,3072
MM 40,176,220
s_add_u32 m0, s85, 0
MM 40,180,216
MM 72,180,232
MM 72,176,236
MM 104,176,192
DM 212,8,83,0
MM 104,180,196
MM 136,180,200
MM 136,176,204
MM 168,176,208
RT 176,204,32768
RT 178,204,33792
MM 168,180,212
s_waitcnt lgkmcnt(2)
RT 180,204,40960
RT 182,204,41984
MM 172,188,212
DM 213,8,83,1024
MM 172,184,208
MM 140,184,204
MM 140,188,200
MM 108,188,196
DM 214,8,83,2048
MM 108,184,192
s_add_u32 m0, s85, 12288
s_bfe_u32 s83, s78, 0x4000c
s_mul_i32 s83, s83, 7
MM 76,184,236
s_and_b32 s83, s83, 15
s_lshl_b32 s83, s83, 7
s_add_u32 s83, s83, 196608
MM 76,188,232
s_add_u32 s90, s59, 0
s_and_b32 s90, s90, 39936
MM 44,188,216
DM 212,8,83,0
MM 44,184,220
MM 12,184,224
RT 184,205,32768
RT 186,205,33792
MM 12,188,228
s_waitcnt lgkmcnt(2)
RT 188,205,40960
RT 190,205,41984
MM 16,176,228
buffer_load_dwordx4 v[240:243], v232, s[16:19], s90 offen offset:0 nt
MM 16,180,224
MM 48,180,220
MM 48,176,216
buffer_load_dwordx4 v[244:247], v233, s[16:19], s90 offen offset:1024 nt
MM 80,176,232
MM 80,180,236
MM 112,180,192
buffer_load_dwordx4 v[248:251], v234, s[16:19], s90 offen offset:2048 nt
MM 112,176,196
MM 144,176,200
MM 144,180,204
buffer_load_dwordx4 v[252:255], v235, s[16:19], s90 offen offset:3072 nt
MM 176,180,208
s_add_u32 s90, s90, 65536
RT 180,206,32768
RT 182,206,33792
MM 176,176,212
s_waitcnt lgkmcnt(2)
RT 176,206,40960
RT 178,206,41984
MM 180,184,212
buffer_load_dwordx4 a[0:3], v232, s[16:19], s90 offen offset:0 nt
MM 180,188,208
MM 148,188,204
MM 148,184,200
buffer_load_dwordx4 a[4:7], v233, s[16:19], s90 offen offset:1024 nt
MM 116,184,196
MM 116,188,192
MM 84,188,236
buffer_load_dwordx4 a[8:11], v234, s[16:19], s90 offen offset:2048 nt
MM 84,184,232
MM 52,184,216
MM 52,188,220
buffer_load_dwordx4 a[12:15], v235, s[16:19], s90 offen offset:3072 nt
MM 20,188,224
RT 188,207,32768
RT 190,207,33792
MM 20,184,228
s_waitcnt lgkmcnt(2)
RT 184,207,40960
RT 186,207,41984
MM 24,180,228
MM 24,176,224
MM 56,176,220
MM 56,180,216
MM 88,180,232
)ASM"
R"ASM(MM 88,176,236
MM 120,176,192
MM 120,180,196
MM 152,180,200
MM 152,176,204
MM 184,176,208
MM 184,180,212
s_waitcnt lgkmcnt(0)
MM 188,188,212
s_waitcnt vmcnt(12) lgkmcnt(0)
s_barrier
MM 188,184,208
RT 176,200,0
RT 178,200,1024
MM 156,184,204
RT 180,200,8192
RT 182,200,9216
MM 156,188,200
RB 252,208,34816
RB 248,209,34816
MM 124,188,196
RB 244,209,32768
RB 240,208,32768
MM 124,184,192
RB 212,208,30720
RB 208,209,30720
MM 92,184,236
RB 204,209,28672
RB 200,208,28672
MM 92,188,232
RB 196,208,26624
RB 192,209,26624
MM 60,188,216
RB 236,209,24576
RB 232,208,24576
MM 60,184,220
MM 28,184,224
RT 184,201,0
RT 186,201,1024
MM 28,188,228
s_waitcnt lgkmcnt(10)
RT 188,201,8192
RT 190,201,9216
MM 160,176,252
MM 160,180,248
MM 128,180,244
s_bfe_u32 s84, s78, 0x4000c
s_mul_i32 s84, s84, 7
s_and_b32 s84, s84, 15
MM 128,176,240
s_waitcnt lgkmcnt(8)
s_lshl_b32 s84, s84, 19
s_add_u32 s84, s84, s87
MM 96,176,212
DM 213,8,83,1024
MM 96,180,208
MM 64,180,204
MM 64,176,200
s_waitcnt lgkmcnt(4)
MM 32,176,196
DM 214,8,83,2048
MM 32,180,192
s_barrier
s_add_u32 m0, s86, 16384
MM 0,180,236
RT 180,202,0
RT 182,202,1024
MM 0,176,232
s_waitcnt lgkmcnt(2)
RT 176,202,8192
RT 178,202,9216
MM 4,184,232
DM 216,12,84,0
MM 4,188,236
MM 36,188,192
MM 36,184,196
MM 68,184,200
DM 217,12,84,1024
MM 68,188,204
MM 100,188,208
MM 100,184,212
MM 132,184,240
DM 218,12,84,2048
MM 132,188,244
MM 164,188,248
RT 188,203,0
RT 190,203,1024
MM 164,184,252
s_waitcnt lgkmcnt(2)
RT 184,203,8192
RT 186,203,9216
MM 168,180,252
DM 219,12,84,3072
MM 168,176,248
s_add_u32 m0, s86, 24576
s_bfe_u32 s84, s78, 0x4000c
s_mul_i32 s84, s84, 7
MM 136,176,244
s_and_b32 s84, s84, 15
s_lshl_b32 s84, s84, 19
s_add_u32 s84, s84, s88
MM 136,180,240
MM 104,180,212
DM 216,12,84,0
MM 104,176,208
MM 72,176,204
MM 72,180,200
MM 40,180,196
DM 217,12,84,1024
MM 40,176,192
MM 8,176,236
s_bfe_u32 s83, s78, 0x40000
RT 176,204,0
RT 178,204,1024
MM 8,180,232
s_waitcnt lgkmcnt(2)
RT 180,204,8192
RT 182,204,9216
MM 12,188,232
DM 218,12,84,2048
MM 12,184,236
s_mul_i32 s83, s83, 7
s_add_u32 s83, s83, 28
s_and_b32 s83, s83, 15
MM 44,184,192
s_lshl_b32 s83, s83, 7
MM 44,188,196
MM 76,188,200
DM 219,12,84,3072
MM 76,184,204
s_add_u32 m0, s85, 24576
MM 108,184,208
MM 108,188,212
MM 140,188,240
DM 212,8,83,0
MM 140,184,244
MM 172,184,248
RT 184,205,0
RT 186,205,1024
MM 172,188,252
s_waitcnt lgkmcnt(2)
RT 188,205,8192
RT 190,205,9216
MM 176,176,252
DM 213,8,83,1024
MM 176,180,248
MM 144,180,244
MM 144,176,240
MM 112,176,212
DM 214,8,83,2048
MM 112,180,208
s_add_u32 m0, s85, 36864
s_bfe_u32 s83, s78, 0x40000
s_mul_i32 s83, s83, 7
MM 80,180,204
s_add_u32 s83, s83, 28
s_and_b32 s83, s83, 15
s_lshl_b32 s83, s83, 7
MM 80,176,200
s_add_u32 s83, s83, 196608
MM 48,176,196
DM 212,8,83,0
MM 48,180,192
MM 16,180,236
s_bfe_u32 s84, s78, 0x40000
RT 180,206,0
RT 182,206,1024
MM 16,176,232
s_waitcnt lgkmcnt(2)
RT 176,206,8192
RT 178,206,9216
MM 20,184,232
DM 213,8,83,1024
MM 20,188,236
s_mul_i32 s84, s84, 7
s_add_u32 s84, s84, 28
s_and_b32 s84, s84, 15
MM 52,188,192
s_lshl_b32 s84, s84, 19
s_add_u32 s84, s84, s87
MM 52,184,196
MM 84,184,200
DM 214,8,83,2048
MM 84,188,204
s_add_u32 m0, s86, 32768
MM 116,188,208
MM 116,184,212
MM 148,184,240
DM 216,12,84,0
MM 148,188,244
MM 180,188,248
RT 188,207,0
RT 190,207,1024
MM 180,184,252
s_waitcnt lgkmcnt(2)
RT 184,207,8192
RT 186,207,9216
MM 184,180,252
DM 217,12,84,1024
MM 184,176,248
MM 152,176,244
MM 152,180,240
MM 120,180,212
DM 218,12,84,2048
MM 120,176,208
MM 88,176,204
MM 88,180,200
MM 56,180,196
DM 219,12,84,3072
MM 56,176,192
s_bfe_u32 s84, s78, 0x40000
s_mul_i32 s84, s84, 7
s_add_u32 s84, s84, 28
MM 24,176,236
s_and_b32 s84, s84, 15
s_lshl_b32 s84, s84, 19
s_add_u32 s84, s84, s88
MM 24,180,232
s_waitcnt lgkmcnt(0)
s_add_u32 m0, s86, 40960
MM 28,188,232
s_waitcnt vmcnt(10) lgkmcnt(0)
s_barrier
MM 28,184,236
RT 176,200,16384
RT 178,200,17408
MM 60,184,192
RT 180,200,24576
RT 182,200,25600
MM 60,188,196
RB 216,208,0
RB 220,209,0
MM 92,188,200
RB 224,209,2048
RB 228,208,2048
MM 92,184,204
DM 216,12,84,0
MM 124,184,208
RB 232,208,4096
RB 236,209,4096
MM 124,188,212
RB 192,209,6144
RB 196,208,6144
MM 156,188,240
RB 200,208,8192
RB 204,209,8192
MM 156,184,244
DM 217,12,84,1024
MM 188,184,248
RB 208,209,10240
RB 212,208,10240
MM 188,188,252
s_waitcnt lgkmcnt(8)
RT 184,201,16384
RT 186,201,17408
MM 0,176,216
s_bfe_u32 s83, s78, 0x40004
RT 188,201,24576
RT 190,201,25600
MM 0,180,220
DM 218,12,84,2048
MM 32,180,224
s_mul_i32 s83, s83, 7
s_add_u32 s83, s83, 28
s_and_b32 s83, s83, 15
MM 32,176,228
s_waitcnt lgkmcnt(8)
s_lshl_b32 s83, s83, 7
MM 64,176,232
MM 64,180,236
DM 219,12,84,3072
MM 96,180,192
s_add_u32 m0, s85, 0
MM 96,176,196
s_waitcnt lgkmcnt(4)
MM 128,176,200
MM 128,180,204
s_barrier
MM 160,180,208
DM 212,8,83,0
MM 160,176,212
s_waitcnt lgkmcnt(0)
RT 180,202,16384
RT 182,202,17408
MM 164,184,212
s_add_u32 s90, s59, 0
RT 176,202,24576
RT 178,202,25600
MM 164,188,208
s_and_b32 s90, s90, 39936
MM 132,188,204
MM 132,184,200
DM 213,8,83,1024
MM 100,184,196
MM 100,188,192
MM 68,188,236
MM 68,184,232
buffer_load_dwordx4 a[16:19], v236, s[20:23], s90 offen offset:0 nt
MM 36,184,228
MM 36,188,224
MM 4,188,220
buffer_load_dwordx4 a[20:23], v199, s[20:23], s90 offen offset:1024 nt
MM 4,184,216
s_waitcnt lgkmcnt(0)
RT 188,203,16384
RT 190,203,17408
MM 8,180,216
RT 184,203,24576
RT 186,203,25600
MM 8,176,220
buffer_load_dwordx4 a[24:27], v238, s[20:23], s90 offen offset:2048 nt
MM 40,176,224
MM 40,180,228
MM 72,180,232
buffer_load_dwordx4 a[28:31], v239, s[20:23], s90 offen offset:3072 nt
MM 72,176,236
s_add_u32 s90, s90, 65536
MM 104,176,192
MM 104,180,196
buffer_load_dwordx4 a[32:35], v236, s[20:23], s90 offen offset:0 nt
MM 136,180,200
MM 136,176,204
MM 168,176,208
buffer_load_dwordx4 a[36:39], v199, s[20:23], s90 offen offset:1024 nt
MM 168,180,212
s_waitcnt lgkmcnt(0)
RT 176,204,16384
RT 178,204,17408
MM 172,188,212
RT 180,204,24576
RT 182,204,25600
MM 172,184,208
buffer_load_dwordx4 a[40:43], v238, s[20:23], s90 offen offset:2048 nt
MM 140,184,204
MM 140,188,200
MM 108,188,196
buffer_load_dwordx4 a[44:47], v239, s[20:23], s90 offen offset:3072 nt
MM 108,184,192
MM 76,184,236
MM 76,188,232
MM 44,188,228
MM 44,184,224
MM 12,184,220
RT 184,205,16384
RT 186,205,17408
MM 12,188,216
s_waitcnt lgkmcnt(2)
RT 188,205,24576
RT 190,205,25600
MM 16,176,216
MM 16,180,220
MM 48,180,224
MM 48,176,228
MM 80,176,232
MM 80,180,236
MM 112,180,192
MM 112,176,196
MM 144,176,200
MM 144,180,204
MM 176,180,208
RT 180,206,16384
RT 182,206,17408
MM 176,176,212
s_waitcnt lgkmcnt(2)
RT 176,206,24576
RT 178,206,25600
MM 180,184,212
MM 180,188,208
MM 148,188,204
MM 148,184,200
MM 116,184,196
MM 116,188,192
MM 84,188,236
MM 84,184,232
MM 52,184,228
MM 52,188,224
MM 20,188,220
RT 188,207,16384
RT 190,207,17408
MM 20,184,216
s_waitcnt lgkmcnt(2)
RT 184,207,24576
RT 186,207,25600
MM 24,180,216
DM 214,8,83,2048
MM 24,176,220
s_add_u32 m0, s85, 12288
s_bfe_u32 s83, s78, 0x40004
s_mul_i32 s83, s83, 7
MM 56,176,224
s_add_u32 s83, s83, 28
s_and_b32 s83, s83, 15
s_lshl_b32 s83, s83, 7
MM 56,180,228
s_add_u32 s83, s83, 196608
MM 88,180,232
DM 212,8,83,0
MM 88,176,236
MM 120,176,192
MM 120,180,196
MM 152,180,200
DM 213,8,83,1024
MM 152,176,204
MM 184,176,208
MM 184,180,212
s_waitcnt lgkmcnt(0)
s_bfe_u32 s84, s78, 0x40004
s_mul_i32 s84, s84, 7
MM 188,188,212
s_waitcnt vmcnt(13) lgkmcnt(0)
s_barrier
s_add_u32 s84, s84, 28
MM 188,184,208
s_and_b32 s84, s84, 15
RT 176,200,32768
RT 178,200,33792
MM 156,184,204
s_lshl_b32 s84, s84, 19
RT 180,200,40960
RT 182,200,41984
MM 156,188,200
s_add_u32 s84, s84, s87
RB 240,208,34816
RB 244,209,34816
MM 124,188,196
RB 248,209,32768
RB 252,208,32768
MM 124,184,192
DM 214,8,83,2048
MM 92,184,236
s_add_u32 m0, s86, 0
RB 212,208,30720
RB 208,209,30720
MM 92,188,232
RB 204,209,28672
RB 200,208,28672
MM 60,188,228
RB 196,208,26624
RB 192,209,26624
MM 60,184,224
DM 216,12,84,0
MM 28,184,220
RB 236,209,24576
RB 232,208,24576
MM 28,188,216
s_waitcnt lgkmcnt(8)
RT 184,201,32768
RT 186,201,33792
MM 160,176,240
RT 188,201,40960
RT 190,201,41984
MM 160,180,244
DM 217,12,84,1024
MM 128,180,248
MM 128,176,252
s_waitcnt lgkmcnt(8)
MM 96,176,212
MM 96,180,208
DM 218,12,84,2048
MM 64,180,204
MM 64,176,200
s_waitcnt lgkmcnt(4)
MM 32,176,196
MM 32,180,192
s_barrier
MM 0,180,236
DM 219,12,84,3072
MM 0,176,232
s_waitcnt lgkmcnt(0)
s_add_u32 m0, s86, 8192
s_bfe_u32 s84, s78, 0x40004
MM 4,184,232
s_mul_i32 s84, s84, 7
s_add_u32 s84, s84, 28
s_and_b32 s84, s84, 15
MM 4,188,236
s_lshl_b32 s84, s84, 19
s_add_u32 s84, s84, s88
RT 180,202,32768
MM 36,188,192
DM 216,12,84,0
MM 36,184,196
RT 182,202,33792
RT 176,202,40960
MM 68,184,200
RT 178,202,41984
MM 68,188,204
MM 100,188,208
DM 217,12,84,1024
MM 100,184,212
MM 132,184,252
MM 132,188,248
s_bfe_u32 s83, s78, 0x40008
s_mul_i32 s83, s83, 7
s_add_u32 s83, s83, 28
MM 164,188,244
DM 218,12,84,2048
MM 164,184,240
s_waitcnt lgkmcnt(0)
RT 188,203,32768
RT 190,203,33792
MM 168,180,240
s_and_b32 s83, s83, 15
RT 184,203,40960
RT 186,203,41984
MM 168,176,244
s_lshl_b32 s83, s83, 7
MM 136,176,248
DM 219,12,84,3072
MM 136,180,252
s_add_u32 m0, s85, 24576
MM 104,180,212
MM 104,176,208
MM 72,176,204
MM 72,180,200
DM 212,8,83,0
MM 40,180,196
MM 40,176,192
MM 8,176,236
RT 176,204,32768
RT 178,204,33792
MM 8,180,232
s_waitcnt lgkmcnt(2)
RT 180,204,40960
RT 182,204,41984
MM 12,188,232
DM 213,8,83,1024
MM 12,184,236
MM 44,184,192
MM 44,188,196
MM 76,188,200
DM 214,8,83,2048
MM 76,184,204
s_add_u32 m0, s85, 36864
s_bfe_u32 s83, s78, 0x40008
s_mul_i32 s83, s83, 7
MM 108,184,208
s_add_u32 s83, s83, 28
s_and_b32 s83, s83, 15
s_lshl_b32 s83, s83, 7
MM 108,188,212
s_add_u32 s83, s83, 196608
MM 140,188,252
DM 212,8,83,0
MM 140,184,248
MM 172,184,244
s_bfe_u32 s84, s78, 0x40008
RT 184,205,32768
RT 186,205,33792
MM 172,188,240
s_waitcnt lgkmcnt(2)
RT 188,205,40960
RT 190,205,41984
MM 176,176,240
DM 213,8,83,1024
MM 176,180,244
s_mul_i32 s84, s84, 7
s_add_u32 s84, s84, 28
s_and_b32 s84, s84, 15
MM 144,180,248
s_lshl_b32 s84, s84, 19
s_add_u32 s84, s84, s87
MM 144,176,252
MM 112,176,212
DM 214,8,83,2048
MM 112,180,208
s_add_u32 m0, s86, 16384
MM 80,180,204
MM 80,176,200
MM 48,176,196
DM 216,12,84,0
MM 48,180,192
MM 16,180,236
RT 180,206,32768
RT 182,206,33792
MM 16,176,232
s_waitcnt lgkmcnt(2)
RT 176,206,40960
RT 178,206,41984
MM 20,184,232
DM 217,12,84,1024
MM 20,188,236
MM 52,188,192
MM 52,184,196
MM 84,184,200
DM 218,12,84,2048
MM 84,188,204
MM 116,188,208
MM 116,184,212
MM 148,184,252
DM 219,12,84,3072
MM 148,188,248
s_add_u32 m0, s86, 24576
s_bfe_u32 s84, s78, 0x40008
s_mul_i32 s84, s84, 7
MM 180,188,244
s_add_u32 s84, s84, 28
s_and_b32 s84, s84, 15
s_lshl_b32 s84, s84, 19
MM 180,184,240
s_add_u32 s84, s84, s88
s_waitcnt lgkmcnt(0)
RT 188,207,32768
MM 184,180,240
DM 216,12,84,0
MM 184,176,244
RT 190,207,33792
RT 184,207,40960
MM 152,176,248
RT 186,207,41984
MM 152,180,252
MM 120,180,212
DM 217,12,84,1024
MM 120,176,208
MM 88,176,204
MM 88,180,200
MM 56,180,196
DM 218,12,84,2048
MM 56,176,192
s_add_u32 s90, s59, 1024
s_and_b32 s90, s90, 39936
MM 24,176,236
MM 24,180,232
s_waitcnt lgkmcnt(0)
MM 28,188,232
s_waitcnt vmcnt(13) lgkmcnt(0)
s_barrier
MM 28,184,236
RT 176,200,0
RT 178,200,1024
MM 60,184,192
RT 180,200,8192
RT 182,200,9216
MM 60,188,196
RB 228,208,0
RB 224,209,0
MM 92,188,200
RB 220,209,2048
RB 216,208,2048
MM 92,184,204
buffer_load_dwordx4 a[48:51], v232, s[16:19], s90 offen offset:0 nt
MM 124,184,208
buffer_load_dwordx4 a[52:55], v233, s[16:19], s90 offen offset:1024 nt
MM 124,188,212
DM 219,12,84,3072
MM 156,188,252
buffer_load_dwordx4 a[56:59], v234, s[16:19], s90 offen offset:2048 nt
MM 156,184,248
RB 232,208,4096
RB 236,209,4096
RB 192,209,6144
RB 196,208,6144
MM 188,184,244
RB 200,208,8192
RB 204,209,8192
MM 188,188,240
s_waitcnt lgkmcnt(6)
RB 208,209,10240
RB 212,208,10240
MM 0,176,228
buffer_load_dwordx4 a[60:63], v235, s[16:19], s90 offen offset:3072 nt
MM 0,180,224
s_add_u32 s90, s90, 65536
RT 184,201,0
RT 186,201,1024
MM 32,180,220
buffer_load_dwordx4 a[64:67], v232, s[16:19], s90 offen offset:0 nt
MM 32,176,216
s_waitcnt lgkmcnt(6)
RT 188,201,8192
RT 190,201,9216
MM 64,176,232
MM 64,180,236
buffer_load_dwordx4 a[68:71], v233, s[16:19], s90 offen offset:1024 nt
MM 96,180,192
MM 96,176,196
s_waitcnt lgkmcnt(4)
MM 128,176,200
buffer_load_dwordx4 a[72:75], v234, s[16:19], s90 offen offset:2048 nt
MM 128,180,204
s_barrier
MM 160,180,208
RT 180,202,0
RT 182,202,1024
MM 160,176,212
s_waitcnt lgkmcnt(2)
RT 176,202,8192
RT 178,202,9216
MM 164,184,212
buffer_load_dwordx4 a[76:79], v235, s[16:19], s90 offen offset:3072 nt
MM 164,188,208
MM 132,188,204
MM 132,184,200
MM 100,184,196
MM 100,188,192
MM 68,188,236
MM 68,184,232
MM 36,184,216
MM 36,188,220
MM 4,188,224
RT 188,203,0
RT 190,203,1024
MM 4,184,228
s_waitcnt lgkmcnt(2)
RT 184,203,8192
RT 186,203,9216
MM 8,180,228
MM 8,176,224
MM 40,176,220
MM 40,180,216
MM 72,180,232
MM 72,176,236
MM 104,176,192
MM 104,180,196
MM 136,180,200
s_bfe_u32 s83, s78, 0x4000c
s_mul_i32 s83, s83, 7
s_add_u32 s83, s83, 28
MM 136,176,204
s_and_b32 s83, s83, 15
s_lshl_b32 s83, s83, 7
MM 168,176,208
RT 176,204,0
RT 178,204,1024
MM 168,180,212
s_waitcnt lgkmcnt(2)
RT 180,204,8192
RT 182,204,9216
MM 172,188,212
s_add_u32 m0, s85, 0
MM 172,184,208
MM 140,184,204
MM 140,188,200
MM 108,188,196
MM 108,184,192
MM 76,184,236
DM 212,8,83,0
MM 76,188,232
MM 44,188,216
MM 44,184,220
MM 12,184,224
DM 213,8,83,1024
MM 12,188,228
s_waitcnt lgkmcnt(0)
RT 184,205,0
RT 186,205,1024
MM 16,176,228
RT 188,205,8192
RT 190,205,9216
MM 16,180,224
MM 48,180,220
DM 214,8,83,2048
MM 48,176,216
s_add_u32 m0, s85, 12288
s_bfe_u32 s83, s78, 0x4000c
s_mul_i32 s83, s83, 7
MM 80,176,232
s_add_u32 s83, s83, 28
s_and_b32 s83, s83, 15
s_lshl_b32 s83, s83, 7
MM 80,180,236
s_add_u32 s83, s83, 196608
MM 112,180,192
DM 212,8,83,0
MM 112,176,196
MM 144,176,200
s_bfe_u32 s84, s78, 0x4000c
s_mul_i32 s84, s84, 7
s_add_u32 s84, s84, 28
MM 144,180,204
s_and_b32 s84, s84, 15
s_lshl_b32 s84, s84, 19
s_add_u32 s84, s84, s87
MM 176,180,208
DM 213,8,83,1024
MM 176,176,212
s_waitcnt lgkmcnt(0)
RT 180,206,0
RT 182,206,1024
MM 180,184,212
RT 176,206,8192
RT 178,206,9216
MM 180,188,208
MM 148,188,204
DM 214,8,83,2048
MM 148,184,200
s_add_u32 m0, s86, 32768
MM 116,184,196
MM 116,188,192
MM 84,188,236
DM 216,12,84,0
MM 84,184,232
MM 52,184,216
MM 52,188,220
MM 20,188,224
DM 217,12,84,1024
MM 20,184,228
s_waitcnt lgkmcnt(0)
RT 188,207,0
RT 190,207,1024
MM 24,180,228
RT 184,207,8192
RT 186,207,9216
MM 24,176,224
MM 56,176,220
DM 218,12,84,2048
MM 56,180,216
MM 88,180,232
MM 88,176,236
MM 120,176,192
DM 219,12,84,3072
MM 120,180,196
s_add_u32 m0, s86, 40960
s_bfe_u32 s84, s78, 0x4000c
s_mul_i32 s84, s84, 7
MM 152,180,200
s_add_u32 s84, s84, 28
s_and_b32 s84, s84, 15
s_lshl_b32 s84, s84, 19
MM 152,176,204
s_add_u32 s84, s84, s88
MM 184,176,208
DM 216,12,84,0
MM 184,180,212
s_waitcnt lgkmcnt(0)
MM 188,188,212
s_waitcnt vmcnt(17) lgkmcnt(0)
s_barrier
MM 188,184,208
RT 176,200,16384
RT 178,200,17408
MM 156,184,204
RT 180,200,24576
RT 182,200,25600
MM 156,188,200
RB 252,208,34816
RB 248,209,34816
MM 124,188,196
RB 244,209,32768
RB 240,208,32768
MM 124,184,192
DM 217,12,84,1024
MM 92,184,236
RB 212,208,30720
RB 208,209,30720
MM 92,188,232
RB 204,209,28672
RB 200,208,28672
MM 60,188,216
RB 196,208,26624
RB 192,209,26624
MM 60,184,220
DM 218,12,84,2048
MM 28,184,224
RB 236,209,24576
RB 232,208,24576
MM 28,188,228
s_waitcnt lgkmcnt(8)
RT 184,201,16384
RT 186,201,17408
MM 160,176,252
s_bfe_u32 s83, s78, 0x40000
RT 188,201,24576
RT 190,201,25600
MM 160,180,248
DM 219,12,84,3072
MM 128,180,244
s_mul_i32 s83, s83, 7
s_add_u32 s83, s83, 56
s_and_b32 s83, s83, 15
MM 128,176,240
s_waitcnt lgkmcnt(8)
s_lshl_b32 s83, s83, 7
MM 96,176,212
s_add_u32 m0, s85, 24576
MM 96,180,208
MM 64,180,204
MM 64,176,200
s_waitcnt lgkmcnt(4)
MM 32,176,196
MM 32,180,192
s_barrier
MM 0,180,236
DM 212,8,83,0
MM 0,176,232
s_waitcnt lgkmcnt(0)
RT 180,202,16384
RT 182,202,17408
MM 4,184,232
RT 176,202,24576
RT 178,202,25600
MM 4,188,236
MM 36,188,192
MM 36,184,196
DM 213,8,83,1024
MM 68,184,200
MM 68,188,204
MM 100,188,208
MM 100,184,212
MM 132,184,240
DM 214,8,83,2048
MM 132,188,244
s_add_u32 m0, s85, 36864
s_bfe_u32 s83, s78, 0x40000
s_mul_i32 s83, s83, 7
MM 164,188,248
s_add_u32 s83, s83, 56
s_and_b32 s83, s83, 15
s_lshl_b32 s83, s83, 7
MM 164,184,252
s_waitcnt lgkmcnt(0)
s_add_u32 s83, s83, 196608
RT 188,203,16384
MM 168,180,252
RT 190,203,17408
RT 184,203,24576
MM 168,176,248
DM 212,8,83,0
MM 136,176,244
RT 186,203,25600
MM 136,180,240
MM 104,180,212
MM 104,176,208
MM 72,176,204
DM 213,8,83,1024
MM 72,180,200
s_bfe_u32 s84, s78, 0x40000
s_mul_i32 s84, s84, 7
s_add_u32 s84, s84, 56
MM 40,180,196
s_and_b32 s84, s84, 15
s_lshl_b32 s84, s84, 19
s_add_u32 s84, s84, s87
MM 40,176,192
MM 8,176,236
RT 176,204,16384
RT 178,204,17408
MM 8,180,232
s_waitcnt lgkmcnt(2)
RT 180,204,24576
RT 182,204,25600
MM 12,188,232
DM 214,8,83,2048
MM 12,184,236
s_add_u32 m0, s86, 0
MM 44,184,192
MM 44,188,196
MM 76,188,200
DM 216,12,84,0
MM 76,184,204
MM 108,184,208
MM 108,188,212
MM 140,188,240
DM 217,12,84,1024
MM 140,184,244
MM 172,184,248
RT 184,205,16384
RT 186,205,17408
MM 172,188,252
s_waitcnt lgkmcnt(2)
RT 188,205,24576
RT 190,205,25600
MM 176,176,252
DM 218,12,84,2048
MM 176,180,248
s_add_u32 s90, s59, 1024
s_and_b32 s90, s90, 39936
MM 144,180,244
MM 144,176,240
MM 112,176,212
DM 219,12,84,3072
MM 112,180,208
s_add_u32 m0, s86, 8192
s_bfe_u32 s84, s78, 0x40000
s_mul_i32 s84, s84, 7
MM 80,180,204
s_add_u32 s84, s84, 56
s_and_b32 s84, s84, 15
s_lshl_b32 s84, s84, 19
MM 80,176,200
s_add_u32 s84, s84, s88
MM 48,176,196
DM 216,12,84,0
MM 48,180,192
buffer_load_dwordx4 a[80:83], v236, s[20:23], s90 offen offset:0 nt
MM 16,180,236
RT 180,206,16384
RT 182,206,17408
MM 16,176,232
s_waitcnt lgkmcnt(2)
RT 176,206,24576
RT 178,206,25600
MM 20,184,232
buffer_load_dwordx4 a[84:87], v199, s[20:23], s90 offen offset:1024 nt
MM 20,188,236
MM 52,188,192
MM 52,184,196
buffer_load_dwordx4 a[88:91], v238, s[20:23], s90 offen offset:2048 nt
MM 84,184,200
MM 84,188,204
)ASM"
R"ASM(MM 116,188,208
buffer_load_dwordx4 a[92:95], v239, s[20:23], s90 offen offset:3072 nt
MM 116,184,212
s_add_u32 s90, s90, 65536
MM 148,184,240
MM 148,188,244
buffer_load_dwordx4 a[96:99], v236, s[20:23], s90 offen offset:0 nt
MM 180,188,248
RT 188,207,16384
RT 190,207,17408
MM 180,184,252
s_waitcnt lgkmcnt(2)
RT 184,207,24576
RT 186,207,25600
MM 184,180,252
buffer_load_dwordx4 a[100:103], v199, s[20:23], s90 offen offset:1024 nt
MM 184,176,248
MM 152,176,244
MM 152,180,240
buffer_load_dwordx4 a[104:107], v238, s[20:23], s90 offen offset:2048 nt
MM 120,180,212
MM 120,176,208
MM 88,176,204
buffer_load_dwordx4 a[108:111], v239, s[20:23], s90 offen offset:3072 nt
MM 88,180,200
MM 56,180,196
MM 56,176,192
MM 24,176,236
MM 24,180,232
s_waitcnt lgkmcnt(0)
MM 28,188,232
s_waitcnt vmcnt(19) lgkmcnt(0)
s_barrier
MM 28,184,236
RT 176,200,32768
RT 178,200,33792
MM 60,184,192
RT 180,200,40960
RT 182,200,41984
MM 60,188,196
RB 216,208,0
RB 220,209,0
MM 92,188,200
RB 224,209,2048
RB 228,208,2048
MM 92,184,204
RB 232,208,4096
RB 236,209,4096
MM 124,184,208
RB 192,209,6144
RB 196,208,6144
MM 124,188,212
RB 200,208,8192
RB 204,209,8192
MM 156,188,240
RB 208,209,10240
RB 212,208,10240
MM 156,184,244
MM 188,184,248
RT 184,201,32768
RT 186,201,33792
MM 188,188,252
s_waitcnt lgkmcnt(10)
RT 188,201,40960
RT 190,201,41984
MM 0,176,216
MM 0,180,220
MM 32,180,224
MM 32,176,228
s_waitcnt lgkmcnt(8)
MM 64,176,232
MM 64,180,236
MM 96,180,192
MM 96,176,196
s_waitcnt lgkmcnt(4)
MM 128,176,200
MM 128,180,204
s_barrier
MM 160,180,208
RT 180,202,32768
RT 182,202,33792
MM 160,176,212
s_waitcnt lgkmcnt(2)
RT 176,202,40960
RT 178,202,41984
MM 164,184,212
MM 164,188,208
DM 217,12,84,1024
MM 132,188,204
MM 132,184,200
MM 100,184,196
s_bfe_u32 s83, s78, 0x40004
s_mul_i32 s83, s83, 7
s_add_u32 s83, s83, 56
MM 100,188,192
DM 218,12,84,2048
MM 68,188,236
s_and_b32 s83, s83, 15
s_lshl_b32 s83, s83, 7
MM 68,184,232
MM 36,184,228
MM 36,188,224
DM 219,12,84,3072
MM 4,188,220
s_add_u32 m0, s85, 0
RT 188,203,32768
RT 190,203,33792
MM 4,184,216
s_waitcnt lgkmcnt(2)
RT 184,203,40960
RT 186,203,41984
MM 8,180,216
MM 8,176,220
MM 40,176,224
DM 212,8,83,0
MM 40,180,228
MM 72,180,232
MM 72,176,236
MM 104,176,192
MM 104,180,196
DM 213,8,83,1024
MM 136,180,200
MM 136,176,204
MM 168,176,208
RT 176,204,32768
RT 178,204,33792
MM 168,180,212
s_waitcnt lgkmcnt(2)
RT 180,204,40960
RT 182,204,41984
MM 172,188,212
DM 214,8,83,2048
MM 172,184,208
s_add_u32 m0, s85, 12288
s_bfe_u32 s83, s78, 0x40004
s_mul_i32 s83, s83, 7
MM 140,184,204
s_add_u32 s83, s83, 56
s_and_b32 s83, s83, 15
s_lshl_b32 s83, s83, 7
MM 140,188,200
s_add_u32 s83, s83, 196608
MM 108,188,196
DM 212,8,83,0
MM 108,184,192
MM 76,184,236
s_bfe_u32 s84, s78, 0x40004
s_mul_i32 s84, s84, 7
s_add_u32 s84, s84, 56
MM 76,188,232
s_and_b32 s84, s84, 15
s_lshl_b32 s84, s84, 19
s_add_u32 s84, s84, s87
MM 44,188,228
DM 213,8,83,1024
MM 44,184,224
MM 12,184,220
RT 184,205,32768
RT 186,205,33792
MM 12,188,216
s_waitcnt lgkmcnt(2)
RT 188,205,40960
RT 190,205,41984
MM 16,176,216
DM 214,8,83,2048
MM 16,180,220
s_add_u32 m0, s86, 16384
MM 48,180,224
MM 48,176,228
MM 80,176,232
DM 216,12,84,0
MM 80,180,236
MM 112,180,192
MM 112,176,196
MM 144,176,200
DM 217,12,84,1024
MM 144,180,204
MM 176,180,208
RT 180,206,32768
RT 182,206,33792
MM 176,176,212
s_waitcnt lgkmcnt(2)
RT 176,206,40960
RT 178,206,41984
MM 180,184,212
DM 218,12,84,2048
MM 180,188,208
MM 148,188,204
MM 148,184,200
MM 116,184,196
DM 219,12,84,3072
MM 116,188,192
s_add_u32 m0, s86, 24576
s_bfe_u32 s84, s78, 0x40004
s_mul_i32 s84, s84, 7
MM 84,188,236
s_add_u32 s84, s84, 56
s_and_b32 s84, s84, 15
s_lshl_b32 s84, s84, 19
MM 84,184,232
s_add_u32 s84, s84, s88
MM 52,184,228
DM 216,12,84,0
MM 52,188,224
MM 20,188,220
RT 188,207,32768
RT 190,207,33792
MM 20,184,216
s_waitcnt lgkmcnt(2)
RT 184,207,40960
RT 186,207,41984
MM 24,180,216
DM 217,12,84,1024
MM 24,176,220
MM 56,176,224
MM 56,180,228
MM 88,180,232
DM 218,12,84,2048
MM 88,176,236
MM 120,176,192
MM 120,180,196
MM 152,180,200
DM 219,12,84,3072
MM 152,176,204
MM 184,176,208
MM 184,180,212
s_waitcnt lgkmcnt(0)
MM 188,188,212
s_waitcnt vmcnt(14) lgkmcnt(0)
s_barrier
MM 188,184,208
RT 176,200,0
RT 178,200,1024
MM 156,184,204
RT 180,200,8192
RT 182,200,9216
MM 156,188,200
RB 240,208,34816
RB 244,209,34816
MM 124,188,196
RB 248,209,32768
RB 252,208,32768
MM 124,184,192
RB 212,208,30720
RB 208,209,30720
MM 92,184,236
RB 204,209,28672
RB 200,208,28672
MM 92,188,232
RB 196,208,26624
RB 192,209,26624
MM 60,188,228
RB 236,209,24576
RB 232,208,24576
MM 60,184,224
MM 28,184,220
RT 184,201,0
RT 186,201,1024
MM 28,188,216
s_waitcnt lgkmcnt(10)
RT 188,201,8192
RT 190,201,9216
MM 160,176,240
s_bfe_u32 s83, s78, 0x40008
s_mul_i32 s83, s83, 7
s_add_u32 s83, s83, 56
MM 160,180,244
s_and_b32 s83, s83, 15
s_lshl_b32 s83, s83, 7
MM 128,180,248
MM 128,176,252
s_waitcnt lgkmcnt(8)
MM 96,176,212
s_add_u32 m0, s85, 24576
MM 96,180,208
MM 64,180,204
MM 64,176,200
s_waitcnt lgkmcnt(4)
MM 32,176,196
MM 32,180,192
s_barrier
MM 0,180,236
DM 212,8,83,0
MM 0,176,232
s_waitcnt lgkmcnt(0)
RT 180,202,0
RT 182,202,1024
MM 4,184,232
RT 176,202,8192
RT 178,202,9216
MM 4,188,236
MM 36,188,192
MM 36,184,196
DM 213,8,83,1024
MM 68,184,200
MM 68,188,204
MM 100,188,208
MM 100,184,212
MM 132,184,252
DM 214,8,83,2048
MM 132,188,248
s_add_u32 m0, s85, 36864
s_bfe_u32 s83, s78, 0x40008
s_mul_i32 s83, s83, 7
MM 164,188,244
s_add_u32 s83, s83, 56
s_and_b32 s83, s83, 15
s_lshl_b32 s83, s83, 7
MM 164,184,240
s_waitcnt lgkmcnt(0)
s_add_u32 s83, s83, 196608
s_add_u32 s90, s59, 2048
MM 168,180,240
s_and_b32 s90, s90, 39936
RT 188,203,0
RT 190,203,1024
MM 168,176,244
DM 212,8,83,0
MM 136,176,248
RT 184,203,8192
RT 186,203,9216
MM 136,180,252
MM 104,180,212
buffer_load_dwordx4 a[112:115], v232, s[16:19], s90 offen offset:0 nt
MM 104,176,208
MM 72,176,204
MM 72,180,200
buffer_load_dwordx4 a[116:119], v233, s[16:19], s90 offen offset:1024 nt
MM 40,180,196
MM 40,176,192
MM 8,176,236
buffer_load_dwordx4 a[120:123], v234, s[16:19], s90 offen offset:2048 nt
MM 8,180,232
s_waitcnt lgkmcnt(0)
RT 176,204,0
RT 178,204,1024
MM 12,188,232
RT 180,204,8192
RT 182,204,9216
MM 12,184,236
buffer_load_dwordx4 a[124:127], v235, s[16:19], s90 offen offset:3072 nt
MM 44,184,192
s_add_u32 s90, s90, 65536
MM 44,188,196
MM 76,188,200
buffer_load_dwordx4 a[128:131], v232, s[16:19], s90 offen offset:0 nt
MM 76,184,204
MM 108,184,208
MM 108,188,212
buffer_load_dwordx4 a[132:135], v233, s[16:19], s90 offen offset:1024 nt
MM 140,188,252
MM 140,184,248
MM 172,184,244
buffer_load_dwordx4 a[136:139], v234, s[16:19], s90 offen offset:2048 nt
MM 172,188,240
s_waitcnt lgkmcnt(0)
RT 184,205,0
RT 186,205,1024
MM 176,176,240
RT 188,205,8192
RT 190,205,9216
MM 176,180,244
buffer_load_dwordx4 a[140:143], v235, s[16:19], s90 offen offset:3072 nt
MM 144,180,248
MM 144,176,252
MM 112,176,212
MM 112,180,208
MM 80,180,204
MM 80,176,200
MM 48,176,196
MM 48,180,192
MM 16,180,236
RT 180,206,0
RT 182,206,1024
MM 16,176,232
s_waitcnt lgkmcnt(2)
RT 176,206,8192
RT 178,206,9216
MM 20,184,232
MM 20,188,236
MM 52,188,192
MM 52,184,196
MM 84,184,200
MM 84,188,204
MM 116,188,208
MM 116,184,212
MM 148,184,252
MM 148,188,248
MM 180,188,244
RT 188,207,0
RT 190,207,1024
MM 180,184,240
s_waitcnt lgkmcnt(2)
RT 184,207,8192
RT 186,207,9216
MM 184,180,240
MM 184,176,244
MM 152,176,248
MM 152,180,252
MM 120,180,212
MM 120,176,208
MM 88,176,204
MM 88,180,200
MM 56,180,196
DM 213,8,83,1024
MM 56,176,192
MM 24,176,236
MM 24,180,232
s_waitcnt lgkmcnt(0)
s_bfe_u32 s84, s78, 0x40008
s_mul_i32 s84, s84, 7
MM 28,188,232
s_waitcnt vmcnt(13) lgkmcnt(0)
s_barrier
s_add_u32 s84, s84, 56
MM 28,184,236
s_and_b32 s84, s84, 15
RT 176,200,16384
RT 178,200,17408
MM 60,184,192
s_lshl_b32 s84, s84, 19
RT 180,200,24576
RT 182,200,25600
MM 60,188,196
s_add_u32 s84, s84, s87
RB 228,208,0
RB 224,209,0
MM 92,188,200
RB 220,209,2048
RB 216,208,2048
MM 92,184,204
DM 214,8,83,2048
MM 124,184,208
s_add_u32 m0, s86, 32768
RB 232,208,4096
RB 236,209,4096
MM 124,188,212
RB 192,209,6144
RB 196,208,6144
MM 156,188,252
RB 200,208,8192
RB 204,209,8192
MM 156,184,248
DM 216,12,84,0
MM 188,184,244
RB 208,209,10240
RB 212,208,10240
MM 188,188,240
s_waitcnt lgkmcnt(8)
RT 184,201,16384
RT 186,201,17408
MM 0,176,228
RT 188,201,24576
RT 190,201,25600
MM 0,180,224
DM 217,12,84,1024
MM 32,180,220
MM 32,176,216
s_waitcnt lgkmcnt(8)
MM 64,176,232
MM 64,180,236
DM 218,12,84,2048
MM 96,180,192
MM 96,176,196
s_waitcnt lgkmcnt(4)
MM 128,176,200
MM 128,180,204
s_barrier
MM 160,180,208
DM 219,12,84,3072
MM 160,176,212
s_waitcnt lgkmcnt(0)
s_add_u32 m0, s86, 40960
s_bfe_u32 s84, s78, 0x40008
MM 164,184,212
s_mul_i32 s84, s84, 7
s_add_u32 s84, s84, 56
s_and_b32 s84, s84, 15
MM 164,188,208
s_lshl_b32 s84, s84, 19
s_add_u32 s84, s84, s88
RT 180,202,16384
MM 132,188,204
DM 216,12,84,0
MM 132,184,200
RT 182,202,17408
RT 176,202,24576
MM 100,184,196
RT 178,202,25600
MM 100,188,192
MM 68,188,236
DM 217,12,84,1024
MM 68,184,232
MM 36,184,216
MM 36,188,220
s_bfe_u32 s83, s78, 0x4000c
s_mul_i32 s83, s83, 7
s_add_u32 s83, s83, 56
MM 4,188,224
DM 218,12,84,2048
MM 4,184,228
s_waitcnt lgkmcnt(0)
RT 188,203,16384
RT 190,203,17408
MM 8,180,228
s_and_b32 s83, s83, 15
RT 184,203,24576
RT 186,203,25600
MM 8,176,224
s_lshl_b32 s83, s83, 7
MM 40,176,220
DM 219,12,84,3072
MM 40,180,216
s_add_u32 m0, s85, 0
MM 72,180,232
MM 72,176,236
MM 104,176,192
MM 104,180,196
DM 212,8,83,0
MM 136,180,200
MM 136,176,204
MM 168,176,208
RT 176,204,16384
RT 178,204,17408
MM 168,180,212
s_waitcnt lgkmcnt(2)
RT 180,204,24576
RT 182,204,25600
MM 172,188,212
DM 213,8,83,1024
MM 172,184,208
MM 140,184,204
MM 140,188,200
MM 108,188,196
DM 214,8,83,2048
MM 108,184,192
s_add_u32 m0, s85, 12288
s_bfe_u32 s83, s78, 0x4000c
s_mul_i32 s83, s83, 7
MM 76,184,236
s_add_u32 s83, s83, 56
s_and_b32 s83, s83, 15
s_lshl_b32 s83, s83, 7
MM 76,188,232
s_add_u32 s83, s83, 196608
MM 44,188,216
DM 212,8,83,0
MM 44,184,220
MM 12,184,224
s_bfe_u32 s84, s78, 0x4000c
RT 184,205,16384
RT 186,205,17408
MM 12,188,228
s_waitcnt lgkmcnt(2)
RT 188,205,24576
RT 190,205,25600
MM 16,176,228
DM 213,8,83,1024
MM 16,180,224
s_mul_i32 s84, s84, 7
s_add_u32 s84, s84, 56
s_and_b32 s84, s84, 15
MM 48,180,220
s_lshl_b32 s84, s84, 19
s_add_u32 s84, s84, s87
MM 48,176,216
MM 80,176,232
DM 214,8,83,2048
MM 80,180,236
s_add_u32 m0, s86, 0
MM 112,180,192
MM 112,176,196
MM 144,176,200
DM 216,12,84,0
MM 144,180,204
MM 176,180,208
RT 180,206,16384
RT 182,206,17408
MM 176,176,212
s_waitcnt lgkmcnt(2)
RT 176,206,24576
RT 178,206,25600
MM 180,184,212
DM 217,12,84,1024
MM 180,188,208
MM 148,188,204
MM 148,184,200
MM 116,184,196
DM 218,12,84,2048
MM 116,188,192
MM 84,188,236
MM 84,184,232
MM 52,184,216
DM 219,12,84,3072
MM 52,188,220
s_add_u32 m0, s86, 8192
s_bfe_u32 s84, s78, 0x4000c
s_mul_i32 s84, s84, 7
MM 20,188,224
s_add_u32 s84, s84, 56
s_and_b32 s84, s84, 15
s_lshl_b32 s84, s84, 19
MM 20,184,228
s_add_u32 s84, s84, s88
s_waitcnt lgkmcnt(0)
RT 188,207,16384
MM 24,180,228
DM 216,12,84,0
MM 24,176,224
RT 190,207,17408
RT 184,207,24576
MM 56,176,220
RT 186,207,25600
MM 56,180,216
MM 88,180,232
DM 217,12,84,1024
MM 88,176,236
MM 120,176,192
MM 120,180,196
MM 152,180,200
DM 218,12,84,2048
MM 152,176,204
MM 184,176,208
MM 184,180,212
s_waitcnt lgkmcnt(0)
MM 188,188,212
s_waitcnt vmcnt(13) lgkmcnt(0)
s_barrier
MM 188,184,208
RT 176,200,32768
RT 178,200,33792
MM 156,184,204
RT 180,200,40960
RT 182,200,41984
MM 156,188,200
RB 252,208,34816
RB 248,209,34816
MM 124,188,196
s_add_u32 s90, s59, 2048
RB 244,209,32768
RB 240,208,32768
MM 124,184,192
DM 219,12,84,3072
MM 92,184,236
s_and_b32 s90, s90, 39936
RB 212,208,30720
RB 208,209,30720
MM 92,188,232
RB 204,209,28672
RB 200,208,28672
MM 60,188,216
RB 196,208,26624
RB 192,209,26624
MM 60,184,220
RB 236,209,24576
RB 232,208,24576
MM 28,184,224
RT 184,201,32768
RT 186,201,33792
MM 28,188,228
s_waitcnt lgkmcnt(10)
RT 188,201,40960
RT 190,201,41984
MM 160,176,252
buffer_load_dwordx4 a[144:147], v236, s[20:23], s90 offen offset:0 nt
MM 160,180,248
MM 128,180,244
MM 128,176,240
s_waitcnt lgkmcnt(8)
MM 96,176,212
buffer_load_dwordx4 a[148:151], v199, s[20:23], s90 offen offset:1024 nt
MM 96,180,208
MM 64,180,204
buffer_load_dwordx4 a[152:155], v238, s[20:23], s90 offen offset:2048 nt
MM 64,176,200
s_waitcnt lgkmcnt(4)
MM 32,176,196
MM 32,180,192
s_barrier
MM 0,180,236
buffer_load_dwordx4 a[156:159], v239, s[20:23], s90 offen offset:3072 nt
MM 0,176,232
s_add_u32 s90, s90, 65536
s_waitcnt lgkmcnt(0)
RT 180,202,32768
MM 4,184,232
buffer_load_dwordx4 a[160:163], v236, s[20:23], s90 offen offset:0 nt
MM 4,188,236
RT 182,202,33792
RT 176,202,40960
MM 36,188,192
RT 178,202,41984
MM 36,184,196
buffer_load_dwordx4 a[164:167], v199, s[20:23], s90 offen offset:1024 nt
MM 68,184,200
MM 68,188,204
MM 100,188,208
buffer_load_dwordx4 a[168:171], v238, s[20:23], s90 offen offset:2048 nt
MM 100,184,212
MM 132,184,240
MM 132,188,244
buffer_load_dwordx4 a[172:175], v239, s[20:23], s90 offen offset:3072 nt
MM 164,188,248
RT 188,203,32768
RT 190,203,33792
MM 164,184,252
s_waitcnt lgkmcnt(2)
RT 184,203,40960
RT 186,203,41984
MM 168,180,252
MM 168,176,248
MM 136,176,244
MM 136,180,240
MM 104,180,212
MM 104,176,208
MM 72,176,204
MM 72,180,200
MM 40,180,196
MM 40,176,192
MM 8,176,236
RT 176,204,32768
RT 178,204,33792
MM 8,180,232
s_waitcnt lgkmcnt(2)
RT 180,204,40960
RT 182,204,41984
MM 12,188,232
MM 12,184,236
MM 44,184,192
MM 44,188,196
MM 76,188,200
MM 76,184,204
MM 108,184,208
s_bfe_u32 s83, s78, 0x40010
s_mul_i32 s83, s83, 7
s_add_u32 s83, s83, 84
MM 108,188,212
s_and_b32 s83, s83, 15
s_lshl_b32 s83, s83, 7
MM 140,188,240
MM 140,184,244
MM 172,184,248
s_add_u32 m0, s85, 24576
RT 184,205,32768
RT 186,205,33792
MM 172,188,252
s_waitcnt lgkmcnt(2)
RT 188,205,40960
RT 190,205,41984
MM 176,176,252
MM 176,180,248
MM 144,180,244
MM 144,176,240
MM 112,176,212
DM 212,8,83,0
MM 112,180,208
MM 80,180,204
MM 80,176,200
MM 48,176,196
DM 213,8,83,1024
MM 48,180,192
MM 16,180,236
RT 180,206,32768
RT 182,206,33792
MM 16,176,232
s_waitcnt lgkmcnt(2)
RT 176,206,40960
RT 178,206,41984
MM 20,184,232
DM 214,8,83,2048
MM 20,188,236
s_add_u32 m0, s85, 36864
s_bfe_u32 s83, s78, 0x40010
s_mul_i32 s83, s83, 7
MM 52,188,192
s_add_u32 s83, s83, 84
s_and_b32 s83, s83, 15
s_lshl_b32 s83, s83, 7
MM 52,184,196
s_add_u32 s83, s83, 196608
MM 84,184,200
DM 212,8,83,0
MM 84,188,204
MM 116,188,208
s_bfe_u32 s84, s78, 0x40010
s_mul_i32 s84, s84, 7
s_add_u32 s84, s84, 84
MM 116,184,212
s_and_b32 s84, s84, 15
s_lshl_b32 s84, s84, 19
s_add_u32 s84, s84, s87
MM 148,184,240
DM 213,8,83,1024
MM 148,188,244
MM 180,188,248
RT 188,207,32768
RT 190,207,33792
MM 180,184,252
s_waitcnt lgkmcnt(2)
RT 184,207,40960
RT 186,207,41984
MM 184,180,252
DM 214,8,83,2048
MM 184,176,248
s_add_u32 m0, s86, 16384
MM 152,176,244
MM 152,180,240
MM 120,180,212
DM 216,12,84,0
MM 120,176,208
MM 88,176,204
MM 88,180,200
MM 56,180,196
DM 217,12,84,1024
MM 56,176,192
MM 24,176,236
MM 24,180,232
s_waitcnt lgkmcnt(0)
MM 28,188,232
s_waitcnt vmcnt(16) lgkmcnt(0)
s_barrier
MM 28,184,236
RT 176,200,0
RT 178,200,1024
MM 60,184,192
RT 180,200,8192
RT 182,200,9216
MM 60,188,196
RB 216,208,0
RB 220,209,0
MM 92,188,200
RB 224,209,2048
RB 228,208,2048
MM 92,184,204
DM 218,12,84,2048
MM 124,184,208
RB 232,208,4096
RB 236,209,4096
MM 124,188,212
RB 192,209,6144
RB 196,208,6144
MM 156,188,240
RB 200,208,8192
RB 204,209,8192
MM 156,184,244
DM 219,12,84,3072
MM 188,184,248
s_add_u32 m0, s86, 24576
s_bfe_u32 s84, s78, 0x40010
s_mul_i32 s84, s84, 7
MM 188,188,252
s_waitcnt lgkmcnt(6)
s_add_u32 s84, s84, 84
s_and_b32 s84, s84, 15
MM 0,176,216
s_lshl_b32 s84, s84, 19
s_add_u32 s84, s84, s88
RB 208,209,10240
MM 0,180,220
DM 216,12,84,0
RB 212,208,10240
MM 32,180,224
RT 184,201,0
RT 186,201,1024
MM 32,176,228
s_waitcnt lgkmcnt(6)
RT 188,201,8192
RT 190,201,9216
MM 64,176,232
MM 64,180,236
DM 217,12,84,1024
MM 96,180,192
MM 96,176,196
s_waitcnt lgkmcnt(4)
MM 128,176,200
MM 128,180,204
s_barrier
s_bfe_u32 s83, s78, 0x40014
s_mul_i32 s83, s83, 7
MM 160,180,208
DM 218,12,84,2048
MM 160,176,212
s_waitcnt lgkmcnt(0)
RT 180,202,0
RT 182,202,1024
MM 164,184,212
s_add_u32 s83, s83, 84
RT 176,202,8192
RT 178,202,9216
MM 164,188,208
s_and_b32 s83, s83, 15
s_lshl_b32 s83, s83, 7
MM 132,188,204
DM 219,12,84,3072
MM 132,184,200
s_add_u32 m0, s85, 0
MM 100,184,196
MM 100,188,192
v_lshrrev_b32 v194, 3, v192
v_add3_u32 v194, v194, s76, 8
MM 68,188,236
v_bfe_u32 v195, v194, 0, 1
v_lshlrev_b32 v193, 10, v195
MM 68,184,232
DM 212,8,83,0
MM 36,184,228
v_bfe_u32 v195, v194, 1, 1
v_lshl_add_u32 v193, v195, 11, v193
MM 36,188,224
v_bfe_u32 v195, v194, 2, 1
v_lshl_add_u32 v193, v195, 12, v193
MM 4,188,220
RT 188,203,0
RT 190,203,1024
MM 4,184,216
s_waitcnt lgkmcnt(2)
RT 184,203,8192
RT 186,203,9216
MM 8,180,216
DM 213,8,83,1024
MM 8,176,220
v_bfe_u32 v195, v194, 3, 1
v_lshl_add_u32 v193, v195, 15, v193
MM 40,176,224
v_bfe_u32 v195, v192, 0, 1
v_lshl_add_u32 v193, v195, 13, v193
MM 40,180,228
v_bfe_u32 v195, v192, 1, 1
v_lshl_add_u32 v193, v195, 14, v193
MM 72,180,232
v_bfe_u32 v195, v192, 2, 1
v_lshl_add_u32 v193, v195, 16, v193
MM 72,176,236
DM 214,8,83,2048
MM 104,176,192
s_add_u32 m0, s85, 12288
s_bfe_u32 s83, s78, 0x40014
s_mul_i32 s83, s83, 7
MM 104,180,196
s_add_u32 s83, s83, 84
s_and_b32 s83, s83, 15
s_lshl_b32 s83, s83, 7
MM 136,180,200
s_add_u32 s83, s83, 196608
v_add_u32 v194, v194, v192
v_and_b32 v195, 1, v194
MM 136,176,204
v_lshl_add_u32 v193, v195, 7, v193
v_lshrrev_b32 v194, 1, v194
MM 168,176,208
DM 212,8,83,0
MM 168,180,212
s_waitcnt lgkmcnt(0)
RT 176,204,0
RT 178,204,1024
MM 172,188,212
s_bfe_u32 s84, s78, 0x40014
RT 180,204,8192
RT 182,204,9216
MM 172,184,208
s_mul_i32 s84, s84, 7
s_add_u32 s84, s84, 84
s_and_b32 s84, s84, 15
MM 140,184,204
DM 213,8,83,1024
MM 140,188,200
s_lshl_b32 s84, s84, 19
s_add_u32 s84, s84, s87
v_bfe_u32 v195, v194, 0, 1
MM 108,188,196
v_lshl_add_u32 v193, v195, 8, v193
v_bfe_u32 v195, v194, 1, 1
MM 108,184,192
v_lshl_add_u32 v193, v195, 9, v193
MM 76,184,236
DM 214,8,83,2048
MM 76,188,232
s_add_u32 m0, s86, 32768
MM 44,188,228
MM 44,184,224
MM 12,184,220
DM 216,12,84,0
MM 12,188,216
s_waitcnt lgkmcnt(0)
RT 184,205,0
RT 186,205,1024
MM 16,176,216
RT 188,205,8192
RT 190,205,9216
MM 16,180,220
MM 48,180,224
DM 217,12,84,1024
MM 48,176,228
MM 80,176,232
MM 80,180,236
MM 112,180,192
DM 218,12,84,2048
MM 112,176,196
MM 144,176,200
MM 144,180,204
)ASM"
R"ASM(buffer_load_dword v215, v193, s[16:19], 0 offen offset:0
MM 176,180,208
RT 180,206,0
RT 182,206,1024
MM 176,176,212
s_waitcnt lgkmcnt(2)
RT 176,206,8192
RT 178,206,9216
MM 180,184,212
MM 180,188,208
MM 148,188,204
MM 148,184,200
MM 116,184,196
MM 116,188,192
MM 84,188,236
MM 84,184,232
MM 52,184,228
MM 52,188,224
MM 20,188,220
RT 188,207,0
RT 190,207,1024
MM 20,184,216
s_waitcnt lgkmcnt(2)
RT 184,207,8192
RT 186,207,9216
MM 24,180,216
MM 24,176,220
MM 56,176,224
MM 56,180,228
MM 88,180,232
MM 88,176,236
MM 120,176,192
MM 120,180,196
MM 152,180,200
MM 152,176,204
MM 184,176,208
MM 184,180,212
s_waitcnt lgkmcnt(0)
MM 188,188,212
s_waitcnt vmcnt(10) lgkmcnt(0)
s_barrier
MM 188,184,208
RT 176,200,16384
RT 178,200,17408
MM 156,184,204
RT 180,200,24576
RT 182,200,25600
MM 156,188,200
RB 240,208,34816
RB 244,209,34816
MM 124,188,196
RB 248,209,32768
RB 252,208,32768
MM 124,184,192
RB 212,208,30720
RB 208,209,30720
MM 92,184,236
RB 204,209,28672
RB 200,208,28672
MM 92,188,232
RB 196,208,26624
RB 192,209,26624
MM 60,188,228
RB 236,209,24576
RB 232,208,24576
MM 60,184,224
MM 28,184,220
RT 184,201,16384
RT 186,201,17408
MM 28,188,216
s_waitcnt lgkmcnt(10)
RT 188,201,24576
RT 190,201,25600
MM 160,176,240
MM 160,180,244
MM 128,180,248
MM 128,176,252
s_waitcnt lgkmcnt(8)
MM 96,176,212
MM 96,180,208
MM 64,180,204
MM 64,176,200
s_waitcnt lgkmcnt(4)
MM 32,176,196
MM 32,180,192
s_barrier
MM 0,180,236
RT 180,202,16384
RT 182,202,17408
MM 0,176,232
s_waitcnt lgkmcnt(2)
RT 176,202,24576
RT 178,202,25600
MM 4,184,232
MM 4,188,236
DM 219,12,84,3072
MM 36,188,192
s_add_u32 m0, s86, 40960
s_bfe_u32 s84, s78, 0x40014
s_mul_i32 s84, s84, 7
MM 36,184,196
s_add_u32 s84, s84, 84
s_and_b32 s84, s84, 15
s_lshl_b32 s84, s84, 19
MM 68,184,200
s_add_u32 s84, s84, s88
MM 68,188,204
DM 216,12,84,0
MM 100,188,208
MM 100,184,212
MM 132,184,252
MM 132,188,248
DM 217,12,84,1024
MM 164,188,244
RT 188,203,16384
RT 190,203,17408
MM 164,184,240
s_waitcnt lgkmcnt(2)
RT 184,203,24576
RT 186,203,25600
MM 168,180,240
s_bfe_u32 s83, s78, 0x40018
s_mul_i32 s83, s83, 7
s_add_u32 s83, s83, 84
MM 168,176,244
DM 218,12,84,2048
MM 136,176,248
s_and_b32 s83, s83, 15
s_lshl_b32 s83, s83, 7
MM 136,180,252
MM 104,180,212
MM 104,176,208
DM 219,12,84,3072
MM 72,176,204
s_add_u32 m0, s85, 24576
MM 72,180,200
MM 40,180,196
MM 40,176,192
MM 8,176,236
DM 212,8,83,0
MM 8,180,232
s_waitcnt lgkmcnt(0)
RT 176,204,16384
RT 178,204,17408
MM 12,188,232
RT 180,204,24576
RT 182,204,25600
MM 12,184,236
MM 44,184,192
DM 213,8,83,1024
MM 44,188,196
MM 76,188,200
MM 76,184,204
MM 108,184,208
DM 214,8,83,2048
MM 108,188,212
s_add_u32 m0, s85, 36864
s_bfe_u32 s83, s78, 0x40018
s_mul_i32 s83, s83, 7
MM 140,188,252
s_add_u32 s83, s83, 84
s_and_b32 s83, s83, 15
s_lshl_b32 s83, s83, 7
MM 140,184,248
s_add_u32 s83, s83, 196608
MM 172,184,244
DM 212,8,83,0
MM 172,188,240
s_waitcnt lgkmcnt(0)
RT 184,205,16384
RT 186,205,17408
MM 176,176,240
s_bfe_u32 s84, s78, 0x40018
RT 188,205,24576
RT 190,205,25600
MM 176,180,244
s_mul_i32 s84, s84, 7
s_add_u32 s84, s84, 84
s_and_b32 s84, s84, 15
MM 144,180,248
DM 213,8,83,1024
MM 144,176,252
s_lshl_b32 s84, s84, 19
s_add_u32 s84, s84, s87
MM 112,176,212
MM 112,180,208
MM 80,180,204
DM 214,8,83,2048
MM 80,176,200
s_add_u32 m0, s86, 0
MM 48,176,196
MM 48,180,192
MM 16,180,236
DM 216,12,84,0
MM 16,176,232
s_waitcnt lgkmcnt(0)
RT 180,206,16384
RT 182,206,17408
MM 20,184,232
RT 176,206,24576
RT 178,206,25600
MM 20,188,236
MM 52,188,192
DM 217,12,84,1024
MM 52,184,196
MM 84,184,200
MM 84,188,204
MM 116,188,208
DM 218,12,84,2048
MM 116,184,212
MM 148,184,252
MM 148,188,248
MM 180,188,244
DM 219,12,84,3072
MM 180,184,240
s_waitcnt lgkmcnt(0)
s_add_u32 m0, s86, 8192
s_bfe_u32 s84, s78, 0x40018
MM 184,180,240
s_mul_i32 s84, s84, 7
s_add_u32 s84, s84, 84
s_and_b32 s84, s84, 15
MM 184,176,244
s_lshl_b32 s84, s84, 19
s_add_u32 s84, s84, s88
RT 188,207,16384
MM 152,176,248
DM 216,12,84,0
MM 152,180,252
RT 190,207,17408
RT 184,207,24576
MM 120,180,212
RT 186,207,25600
MM 120,176,208
MM 88,176,204
DM 217,12,84,1024
MM 88,180,200
MM 56,180,196
MM 56,176,192
MM 24,176,236
DM 218,12,84,2048
MM 24,180,232
s_waitcnt lgkmcnt(0)
s_getpc_b64 s[62:63]
L_w3:
MM 28,188,232
s_waitcnt vmcnt(13) lgkmcnt(0)
s_barrier
MM 28,184,236
s_add_u32 s62, s62, L_c1-L_w3
s_addc_u32 s63, s63, 0
RT 176,200,32768
MM 60,184,192
RT 178,200,33792
RT 180,200,40960
MM 60,188,196
RT 182,200,41984
RB 228,208,0
MM 92,188,200
RB 224,209,0
RB 220,209,2048
MM 92,184,204
DM 219,12,84,3072
RB 216,208,2048
MM 124,184,208
v_min_u32 v196, L_c2-L_c1, v211
RB 232,208,4096
MM 124,188,212
buffer_load_dword v215, v193, s[20:23], 0 offen offset:0
MM 156,188,252
s_mov_b64 exec, 3
global_load_dword v215, v196, s[62:63]
s_mov_b64 exec, -1
MM 156,184,248
RB 236,209,4096
RB 192,209,6144
RB 196,208,6144
MM 188,184,244
RB 200,208,8192
RB 204,209,8192
MM 188,188,240
s_waitcnt lgkmcnt(6)
RB 208,209,10240
RB 212,208,10240
MM 0,176,228
RT 184,201,32768
RT 186,201,33792
MM 0,180,224
RT 188,201,40960
RT 190,201,41984
MM 32,180,220
MM 32,176,216
s_waitcnt lgkmcnt(8)
MM 64,176,232
MM 64,180,236
MM 96,180,192
MM 96,176,196
s_waitcnt lgkmcnt(4)
MM 128,176,200
MM 128,180,204
MM 160,180,208
RT 180,202,32768
RT 182,202,33792
MM 160,176,212
s_waitcnt lgkmcnt(2)
RT 176,202,40960
RT 178,202,41984
MM 164,184,212
MM 164,188,208
MM 132,188,204
MM 132,184,200
MM 100,184,196
MM 100,188,192
MM 68,188,236
MM 68,184,232
MM 36,184,216
MM 36,188,220
MM 4,188,224
RT 188,203,32768
RT 190,203,33792
MM 4,184,228
s_waitcnt lgkmcnt(2)
RT 184,203,40960
RT 186,203,41984
MM 8,180,228
MM 8,176,224
MM 40,176,220
MM 40,180,216
MM 72,180,232
MM 72,176,236
MM 104,176,192
MM 104,180,196
MM 136,180,200
MM 136,176,204
MM 168,176,208
RT 176,204,32768
RT 178,204,33792
MM 168,180,212
s_waitcnt lgkmcnt(2)
RT 180,204,40960
RT 182,204,41984
MM 172,188,212
MM 172,184,208
MM 140,184,204
MM 140,188,200
MM 108,188,196
MM 108,184,192
MM 76,184,236
MM 76,188,232
MM 44,188,216
MM 44,184,220
MM 12,184,224
RT 184,205,32768
RT 186,205,33792
MM 12,188,228
s_waitcnt lgkmcnt(2)
RT 188,205,40960
RT 190,205,41984
MM 16,176,228
MM 16,180,224
v_xor_b32 v193, 512, v193
MM 48,180,220
MM 48,176,216
MM 80,176,232
MM 80,180,236
MM 112,180,192
MM 112,176,196
MM 144,176,200
MM 144,180,204
MM 176,180,208
RT 180,206,32768
RT 182,206,33792
MM 176,176,212
s_waitcnt lgkmcnt(2)
RT 176,206,40960
RT 178,206,41984
MM 180,184,212
MM 180,188,208
MM 148,188,204
MM 148,184,200
MM 116,184,196
MM 116,188,192
MM 84,188,236
MM 84,184,232
MM 52,184,216
MM 52,188,220
MM 20,188,224
RT 188,207,32768
RT 190,207,33792
MM 20,184,228
s_waitcnt lgkmcnt(2)
RT 184,207,40960
RT 186,207,41984
MM 24,180,228
MM 24,176,224
MM 56,176,220
MM 56,180,216
MM 88,180,232
MM 88,176,236
MM 120,176,192
MM 120,180,196
MM 152,180,200
MM 152,176,204
MM 184,176,208
MM 184,180,212
s_waitcnt lgkmcnt(0)
MM 188,188,212
s_waitcnt vmcnt(2) lgkmcnt(0)
s_barrier
MM 188,184,208
RT 176,200,0
RT 178,200,1024
MM 156,184,204
RT 180,200,8192
RT 182,200,9216
MM 156,188,200
RB 252,208,34816
RB 248,209,34816
MM 124,188,196
RB 244,209,32768
RB 240,208,32768
MM 124,184,192
buffer_load_dword v215, v193, s[16:19], 0 offen offset:0
MM 92,184,236
RB 212,208,30720
RB 208,209,30720
MM 92,188,232
RB 204,209,28672
RB 200,208,28672
MM 60,188,216
RB 196,208,26624
RB 192,209,26624
MM 60,184,220
RB 236,209,24576
RB 232,208,24576
MM 28,184,224
RT 184,201,0
RT 186,201,1024
MM 28,188,228
s_waitcnt lgkmcnt(10)
RT 188,201,8192
RT 190,201,9216
MM 160,176,252
RT 216,202,0
RT 218,202,1024
MM 160,180,248
RT 220,202,8192
RT 222,202,9216
MM 128,180,244
RT 224,203,0
RT 226,203,1024
MM 128,176,240
s_waitcnt lgkmcnt(14)
RT 228,203,8192
RT 230,203,9216
MM 96,176,212
MM 96,180,208
MM 64,180,204
MM 64,176,200
s_waitcnt lgkmcnt(12)
MM 32,176,196
MM 32,180,192
MM 0,180,236
RT 180,204,0
RT 182,204,1024
MM 0,176,232
s_waitcnt lgkmcnt(10)
RT 176,204,8192
RT 178,204,9216
MM 4,184,232
MM 4,188,236
MM 36,188,192
MM 36,184,196
MM 68,184,200
MM 68,188,204
MM 100,188,208
MM 100,184,212
MM 132,184,240
MM 132,188,244
MM 164,188,248
RT 188,205,0
RT 190,205,1024
MM 164,184,252
s_waitcnt lgkmcnt(10)
RT 184,205,8192
RT 186,205,9216
MM 168,216,252
MM 168,220,248
MM 136,220,244
MM 136,216,240
MM 104,216,212
MM 104,220,208
MM 72,220,204
MM 72,216,200
MM 40,216,196
MM 40,220,192
MM 8,220,236
RT 220,206,0
RT 222,206,1024
MM 8,216,232
s_waitcnt lgkmcnt(10)
RT 216,206,8192
RT 218,206,9216
MM 12,224,232
MM 12,228,236
MM 44,228,192
MM 44,224,196
MM 76,224,200
MM 76,228,204
MM 108,228,208
MM 108,224,212
MM 140,224,240
MM 140,228,244
MM 172,228,248
RT 228,207,0
RT 230,207,1024
MM 172,224,252
s_waitcnt lgkmcnt(10)
RT 224,207,8192
RT 226,207,9216
MM 176,180,252
MM 176,176,248
MM 144,176,244
MM 144,180,240
MM 112,180,212
MM 112,176,208
MM 80,176,204
MM 80,180,200
MM 48,180,196
MM 48,176,192
MM 16,176,236
MM 16,180,232
s_waitcnt lgkmcnt(8)
MM 20,188,232
MM 20,184,236
MM 52,184,192
s_waitcnt lgkmcnt(0)
s_barrier
MM 52,188,196
s_waitcnt vmcnt(63)
MM 84,188,200
ds_write_b128 v198, v[240:243] offset:0
MM 84,184,204
MM 116,184,208
MM 116,188,212
ds_write_b128 v198, v[244:247] offset:1024
MM 148,188,240
MM 148,184,244
MM 180,184,248
ds_write_b128 v198, v[248:251] offset:2048
MM 180,188,252
MM 184,220,252
MM 184,216,248
ds_write_b128 v198, v[252:255] offset:3072
MM 152,216,244
MM 152,220,240
MM 120,220,212
ds_write_b128 v198, a[0:3] offset:4096
MM 120,216,208
MM 88,216,204
MM 88,220,200
ds_write_b128 v198, a[4:7] offset:5120
MM 56,220,196
MM 56,216,192
MM 24,216,236
ds_write_b128 v198, a[8:11] offset:6144
MM 24,220,232
MM 28,228,232
MM 28,224,236
ds_write_b128 v198, a[12:15] offset:7168
MM 60,224,192
MM 60,228,196
MM 92,228,200
ds_write_b128 v198, a[16:19] offset:16384
MM 92,224,204
MM 124,224,208
MM 124,228,212
ds_write_b128 v198, a[20:23] offset:17408
MM 156,228,240
MM 156,224,244
MM 188,224,248
MM 188,228,252
s_branch L_q3
L_q2: ; ---- Q projection, schedule variant 1
v_mfma_f32_16x16x32_bf16 v[0:3], a[176:179], a[192:195], 0
s_add_u32 m0, s85, 24576
s_bfe_u32 s83, s78, 0x40000
s_mul_i32 s83, s83, 7
v_mfma_f32_16x16x32_bf16 v[32:35], a[176:179], a[196:199], 0
s_and_b32 s83, s83, 15
s_lshl_b32 s83, s83, 7
RB 216,209,0
v_mfma_f32_16x16x32_bf16 v[64:67], a[176:179], a[200:203], 0
RB 220,209,2048
RB 224,209,4096
v_mfma_f32_16x16x32_bf16 v[96:99], a[176:179], a[204:207], 0
RB 228,209,6144
RB 232,209,8192
v_mfma_f32_16x16x32_bf16 v[128:131], a[176:179], a[208:211], 0
DM 212,8,83,0
v_mfma_f32_16x16x32_bf16 v[160:163], a[176:179], a[212:215], 0
s_waitcnt lgkmcnt(9)
RT 176,204,0
RT 178,204,1024
v_mfma_f32_16x16x32_bf16 v[164:167], a[180:183], a[212:215], 0
RB 236,209,10240
v_mfma_f32_16x16x32_bf16 v[132:135], a[180:183], a[208:211], 0
v_mfma_f32_16x16x32_bf16 v[100:103], a[180:183], a[204:207], 0
v_mfma_f32_16x16x32_bf16 v[68:71], a[180:183], a[200:203], 0
DM 213,8,83,1024
v_mfma_f32_16x16x32_bf16 v[36:39], a[180:183], a[196:199], 0
v_mfma_f32_16x16x32_bf16 v[4:7], a[180:183], a[192:195], 0
s_waitcnt lgkmcnt(10)
RT 180,205,0
RT 182,205,1024
v_mfma_f32_16x16x32_bf16 v[8:11], a[184:187], a[192:195], 0
v_mfma_f32_16x16x32_bf16 v[40:43], a[184:187], a[196:199], 0
v_mfma_f32_16x16x32_bf16 v[72:75], a[184:187], a[200:203], 0
DM 214,8,83,2048
v_mfma_f32_16x16x32_bf16 v[104:107], a[184:187], a[204:207], 0
s_add_u32 m0, s85, 36864
s_bfe_u32 s83, s78, 0x40000
s_mul_i32 s83, s83, 7
v_mfma_f32_16x16x32_bf16 v[136:139], a[184:187], a[208:211], 0
s_and_b32 s83, s83, 15
s_lshl_b32 s83, s83, 7
s_add_u32 s83, s83, 196608
v_mfma_f32_16x16x32_bf16 v[168:171], a[184:187], a[212:215], 0
s_waitcnt lgkmcnt(10)
RT 184,206,0
RT 186,206,1024
v_mfma_f32_16x16x32_bf16 v[172:175], a[188:191], a[212:215], 0
v_mfma_f32_16x16x32_bf16 v[140:143], a[188:191], a[208:211], 0
DM 212,8,83,0
v_mfma_f32_16x16x32_bf16 v[108:111], a[188:191], a[204:207], 0
s_waitcnt vmcnt(7)
s_barrier
v_mfma_f32_16x16x32_bf16 v[76:79], a[188:191], a[200:203], 0
v_mfma_f32_16x16x32_bf16 v[44:47], a[188:191], a[196:199], 0
v_mfma_f32_16x16x32_bf16 v[12:15], a[188:191], a[192:195], 0
s_waitcnt lgkmcnt(5)
RT 188,207,0
RT 190,207,1024
v_mfma_f32_16x16x32_bf16 v[16:19], a[176:179], a[192:195], 0
DM 213,8,83,1024
v_mfma_f32_16x16x32_bf16 v[48:51], a[176:179], a[196:199], 0
s_bfe_u32 s84, s78, 0x40000
s_mul_i32 s84, s84, 7
s_and_b32 s84, s84, 15
v_mfma_f32_16x16x32_bf16 v[80:83], a[176:179], a[200:203], 0
s_lshl_b32 s84, s84, 19
s_add_u32 s84, s84, s87
v_mfma_f32_16x16x32_bf16 v[112:115], a[176:179], a[204:207], 0
v_mfma_f32_16x16x32_bf16 v[144:147], a[176:179], a[208:211], 0
v_mfma_f32_16x16x32_bf16 v[176:179], a[176:179], a[212:215], 0
DM 214,8,83,2048
s_waitcnt lgkmcnt(4)
v_mfma_f32_16x16x32_bf16 v[180:183], a[180:183], a[212:215], 0
s_add_u32 m0, s86, 16384
RT 176,200,8192
RT 178,200,9216
v_mfma_f32_16x16x32_bf16 v[148:151], a[180:183], a[208:211], 0
v_mfma_f32_16x16x32_bf16 v[116:119], a[180:183], a[204:207], 0
v_mfma_f32_16x16x32_bf16 v[84:87], a[180:183], a[200:203], 0
v_mfma_f32_16x16x32_bf16 v[52:55], a[180:183], a[196:199], 0
DM 216,12,84,0
v_mfma_f32_16x16x32_bf16 v[20:23], a[180:183], a[192:195], 0
s_waitcnt lgkmcnt(4)
RT 180,201,8192
RT 182,201,9216
v_mfma_f32_16x16x32_bf16 v[24:27], a[184:187], a[192:195], 0
v_mfma_f32_16x16x32_bf16 v[56:59], a[184:187], a[196:199], 0
v_mfma_f32_16x16x32_bf16 v[88:91], a[184:187], a[200:203], 0
v_mfma_f32_16x16x32_bf16 v[120:123], a[184:187], a[204:207], 0
DM 217,12,84,1024
v_mfma_f32_16x16x32_bf16 v[152:155], a[184:187], a[208:211], 0
v_mfma_f32_16x16x32_bf16 v[184:187], a[184:187], a[212:215], 0
s_waitcnt lgkmcnt(4)
RT 184,202,8192
RT 186,202,9216
v_mfma_f32_16x16x32_bf16 v[188:191], a[188:191], a[212:215], 0
v_mfma_f32_16x16x32_bf16 v[156:159], a[188:191], a[208:211], 0
v_mfma_f32_16x16x32_bf16 v[124:127], a[188:191], a[204:207], 0
DM 218,12,84,2048
v_mfma_f32_16x16x32_bf16 v[92:95], a[188:191], a[200:203], 0
v_mfma_f32_16x16x32_bf16 v[60:63], a[188:191], a[196:199], 0
v_mfma_f32_16x16x32_bf16 v[28:31], a[188:191], a[192:195], 0
s_waitcnt lgkmcnt(4)
RT 188,203,8192
RT 190,203,9216
MM 0,176,216
MM 32,176,220
s_barrier
MM 64,176,224
DM 219,12,84,3072
MM 96,176,228
s_add_u32 m0, s86, 24576
s_bfe_u32 s84, s78, 0x40000
s_mul_i32 s84, s84, 7
MM 128,176,232
s_and_b32 s84, s84, 15
s_lshl_b32 s84, s84, 19
s_add_u32 s84, s84, s88
MM 160,176,236
s_waitcnt lgkmcnt(4)
RT 176,204,8192
RT 178,204,9216
MM 164,180,236
MM 132,180,232
DM 216,12,84,0
MM 100,180,228
MM 68,180,224
MM 36,180,220
MM 4,180,216
s_waitcnt lgkmcnt(4)
RT 180,205,8192
RT 182,205,9216
MM 8,184,216
DM 217,12,84,1024
MM 40,184,220
MM 72,184,224
MM 104,184,228
MM 136,184,232
MM 168,184,236
DM 218,12,84,2048
s_waitcnt lgkmcnt(4)
MM 172,188,236
s_bfe_u32 s83, s78, 0x40004
s_mul_i32 s83, s83, 7
s_and_b32 s83, s83, 15
MM 140,188,232
s_lshl_b32 s83, s83, 7
RT 184,206,8192
RT 186,206,9216
MM 108,188,228
MM 76,188,224
MM 44,188,220
DM 219,12,84,3072
MM 12,188,216
s_waitcnt lgkmcnt(4)
s_add_u32 m0, s85, 0
RT 188,207,8192
MM 16,176,216
RT 190,207,9216
MM 48,176,220
MM 80,176,224
MM 112,176,228
DM 212,8,83,0
MM 144,176,232
MM 176,176,236
s_waitcnt lgkmcnt(4)
MM 180,180,236
MM 148,180,232
MM 116,180,228
DM 213,8,83,1024
MM 84,180,224
MM 52,180,220
MM 20,180,216
s_waitcnt lgkmcnt(2)
MM 24,184,216
s_waitcnt vmcnt(2)
s_barrier
MM 56,184,220
RT 176,200,16384
RT 178,200,17408
MM 88,184,224
RT 180,200,24576
RT 182,200,25600
MM 120,184,228
RB 240,208,34816
RB 244,209,34816
MM 152,184,232
RB 248,209,32768
RB 252,208,32768
MM 184,184,236
DM 214,8,83,2048
s_waitcnt lgkmcnt(8)
MM 188,188,236
s_add_u32 m0, s85, 12288
s_bfe_u32 s83, s78, 0x40004
s_mul_i32 s83, s83, 7
MM 156,188,232
s_and_b32 s83, s83, 15
s_lshl_b32 s83, s83, 7
s_add_u32 s83, s83, 196608
MM 124,188,228
RB 212,208,30720
RB 208,209,30720
MM 92,188,224
RB 204,209,28672
RB 200,208,28672
MM 60,188,220
DM 212,8,83,0
MM 28,188,216
s_waitcnt lgkmcnt(4)
RB 196,208,26624
RB 192,209,26624
MM 160,176,240
RB 236,209,24576
RB 232,208,24576
MM 160,180,244
RT 184,201,16384
RT 186,201,17408
MM 128,180,248
RT 188,201,24576
RT 190,201,25600
MM 128,176,252
s_waitcnt lgkmcnt(8)
v_add_u32 v197, 1, v197
MM 96,176,212
DM 213,8,83,1024
MM 96,180,208
s_bfe_u32 s84, s78, 0x40004
s_mul_i32 s84, s84, 7
s_and_b32 s84, s84, 15
MM 64,180,204
s_lshl_b32 s84, s84, 19
s_add_u32 s84, s84, s87
MM 64,176,200
s_waitcnt lgkmcnt(4)
MM 32,176,196
MM 32,180,192
s_barrier
MM 0,180,236
DM 214,8,83,2048
MM 0,176,232
s_waitcnt lgkmcnt(0)
s_add_u32 m0, s86, 32768
RT 180,202,16384
MM 4,184,232
RT 182,202,17408
RT 176,202,24576
MM 4,188,236
RT 178,202,25600
MM 36,188,192
MM 36,184,196
DM 216,12,84,0
MM 68,184,200
buffer_store_dword v197, off, s[40:43], s77 offset:2560 sc1
MM 68,188,204
MM 100,188,208
MM 100,184,212
MM 132,184,252
DM 217,12,84,1024
MM 132,188,248
MM 164,188,244
RT 188,203,16384
RT 190,203,17408
MM 164,184,240
s_waitcnt lgkmcnt(2)
RT 184,203,24576
RT 186,203,25600
MM 168,180,240
MM 168,176,244
DM 218,12,84,2048
MM 136,176,248
MM 136,180,252
MM 104,180,212
MM 104,176,208
MM 72,176,204
DM 219,12,84,3072
MM 72,180,200
s_add_u32 m0, s86, 40960
s_bfe_u32 s84, s78, 0x40004
s_mul_i32 s84, s84, 7
MM 40,180,196
s_and_b32 s84, s84, 15
s_lshl_b32 s84, s84, 19
s_add_u32 s84, s84, s88
MM 40,176,192
MM 8,176,236
RT 176,204,16384
RT 178,204,17408
MM 8,180,232
s_waitcnt lgkmcnt(2)
RT 180,204,24576
RT 182,204,25600
MM 12,188,232
DM 216,12,84,0
MM 12,184,236
MM 44,184,192
MM 44,188,196
MM 76,188,200
MM 76,184,204
DM 217,12,84,1024
MM 108,184,208
MM 108,188,212
MM 140,188,252
MM 140,184,248
MM 172,184,244
DM 218,12,84,2048
MM 172,188,240
s_waitcnt lgkmcnt(0)
RT 184,205,16384
RT 186,205,17408
MM 176,176,240
s_bfe_u32 s83, s78, 0x40008
RT 188,205,24576
RT 190,205,25600
MM 176,180,244
s_mul_i32 s83, s83, 7
s_and_b32 s83, s83, 15
s_lshl_b32 s83, s83, 7
MM 144,180,248
MM 144,176,252
DM 219,12,84,3072
MM 112,176,212
s_add_u32 m0, s85, 24576
MM 112,180,208
MM 80,180,204
MM 80,176,200
MM 48,176,196
DM 212,8,83,0
MM 48,180,192
MM 16,180,236
RT 180,206,16384
RT 182,206,17408
MM 16,176,232
s_waitcnt lgkmcnt(2)
RT 176,206,24576
RT 178,206,25600
MM 20,184,232
MM 20,188,236
DM 213,8,83,1024
MM 52,188,192
MM 52,184,196
MM 84,184,200
MM 84,188,204
MM 116,188,208
DM 214,8,83,2048
MM 116,184,212
s_add_u32 m0, s85, 36864
s_bfe_u32 s83, s78, 0x40008
s_mul_i32 s83, s83, 7
MM 148,184,252
s_and_b32 s83, s83, 15
s_lshl_b32 s83, s83, 7
s_add_u32 s83, s83, 196608
MM 148,188,248
MM 180,188,244
RT 188,207,16384
RT 190,207,17408
MM 180,184,240
s_waitcnt lgkmcnt(2)
RT 184,207,24576
RT 186,207,25600
MM 184,180,240
DM 212,8,83,0
MM 184,176,244
MM 152,176,248
MM 152,180,252
MM 120,180,212
MM 120,176,208
DM 213,8,83,1024
MM 88,176,204
MM 88,180,200
s_bfe_u32 s84, s78, 0x40008
s_mul_i32 s84, s84, 7
s_and_b32 s84, s84, 15
MM 56,180,196
s_lshl_b32 s84, s84, 19
s_add_u32 s84, s84, s87
MM 56,176,192
MM 24,176,236
DM 214,8,83,2048
MM 24,180,232
s_waitcnt lgkmcnt(0)
s_add_u32 m0, s86, 0
MM 28,188,232
s_waitcnt vmcnt(6) lgkmcnt(0)
s_barrier
MM 28,184,236
RT 176,200,32768
RT 178,200,33792
MM 60,184,192
RT 180,200,40960
RT 182,200,41984
MM 60,188,196
RB 228,208,0
RB 224,209,0
MM 92,188,200
RB 220,209,2048
RB 216,208,2048
MM 92,184,204
DM 216,12,84,0
MM 124,184,208
RB 232,208,4096
RB 236,209,4096
MM 124,188,212
RB 192,209,6144
RB 196,208,6144
MM 156,188,252
RB 200,208,8192
RB 204,209,8192
MM 156,184,248
RB 208,209,10240
RB 212,208,10240
MM 188,184,244
DM 217,12,84,1024
MM 188,188,240
)ASM"
R"ASM(s_waitcnt lgkmcnt(8)
RT 184,201,32768
RT 186,201,33792
MM 0,176,228
RT 188,201,40960
RT 190,201,41984
MM 0,180,224
MM 32,180,220
MM 32,176,216
s_waitcnt lgkmcnt(8)
MM 64,176,232
DM 218,12,84,2048
MM 64,180,236
MM 96,180,192
MM 96,176,196
s_waitcnt lgkmcnt(4)
MM 128,176,200
DM 219,12,84,3072
MM 128,180,204
s_barrier
s_add_u32 m0, s86, 8192
s_bfe_u32 s84, s78, 0x40008
MM 160,180,208
s_mul_i32 s84, s84, 7
s_and_b32 s84, s84, 15
s_lshl_b32 s84, s84, 19
MM 160,176,212
s_waitcnt lgkmcnt(0)
s_add_u32 s84, s84, s88
RT 180,202,32768
MM 164,184,212
RT 182,202,33792
RT 176,202,40960
MM 164,188,208
DM 216,12,84,0
MM 132,188,204
RT 178,202,41984
MM 132,184,200
MM 100,184,196
MM 100,188,192
DM 217,12,84,1024
MM 68,188,236
MM 68,184,232
MM 36,184,216
s_bfe_u32 s83, s78, 0x4000c
s_mul_i32 s83, s83, 7
s_and_b32 s83, s83, 15
MM 36,188,220
DM 218,12,84,2048
MM 4,188,224
s_lshl_b32 s83, s83, 7
RT 188,203,32768
RT 190,203,33792
MM 4,184,228
s_waitcnt lgkmcnt(2)
RT 184,203,40960
RT 186,203,41984
MM 8,180,228
MM 8,176,224
DM 219,12,84,3072
MM 40,176,220
s_add_u32 m0, s85, 0
MM 40,180,216
MM 72,180,232
MM 72,176,236
MM 104,176,192
DM 212,8,83,0
MM 104,180,196
MM 136,180,200
MM 136,176,204
MM 168,176,208
RT 176,204,32768
RT 178,204,33792
MM 168,180,212
s_waitcnt lgkmcnt(2)
RT 180,204,40960
RT 182,204,41984
MM 172,188,212
DM 213,8,83,1024
MM 172,184,208
MM 140,184,204
MM 140,188,200
MM 108,188,196
DM 214,8,83,2048
MM 108,184,192
s_add_u32 m0, s85, 12288
s_bfe_u32 s83, s78, 0x4000c
s_mul_i32 s83, s83, 7
MM 76,184,236
s_and_b32 s83, s83, 15
s_lshl_b32 s83, s83, 7
s_add_u32 s83, s83, 196608
MM 76,188,232
MM 44,188,216
DM 212,8,83,0
MM 44,184,220
MM 12,184,224
s_bfe_u32 s84, s78, 0x4000c
RT 184,205,32768
RT 186,205,33792
MM 12,188,228
s_waitcnt lgkmcnt(2)
RT 188,205,40960
RT 190,205,41984
MM 16,176,228
DM 213,8,83,1024
MM 16,180,224
s_mul_i32 s84, s84, 7
s_and_b32 s84, s84, 15
s_lshl_b32 s84, s84, 19
MM 48,180,220
s_add_u32 s84, s84, s87
MM 48,176,216
MM 80,176,232
DM 214,8,83,2048
MM 80,180,236
s_add_u32 m0, s86, 16384
MM 112,180,192
MM 112,176,196
MM 144,176,200
DM 216,12,84,0
MM 144,180,204
MM 176,180,208
RT 180,206,32768
RT 182,206,33792
MM 176,176,212
s_waitcnt lgkmcnt(2)
RT 176,206,40960
RT 178,206,41984
MM 180,184,212
DM 217,12,84,1024
MM 180,188,208
MM 148,188,204
MM 148,184,200
MM 116,184,196
DM 218,12,84,2048
MM 116,188,192
MM 84,188,236
MM 84,184,232
MM 52,184,216
DM 219,12,84,3072
MM 52,188,220
s_add_u32 m0, s86, 24576
s_bfe_u32 s84, s78, 0x4000c
s_mul_i32 s84, s84, 7
MM 20,188,224
s_and_b32 s84, s84, 15
s_lshl_b32 s84, s84, 19
s_add_u32 s84, s84, s88
MM 20,184,228
s_waitcnt lgkmcnt(0)
RT 188,207,32768
RT 190,207,33792
MM 24,180,228
DM 216,12,84,0
MM 24,176,224
RT 184,207,40960
RT 186,207,41984
MM 56,176,220
MM 56,180,216
MM 88,180,232
DM 217,12,84,1024
MM 88,176,236
MM 120,176,192
MM 120,180,196
MM 152,180,200
DM 218,12,84,2048
MM 152,176,204
MM 184,176,208
MM 184,180,212
s_waitcnt lgkmcnt(0)
MM 188,188,212
s_waitcnt vmcnt(13) lgkmcnt(0)
s_barrier
MM 188,184,208
RT 176,200,0
RT 178,200,1024
MM 156,184,204
RT 180,200,8192
RT 182,200,9216
MM 156,188,200
RB 252,208,34816
RB 248,209,34816
MM 124,188,196
RB 244,209,32768
RB 240,208,32768
MM 124,184,192
DM 219,12,84,3072
MM 92,184,236
RB 212,208,30720
RB 208,209,30720
MM 92,188,232
RB 204,209,28672
RB 200,208,28672
MM 60,188,216
RB 196,208,26624
RB 192,209,26624
MM 60,184,220
RB 236,209,24576
RB 232,208,24576
MM 28,184,224
RT 184,201,0
RT 186,201,1024
MM 28,188,228
s_waitcnt lgkmcnt(10)
RT 188,201,8192
RT 190,201,9216
MM 160,176,252
s_bfe_u32 s83, s78, 0x40000
s_mul_i32 s83, s83, 7
s_add_u32 s83, s83, 28
MM 160,180,248
s_and_b32 s83, s83, 15
s_lshl_b32 s83, s83, 7
MM 128,180,244
MM 128,176,240
s_waitcnt lgkmcnt(8)
MM 96,176,212
s_add_u32 m0, s85, 24576
MM 96,180,208
MM 64,180,204
MM 64,176,200
s_waitcnt lgkmcnt(4)
MM 32,176,196
MM 32,180,192
s_barrier
MM 0,180,236
DM 212,8,83,0
MM 0,176,232
s_waitcnt lgkmcnt(0)
RT 180,202,0
RT 182,202,1024
MM 4,184,232
RT 176,202,8192
RT 178,202,9216
MM 4,188,236
MM 36,188,192
MM 36,184,196
DM 213,8,83,1024
MM 68,184,200
MM 68,188,204
MM 100,188,208
MM 100,184,212
MM 132,184,240
DM 214,8,83,2048
MM 132,188,244
s_add_u32 m0, s85, 36864
s_bfe_u32 s83, s78, 0x40000
s_mul_i32 s83, s83, 7
MM 164,188,248
s_add_u32 s83, s83, 28
s_and_b32 s83, s83, 15
s_lshl_b32 s83, s83, 7
MM 164,184,252
s_waitcnt lgkmcnt(0)
s_add_u32 s83, s83, 196608
RT 188,203,0
MM 168,180,252
RT 190,203,1024
RT 184,203,8192
MM 168,176,248
DM 212,8,83,0
MM 136,176,244
s_add_u32 s90, s59, 0
RT 186,203,9216
s_and_b32 s90, s90, 39936
MM 136,180,240
MM 104,180,212
MM 104,176,208
MM 72,176,204
DM 213,8,83,1024
MM 72,180,200
MM 40,180,196
MM 40,176,192
buffer_load_dwordx4 v[240:243], v232, s[16:19], s90 offen offset:0 nt
MM 8,176,236
RT 176,204,0
RT 178,204,1024
MM 8,180,232
s_waitcnt lgkmcnt(2)
RT 180,204,8192
RT 182,204,9216
MM 12,188,232
buffer_load_dwordx4 v[244:247], v233, s[16:19], s90 offen offset:1024 nt
MM 12,184,236
MM 44,184,192
MM 44,188,196
buffer_load_dwordx4 v[248:251], v234, s[16:19], s90 offen offset:2048 nt
MM 76,188,200
MM 76,184,204
MM 108,184,208
buffer_load_dwordx4 v[252:255], v235, s[16:19], s90 offen offset:3072 nt
MM 108,188,212
s_add_u32 s90, s90, 65536
MM 140,188,240
MM 140,184,244
buffer_load_dwordx4 a[0:3], v232, s[16:19], s90 offen offset:0 nt
MM 172,184,248
RT 184,205,0
RT 186,205,1024
MM 172,188,252
s_waitcnt lgkmcnt(2)
RT 188,205,8192
RT 190,205,9216
MM 176,176,252
buffer_load_dwordx4 a[4:7], v233, s[16:19], s90 offen offset:1024 nt
MM 176,180,248
MM 144,180,244
MM 144,176,240
buffer_load_dwordx4 a[8:11], v234, s[16:19], s90 offen offset:2048 nt
MM 112,176,212
MM 112,180,208
MM 80,180,204
buffer_load_dwordx4 a[12:15], v235, s[16:19], s90 offen offset:3072 nt
MM 80,176,200
MM 48,176,196
MM 48,180,192
MM 16,180,236
RT 180,206,0
RT 182,206,1024
MM 16,176,232
s_waitcnt lgkmcnt(2)
RT 176,206,8192
RT 178,206,9216
MM 20,184,232
MM 20,188,236
MM 52,188,192
MM 52,184,196
MM 84,184,200
MM 84,188,204
MM 116,188,208
MM 116,184,212
MM 148,184,240
MM 148,188,244
MM 180,188,248
RT 188,207,0
RT 190,207,1024
MM 180,184,252
s_waitcnt lgkmcnt(2)
RT 184,207,8192
RT 186,207,9216
MM 184,180,252
MM 184,176,248
MM 152,176,244
MM 152,180,240
MM 120,180,212
MM 120,176,208
MM 88,176,204
MM 88,180,200
MM 56,180,196
MM 56,176,192
MM 24,176,236
MM 24,180,232
s_waitcnt lgkmcnt(0)
s_bfe_u32 s84, s78, 0x40000
s_mul_i32 s84, s84, 7
MM 28,188,232
s_waitcnt vmcnt(13) lgkmcnt(0)
s_barrier
s_add_u32 s84, s84, 28
MM 28,184,236
s_and_b32 s84, s84, 15
RT 176,200,16384
RT 178,200,17408
MM 60,184,192
s_lshl_b32 s84, s84, 19
RT 180,200,24576
RT 182,200,25600
MM 60,188,196
s_add_u32 s84, s84, s87
RB 216,208,0
RB 220,209,0
MM 92,188,200
RB 224,209,2048
RB 228,208,2048
MM 92,184,204
DM 214,8,83,2048
MM 124,184,208
s_add_u32 m0, s86, 32768
RB 232,208,4096
RB 236,209,4096
MM 124,188,212
RB 192,209,6144
RB 196,208,6144
MM 156,188,240
RB 200,208,8192
RB 204,209,8192
MM 156,184,244
DM 216,12,84,0
MM 188,184,248
RB 208,209,10240
RB 212,208,10240
MM 188,188,252
s_waitcnt lgkmcnt(8)
RT 184,201,16384
RT 186,201,17408
MM 0,176,216
RT 188,201,24576
RT 190,201,25600
MM 0,180,220
DM 217,12,84,1024
MM 32,180,224
MM 32,176,228
s_waitcnt lgkmcnt(8)
MM 64,176,232
MM 64,180,236
DM 218,12,84,2048
MM 96,180,192
MM 96,176,196
s_waitcnt lgkmcnt(4)
MM 128,176,200
MM 128,180,204
s_barrier
MM 160,180,208
DM 219,12,84,3072
MM 160,176,212
s_waitcnt lgkmcnt(0)
s_add_u32 m0, s86, 40960
s_bfe_u32 s84, s78, 0x40000
MM 164,184,212
s_mul_i32 s84, s84, 7
s_add_u32 s84, s84, 28
s_and_b32 s84, s84, 15
MM 164,188,208
s_lshl_b32 s84, s84, 19
s_add_u32 s84, s84, s88
RT 180,202,16384
MM 132,188,204
DM 216,12,84,0
MM 132,184,200
RT 182,202,17408
RT 176,202,24576
MM 100,184,196
RT 178,202,25600
MM 100,188,192
MM 68,188,236
DM 217,12,84,1024
MM 68,184,232
MM 36,184,228
MM 36,188,224
s_bfe_u32 s83, s78, 0x40004
s_mul_i32 s83, s83, 7
s_add_u32 s83, s83, 28
MM 4,188,220
DM 218,12,84,2048
MM 4,184,216
s_waitcnt lgkmcnt(0)
RT 188,203,16384
RT 190,203,17408
MM 8,180,216
s_and_b32 s83, s83, 15
RT 184,203,24576
RT 186,203,25600
MM 8,176,220
s_lshl_b32 s83, s83, 7
MM 40,176,224
DM 219,12,84,3072
MM 40,180,228
s_add_u32 m0, s85, 0
MM 72,180,232
MM 72,176,236
MM 104,176,192
MM 104,180,196
DM 212,8,83,0
MM 136,180,200
MM 136,176,204
MM 168,176,208
RT 176,204,16384
RT 178,204,17408
MM 168,180,212
s_waitcnt lgkmcnt(2)
RT 180,204,24576
RT 182,204,25600
MM 172,188,212
DM 213,8,83,1024
MM 172,184,208
MM 140,184,204
MM 140,188,200
MM 108,188,196
DM 214,8,83,2048
MM 108,184,192
s_add_u32 m0, s85, 12288
s_bfe_u32 s83, s78, 0x40004
s_mul_i32 s83, s83, 7
MM 76,184,236
s_add_u32 s83, s83, 28
s_and_b32 s83, s83, 15
s_lshl_b32 s83, s83, 7
MM 76,188,232
s_add_u32 s83, s83, 196608
MM 44,188,228
DM 212,8,83,0
MM 44,184,224
MM 12,184,220
s_bfe_u32 s84, s78, 0x40004
RT 184,205,16384
RT 186,205,17408
MM 12,188,216
s_waitcnt lgkmcnt(2)
RT 188,205,24576
RT 190,205,25600
MM 16,176,216
DM 213,8,83,1024
MM 16,180,220
s_mul_i32 s84, s84, 7
s_add_u32 s84, s84, 28
s_and_b32 s84, s84, 15
MM 48,180,224
s_lshl_b32 s84, s84, 19
s_add_u32 s84, s84, s87
MM 48,176,228
MM 80,176,232
DM 214,8,83,2048
MM 80,180,236
s_add_u32 m0, s86, 0
MM 112,180,192
MM 112,176,196
MM 144,176,200
DM 216,12,84,0
MM 144,180,204
MM 176,180,208
RT 180,206,16384
RT 182,206,17408
MM 176,176,212
s_waitcnt lgkmcnt(2)
RT 176,206,24576
RT 178,206,25600
MM 180,184,212
DM 217,12,84,1024
MM 180,188,208
MM 148,188,204
MM 148,184,200
MM 116,184,196
DM 218,12,84,2048
MM 116,188,192
MM 84,188,236
MM 84,184,232
MM 52,184,228
DM 219,12,84,3072
MM 52,188,224
s_add_u32 m0, s86, 8192
s_bfe_u32 s84, s78, 0x40004
s_mul_i32 s84, s84, 7
MM 20,188,220
s_add_u32 s84, s84, 28
s_and_b32 s84, s84, 15
s_lshl_b32 s84, s84, 19
MM 20,184,216
s_add_u32 s84, s84, s88
s_waitcnt lgkmcnt(0)
RT 188,207,16384
MM 24,180,216
DM 216,12,84,0
MM 24,176,220
RT 190,207,17408
RT 184,207,24576
MM 56,176,224
RT 186,207,25600
MM 56,180,228
MM 88,180,232
DM 217,12,84,1024
MM 88,176,236
MM 120,176,192
MM 120,180,196
MM 152,180,200
DM 218,12,84,2048
MM 152,176,204
MM 184,176,208
MM 184,180,212
s_waitcnt lgkmcnt(0)
MM 188,188,212
s_waitcnt vmcnt(13) lgkmcnt(0)
s_barrier
MM 188,184,208
RT 176,200,32768
RT 178,200,33792
MM 156,184,204
RT 180,200,40960
RT 182,200,41984
MM 156,188,200
RB 240,208,34816
RB 244,209,34816
MM 124,188,196
RB 248,209,32768
RB 252,208,32768
MM 124,184,192
DM 219,12,84,3072
MM 92,184,236
RB 212,208,30720
RB 208,209,30720
MM 92,188,232
RB 204,209,28672
RB 200,208,28672
MM 60,188,228
s_add_u32 s90, s59, 0
RB 196,208,26624
RB 192,209,26624
MM 60,184,224
s_and_b32 s90, s90, 39936
RB 236,209,24576
RB 232,208,24576
MM 28,184,220
RT 184,201,32768
RT 186,201,33792
MM 28,188,216
s_waitcnt lgkmcnt(10)
RT 188,201,40960
RT 190,201,41984
MM 160,176,240
MM 160,180,244
MM 128,180,248
MM 128,176,252
s_waitcnt lgkmcnt(8)
MM 96,176,212
buffer_load_dwordx4 a[16:19], v236, s[20:23], s90 offen offset:0 nt
MM 96,180,208
MM 64,180,204
MM 64,176,200
s_waitcnt lgkmcnt(4)
MM 32,176,196
buffer_load_dwordx4 a[20:23], v199, s[20:23], s90 offen offset:1024 nt
MM 32,180,192
s_barrier
MM 0,180,236
buffer_load_dwordx4 a[24:27], v238, s[20:23], s90 offen offset:2048 nt
MM 0,176,232
s_waitcnt lgkmcnt(0)
RT 180,202,32768
RT 182,202,33792
MM 4,184,232
RT 176,202,40960
RT 178,202,41984
MM 4,188,236
buffer_load_dwordx4 a[28:31], v239, s[20:23], s90 offen offset:3072 nt
MM 36,188,192
s_add_u32 s90, s90, 65536
MM 36,184,196
MM 68,184,200
buffer_load_dwordx4 a[32:35], v236, s[20:23], s90 offen offset:0 nt
MM 68,188,204
MM 100,188,208
MM 100,184,212
buffer_load_dwordx4 a[36:39], v199, s[20:23], s90 offen offset:1024 nt
MM 132,184,252
MM 132,188,248
MM 164,188,244
buffer_load_dwordx4 a[40:43], v238, s[20:23], s90 offen offset:2048 nt
MM 164,184,240
s_waitcnt lgkmcnt(0)
RT 188,203,32768
RT 190,203,33792
MM 168,180,240
RT 184,203,40960
RT 186,203,41984
MM 168,176,244
buffer_load_dwordx4 a[44:47], v239, s[20:23], s90 offen offset:3072 nt
MM 136,176,248
MM 136,180,252
MM 104,180,212
MM 104,176,208
MM 72,176,204
MM 72,180,200
MM 40,180,196
MM 40,176,192
MM 8,176,236
RT 176,204,32768
RT 178,204,33792
MM 8,180,232
s_waitcnt lgkmcnt(2)
RT 180,204,40960
RT 182,204,41984
MM 12,188,232
MM 12,184,236
MM 44,184,192
MM 44,188,196
MM 76,188,200
MM 76,184,204
MM 108,184,208
MM 108,188,212
MM 140,188,252
MM 140,184,248
MM 172,184,244
s_bfe_u32 s83, s78, 0x40008
RT 184,205,32768
RT 186,205,33792
MM 172,188,240
s_waitcnt lgkmcnt(2)
RT 188,205,40960
RT 190,205,41984
MM 176,176,240
s_mul_i32 s83, s83, 7
s_add_u32 s83, s83, 28
s_and_b32 s83, s83, 15
MM 176,180,244
s_lshl_b32 s83, s83, 7
MM 144,180,248
s_add_u32 m0, s85, 24576
MM 144,176,252
MM 112,176,212
MM 112,180,208
MM 80,180,204
MM 80,176,200
MM 48,176,196
DM 212,8,83,0
MM 48,180,192
MM 16,180,236
RT 180,206,32768
RT 182,206,33792
MM 16,176,232
s_waitcnt lgkmcnt(2)
RT 176,206,40960
RT 178,206,41984
MM 20,184,232
DM 213,8,83,1024
MM 20,188,236
MM 52,188,192
MM 52,184,196
MM 84,184,200
DM 214,8,83,2048
MM 84,188,204
s_add_u32 m0, s85, 36864
s_bfe_u32 s83, s78, 0x40008
s_mul_i32 s83, s83, 7
MM 116,188,208
s_add_u32 s83, s83, 28
s_and_b32 s83, s83, 15
s_lshl_b32 s83, s83, 7
MM 116,184,212
s_add_u32 s83, s83, 196608
MM 148,184,252
DM 212,8,83,0
MM 148,188,248
MM 180,188,244
s_bfe_u32 s84, s78, 0x40008
RT 188,207,32768
RT 190,207,33792
MM 180,184,240
s_waitcnt lgkmcnt(2)
RT 184,207,40960
RT 186,207,41984
MM 184,180,240
DM 213,8,83,1024
MM 184,176,244
s_mul_i32 s84, s84, 7
s_add_u32 s84, s84, 28
s_and_b32 s84, s84, 15
MM 152,176,248
s_lshl_b32 s84, s84, 19
s_add_u32 s84, s84, s87
MM 152,180,252
MM 120,180,212
DM 214,8,83,2048
MM 120,176,208
s_add_u32 m0, s86, 16384
MM 88,176,204
MM 88,180,200
MM 56,180,196
DM 216,12,84,0
MM 56,176,192
MM 24,176,236
MM 24,180,232
s_waitcnt lgkmcnt(0)
MM 28,188,232
s_waitcnt vmcnt(15) lgkmcnt(0)
s_barrier
MM 28,184,236
RT 176,200,0
RT 178,200,1024
MM 60,184,192
RT 180,200,8192
RT 182,200,9216
MM 60,188,196
RB 228,208,0
RB 224,209,0
MM 92,188,200
RB 220,209,2048
RB 216,208,2048
MM 92,184,204
DM 217,12,84,1024
MM 124,184,208
RB 232,208,4096
RB 236,209,4096
MM 124,188,212
RB 192,209,6144
RB 196,208,6144
MM 156,188,252
RB 200,208,8192
RB 204,209,8192
MM 156,184,248
DM 218,12,84,2048
MM 188,184,244
RB 208,209,10240
RB 212,208,10240
MM 188,188,240
s_waitcnt lgkmcnt(8)
RT 184,201,0
RT 186,201,1024
MM 0,176,228
RT 188,201,8192
RT 190,201,9216
MM 0,180,224
DM 219,12,84,3072
MM 32,180,220
s_add_u32 m0, s86, 24576
s_bfe_u32 s84, s78, 0x40008
s_mul_i32 s84, s84, 7
MM 32,176,216
s_waitcnt lgkmcnt(8)
s_add_u32 s84, s84, 28
s_and_b32 s84, s84, 15
MM 64,176,232
s_lshl_b32 s84, s84, 19
s_add_u32 s84, s84, s88
MM 64,180,236
DM 216,12,84,0
MM 96,180,192
MM 96,176,196
s_waitcnt lgkmcnt(4)
MM 128,176,200
MM 128,180,204
s_barrier
MM 160,180,208
DM 217,12,84,1024
MM 160,176,212
s_waitcnt lgkmcnt(0)
RT 180,202,0
RT 182,202,1024
MM 164,184,212
RT 176,202,8192
RT 178,202,9216
MM 164,188,208
MM 132,188,204
DM 218,12,84,2048
MM 132,184,200
s_bfe_u32 s83, s78, 0x4000c
s_mul_i32 s83, s83, 7
s_add_u32 s83, s83, 28
MM 100,184,196
s_and_b32 s83, s83, 15
s_lshl_b32 s83, s83, 7
MM 100,188,192
MM 68,188,236
DM 219,12,84,3072
MM 68,184,232
s_add_u32 m0, s85, 0
MM 36,184,216
MM 36,188,220
MM 4,188,224
RT 188,203,0
RT 190,203,1024
MM 4,184,228
s_waitcnt lgkmcnt(2)
RT 184,203,8192
RT 186,203,9216
MM 8,180,228
DM 212,8,83,0
MM 8,176,224
MM 40,176,220
MM 40,180,216
MM 72,180,232
MM 72,176,236
DM 213,8,83,1024
MM 104,176,192
MM 104,180,196
MM 136,180,200
MM 136,176,204
MM 168,176,208
DM 214,8,83,2048
MM 168,180,212
s_waitcnt lgkmcnt(0)
s_add_u32 m0, s85, 12288
s_bfe_u32 s83, s78, 0x4000c
MM 172,188,212
s_mul_i32 s83, s83, 7
s_add_u32 s83, s83, 28
s_and_b32 s83, s83, 15
MM 172,184,208
s_lshl_b32 s83, s83, 7
s_add_u32 s83, s83, 196608
RT 176,204,0
MM 140,184,204
DM 212,8,83,0
MM 140,188,200
RT 178,204,1024
RT 180,204,8192
MM 108,188,196
s_bfe_u32 s84, s78, 0x4000c
RT 182,204,9216
s_mul_i32 s84, s84, 7
MM 108,184,192
s_add_u32 s84, s84, 28
s_and_b32 s84, s84, 15
s_lshl_b32 s84, s84, 19
MM 76,184,236
DM 213,8,83,1024
MM 76,188,232
s_add_u32 s84, s84, s87
MM 44,188,216
MM 44,184,220
MM 12,184,224
DM 214,8,83,2048
MM 12,188,228
s_waitcnt lgkmcnt(0)
s_add_u32 m0, s86, 32768
RT 184,205,0
MM 16,176,228
RT 186,205,1024
RT 188,205,8192
MM 16,180,224
RT 190,205,9216
MM 48,180,220
DM 216,12,84,0
MM 48,176,216
MM 80,176,232
MM 80,180,236
MM 112,180,192
DM 217,12,84,1024
MM 112,176,196
MM 144,176,200
MM 144,180,204
MM 176,180,208
DM 218,12,84,2048
MM 176,176,212
s_waitcnt lgkmcnt(0)
RT 180,206,0
RT 182,206,1024
MM 180,184,212
RT 176,206,8192
RT 178,206,9216
MM 180,188,208
MM 148,188,204
DM 219,12,84,3072
MM 148,184,200
s_add_u32 m0, s86, 40960
s_bfe_u32 s84, s78, 0x4000c
s_mul_i32 s84, s84, 7
MM 116,184,196
s_add_u32 s84, s84, 28
s_and_b32 s84, s84, 15
s_lshl_b32 s84, s84, 19
MM 116,188,192
s_add_u32 s84, s84, s88
s_add_u32 s90, s59, 1024
s_and_b32 s90, s90, 39936
MM 84,188,236
DM 216,12,84,0
MM 84,184,232
MM 52,184,216
MM 52,188,220
MM 20,188,224
DM 217,12,84,1024
MM 20,184,228
s_waitcnt lgkmcnt(0)
RT 188,207,0
RT 190,207,1024
MM 24,180,228
buffer_load_dwordx4 a[48:51], v232, s[16:19], s90 offen offset:0 nt
MM 24,176,224
RT 184,207,8192
RT 186,207,9216
MM 56,176,220
MM 56,180,216
buffer_load_dwordx4 a[52:55], v233, s[16:19], s90 offen offset:1024 nt
MM 88,180,232
MM 88,176,236
MM 120,176,192
buffer_load_dwordx4 a[56:59], v234, s[16:19], s90 offen offset:2048 nt
MM 120,180,196
MM 152,180,200
MM 152,176,204
buffer_load_dwordx4 a[60:63], v235, s[16:19], s90 offen offset:3072 nt
MM 184,176,208
s_add_u32 s90, s90, 65536
MM 184,180,212
s_waitcnt lgkmcnt(0)
MM 188,188,212
s_waitcnt vmcnt(16) lgkmcnt(0)
s_barrier
MM 188,184,208
RT 176,200,16384
RT 178,200,17408
MM 156,184,204
RT 180,200,24576
RT 182,200,25600
MM 156,188,200
RB 252,208,34816
RB 248,209,34816
MM 124,188,196
RB 244,209,32768
RB 240,208,32768
MM 124,184,192
buffer_load_dwordx4 a[64:67], v232, s[16:19], s90 offen offset:0 nt
MM 92,184,236
buffer_load_dwordx4 a[68:71], v233, s[16:19], s90 offen offset:1024 nt
MM 92,188,232
buffer_load_dwordx4 a[72:75], v234, s[16:19], s90 offen offset:2048 nt
MM 60,188,216
RB 212,208,30720
RB 208,209,30720
MM 60,184,220
)ASM"
R"ASM(buffer_load_dwordx4 a[76:79], v235, s[16:19], s90 offen offset:3072 nt
RB 204,209,28672
RB 200,208,28672
MM 28,184,224
RB 196,208,26624
RB 192,209,26624
MM 28,188,228
s_waitcnt lgkmcnt(6)
RB 236,209,24576
RB 232,208,24576
MM 160,176,252
RT 184,201,16384
RT 186,201,17408
MM 160,180,248
RT 188,201,24576
RT 190,201,25600
MM 128,180,244
MM 128,176,240
s_waitcnt lgkmcnt(8)
MM 96,176,212
MM 96,180,208
MM 64,180,204
MM 64,176,200
s_waitcnt lgkmcnt(4)
MM 32,176,196
MM 32,180,192
s_barrier
MM 0,180,236
RT 180,202,16384
RT 182,202,17408
MM 0,176,232
s_waitcnt lgkmcnt(2)
RT 176,202,24576
RT 178,202,25600
MM 4,184,232
MM 4,188,236
MM 36,188,192
MM 36,184,196
MM 68,184,200
MM 68,188,204
MM 100,188,208
MM 100,184,212
MM 132,184,240
MM 132,188,244
MM 164,188,248
RT 188,203,16384
RT 190,203,17408
MM 164,184,252
s_waitcnt lgkmcnt(2)
RT 184,203,24576
RT 186,203,25600
MM 168,180,252
MM 168,176,248
MM 136,176,244
s_bfe_u32 s83, s78, 0x40000
s_mul_i32 s83, s83, 7
s_add_u32 s83, s83, 56
MM 136,180,240
s_and_b32 s83, s83, 15
s_lshl_b32 s83, s83, 7
MM 104,180,212
DM 218,12,84,2048
MM 104,176,208
MM 72,176,204
MM 72,180,200
MM 40,180,196
DM 219,12,84,3072
MM 40,176,192
s_add_u32 m0, s85, 24576
MM 8,176,236
RT 176,204,16384
RT 178,204,17408
MM 8,180,232
s_waitcnt lgkmcnt(2)
RT 180,204,24576
RT 182,204,25600
MM 12,188,232
DM 212,8,83,0
MM 12,184,236
MM 44,184,192
MM 44,188,196
MM 76,188,200
DM 213,8,83,1024
MM 76,184,204
MM 108,184,208
MM 108,188,212
MM 140,188,240
DM 214,8,83,2048
MM 140,184,244
s_add_u32 m0, s85, 36864
s_bfe_u32 s83, s78, 0x40000
s_mul_i32 s83, s83, 7
MM 172,184,248
s_add_u32 s83, s83, 56
s_and_b32 s83, s83, 15
s_lshl_b32 s83, s83, 7
MM 172,188,252
s_add_u32 s83, s83, 196608
s_waitcnt lgkmcnt(0)
RT 184,205,16384
MM 176,176,252
DM 212,8,83,0
MM 176,180,248
RT 186,205,17408
RT 188,205,24576
MM 144,180,244
s_bfe_u32 s84, s78, 0x40000
RT 190,205,25600
s_mul_i32 s84, s84, 7
MM 144,176,240
s_add_u32 s84, s84, 56
s_and_b32 s84, s84, 15
s_lshl_b32 s84, s84, 19
MM 112,176,212
DM 213,8,83,1024
MM 112,180,208
s_add_u32 s84, s84, s87
MM 80,180,204
MM 80,176,200
MM 48,176,196
DM 214,8,83,2048
MM 48,180,192
s_add_u32 m0, s86, 0
MM 16,180,236
RT 180,206,16384
RT 182,206,17408
MM 16,176,232
s_waitcnt lgkmcnt(2)
RT 176,206,24576
RT 178,206,25600
MM 20,184,232
DM 216,12,84,0
MM 20,188,236
MM 52,188,192
MM 52,184,196
MM 84,184,200
DM 217,12,84,1024
MM 84,188,204
MM 116,188,208
MM 116,184,212
MM 148,184,240
DM 218,12,84,2048
MM 148,188,244
MM 180,188,248
RT 188,207,16384
RT 190,207,17408
MM 180,184,252
s_waitcnt lgkmcnt(2)
RT 184,207,24576
RT 186,207,25600
MM 184,180,252
DM 219,12,84,3072
MM 184,176,248
s_add_u32 m0, s86, 8192
s_bfe_u32 s84, s78, 0x40000
s_mul_i32 s84, s84, 7
MM 152,176,244
s_add_u32 s84, s84, 56
s_and_b32 s84, s84, 15
s_lshl_b32 s84, s84, 19
MM 152,180,240
s_add_u32 s84, s84, s88
MM 120,180,212
DM 216,12,84,0
MM 120,176,208
MM 88,176,204
MM 88,180,200
MM 56,180,196
DM 217,12,84,1024
MM 56,176,192
MM 24,176,236
MM 24,180,232
s_waitcnt lgkmcnt(0)
MM 28,188,232
s_waitcnt vmcnt(12) lgkmcnt(0)
s_barrier
MM 28,184,236
RT 176,200,32768
RT 178,200,33792
MM 60,184,192
RT 180,200,40960
RT 182,200,41984
MM 60,188,196
RB 216,208,0
RB 220,209,0
MM 92,188,200
RB 224,209,2048
RB 228,208,2048
MM 92,184,204
DM 218,12,84,2048
MM 124,184,208
RB 232,208,4096
RB 236,209,4096
MM 124,188,212
RB 192,209,6144
RB 196,208,6144
MM 156,188,240
RB 200,208,8192
RB 204,209,8192
MM 156,184,244
DM 219,12,84,3072
MM 188,184,248
RB 208,209,10240
RB 212,208,10240
MM 188,188,252
s_waitcnt lgkmcnt(8)
RT 184,201,32768
RT 186,201,33792
MM 0,176,216
s_bfe_u32 s83, s78, 0x40004
RT 188,201,40960
RT 190,201,41984
MM 0,180,220
s_mul_i32 s83, s83, 7
s_add_u32 s83, s83, 56
s_and_b32 s83, s83, 15
MM 32,180,224
s_lshl_b32 s83, s83, 7
MM 32,176,228
s_waitcnt lgkmcnt(8)
MM 64,176,232
s_add_u32 m0, s85, 0
MM 64,180,236
MM 96,180,192
MM 96,176,196
s_waitcnt lgkmcnt(4)
MM 128,176,200
MM 128,180,204
s_barrier
MM 160,180,208
DM 212,8,83,0
MM 160,176,212
s_waitcnt lgkmcnt(0)
RT 180,202,32768
RT 182,202,33792
MM 164,184,212
RT 176,202,40960
RT 178,202,41984
MM 164,188,208
MM 132,188,204
MM 132,184,200
DM 213,8,83,1024
MM 100,184,196
MM 100,188,192
MM 68,188,236
MM 68,184,232
MM 36,184,228
DM 214,8,83,2048
MM 36,188,224
s_add_u32 m0, s85, 12288
s_bfe_u32 s83, s78, 0x40004
s_mul_i32 s83, s83, 7
MM 4,188,220
s_add_u32 s83, s83, 56
s_and_b32 s83, s83, 15
s_lshl_b32 s83, s83, 7
MM 4,184,216
s_waitcnt lgkmcnt(0)
s_add_u32 s83, s83, 196608
RT 188,203,32768
MM 8,180,216
RT 190,203,33792
RT 184,203,40960
MM 8,176,220
DM 212,8,83,0
MM 40,176,224
RT 186,203,41984
MM 40,180,228
MM 72,180,232
MM 72,176,236
MM 104,176,192
DM 213,8,83,1024
MM 104,180,196
s_bfe_u32 s84, s78, 0x40004
s_mul_i32 s84, s84, 7
s_add_u32 s84, s84, 56
MM 136,180,200
s_and_b32 s84, s84, 15
s_lshl_b32 s84, s84, 19
s_add_u32 s84, s84, s87
MM 136,176,204
MM 168,176,208
RT 176,204,32768
RT 178,204,33792
MM 168,180,212
s_waitcnt lgkmcnt(2)
RT 180,204,40960
RT 182,204,41984
MM 172,188,212
DM 214,8,83,2048
MM 172,184,208
s_add_u32 m0, s86, 16384
s_add_u32 s90, s59, 1024
s_and_b32 s90, s90, 39936
MM 140,184,204
MM 140,188,200
MM 108,188,196
DM 216,12,84,0
MM 108,184,192
MM 76,184,236
MM 76,188,232
buffer_load_dwordx4 a[80:83], v236, s[20:23], s90 offen offset:0 nt
MM 44,188,228
MM 44,184,224
MM 12,184,220
buffer_load_dwordx4 a[84:87], v199, s[20:23], s90 offen offset:1024 nt
MM 12,188,216
s_waitcnt lgkmcnt(0)
RT 184,205,32768
RT 186,205,33792
MM 16,176,216
RT 188,205,40960
RT 190,205,41984
MM 16,180,220
buffer_load_dwordx4 a[88:91], v238, s[20:23], s90 offen offset:2048 nt
MM 48,180,224
MM 48,176,228
MM 80,176,232
buffer_load_dwordx4 a[92:95], v239, s[20:23], s90 offen offset:3072 nt
MM 80,180,236
s_add_u32 s90, s90, 65536
MM 112,180,192
MM 112,176,196
buffer_load_dwordx4 a[96:99], v236, s[20:23], s90 offen offset:0 nt
MM 144,176,200
MM 144,180,204
MM 176,180,208
buffer_load_dwordx4 a[100:103], v199, s[20:23], s90 offen offset:1024 nt
MM 176,176,212
s_waitcnt lgkmcnt(0)
RT 180,206,32768
RT 182,206,33792
MM 180,184,212
RT 176,206,40960
RT 178,206,41984
MM 180,188,208
buffer_load_dwordx4 a[104:107], v238, s[20:23], s90 offen offset:2048 nt
MM 148,188,204
MM 148,184,200
MM 116,184,196
buffer_load_dwordx4 a[108:111], v239, s[20:23], s90 offen offset:3072 nt
MM 116,188,192
MM 84,188,236
MM 84,184,232
MM 52,184,228
MM 52,188,224
MM 20,188,220
RT 188,207,32768
RT 190,207,33792
MM 20,184,216
s_waitcnt lgkmcnt(2)
RT 184,207,40960
RT 186,207,41984
MM 24,180,216
MM 24,176,220
MM 56,176,224
MM 56,180,228
MM 88,180,232
MM 88,176,236
MM 120,176,192
MM 120,180,196
MM 152,180,200
MM 152,176,204
MM 184,176,208
MM 184,180,212
s_waitcnt lgkmcnt(0)
MM 188,188,212
s_waitcnt vmcnt(15) lgkmcnt(0)
s_barrier
MM 188,184,208
RT 176,200,0
RT 178,200,1024
MM 156,184,204
RT 180,200,8192
RT 182,200,9216
MM 156,188,200
RB 240,208,34816
RB 244,209,34816
MM 124,188,196
RB 248,209,32768
RB 252,208,32768
MM 124,184,192
RB 212,208,30720
RB 208,209,30720
MM 92,184,236
RB 204,209,28672
RB 200,208,28672
MM 92,188,232
RB 196,208,26624
RB 192,209,26624
MM 60,188,228
RB 236,209,24576
RB 232,208,24576
MM 60,184,224
MM 28,184,220
RT 184,201,0
RT 186,201,1024
MM 28,188,216
s_waitcnt lgkmcnt(10)
RT 188,201,8192
RT 190,201,9216
MM 160,176,240
DM 217,12,84,1024
MM 160,180,244
MM 128,180,248
MM 128,176,252
s_waitcnt lgkmcnt(8)
MM 96,176,212
DM 218,12,84,2048
MM 96,180,208
MM 64,180,204
MM 64,176,200
s_waitcnt lgkmcnt(4)
MM 32,176,196
DM 219,12,84,3072
MM 32,180,192
s_barrier
s_add_u32 m0, s86, 24576
s_bfe_u32 s84, s78, 0x40004
MM 0,180,236
s_mul_i32 s84, s84, 7
s_add_u32 s84, s84, 56
s_and_b32 s84, s84, 15
MM 0,176,232
s_lshl_b32 s84, s84, 19
s_add_u32 s84, s84, s88
s_waitcnt lgkmcnt(0)
MM 4,184,232
DM 216,12,84,0
MM 4,188,236
RT 180,202,0
RT 182,202,1024
MM 36,188,192
RT 176,202,8192
RT 178,202,9216
MM 36,184,196
MM 68,184,200
DM 217,12,84,1024
MM 68,188,204
MM 100,188,208
MM 100,184,212
s_bfe_u32 s83, s78, 0x40008
s_mul_i32 s83, s83, 7
s_add_u32 s83, s83, 56
MM 132,184,252
DM 218,12,84,2048
MM 132,188,248
s_and_b32 s83, s83, 15
s_lshl_b32 s83, s83, 7
MM 164,188,244
RT 188,203,0
RT 190,203,1024
MM 164,184,240
s_waitcnt lgkmcnt(2)
RT 184,203,8192
RT 186,203,9216
MM 168,180,240
DM 219,12,84,3072
MM 168,176,244
s_add_u32 m0, s85, 24576
MM 136,176,248
MM 136,180,252
MM 104,180,212
MM 104,176,208
DM 212,8,83,0
MM 72,176,204
MM 72,180,200
MM 40,180,196
MM 40,176,192
MM 8,176,236
DM 213,8,83,1024
MM 8,180,232
s_waitcnt lgkmcnt(0)
RT 176,204,0
RT 178,204,1024
MM 12,188,232
RT 180,204,8192
RT 182,204,9216
MM 12,184,236
MM 44,184,192
DM 214,8,83,2048
MM 44,188,196
s_add_u32 m0, s85, 36864
s_bfe_u32 s83, s78, 0x40008
s_mul_i32 s83, s83, 7
MM 76,188,200
s_add_u32 s83, s83, 56
s_and_b32 s83, s83, 15
s_lshl_b32 s83, s83, 7
MM 76,184,204
s_add_u32 s83, s83, 196608
MM 108,184,208
DM 212,8,83,0
MM 108,188,212
MM 140,188,252
s_bfe_u32 s84, s78, 0x40008
s_mul_i32 s84, s84, 7
s_add_u32 s84, s84, 56
MM 140,184,248
s_and_b32 s84, s84, 15
s_lshl_b32 s84, s84, 19
s_add_u32 s84, s84, s87
MM 172,184,244
DM 213,8,83,1024
MM 172,188,240
s_waitcnt lgkmcnt(0)
RT 184,205,0
RT 186,205,1024
MM 176,176,240
RT 188,205,8192
RT 190,205,9216
MM 176,180,244
MM 144,180,248
DM 214,8,83,2048
MM 144,176,252
s_add_u32 m0, s86, 32768
MM 112,176,212
MM 112,180,208
MM 80,180,204
DM 216,12,84,0
MM 80,176,200
MM 48,176,196
MM 48,180,192
MM 16,180,236
DM 217,12,84,1024
MM 16,176,232
s_waitcnt lgkmcnt(0)
RT 180,206,0
RT 182,206,1024
MM 20,184,232
RT 176,206,8192
RT 178,206,9216
MM 20,188,236
MM 52,188,192
DM 218,12,84,2048
MM 52,184,196
MM 84,184,200
MM 84,188,204
MM 116,188,208
DM 219,12,84,3072
MM 116,184,212
s_add_u32 m0, s86, 40960
s_bfe_u32 s84, s78, 0x40008
s_mul_i32 s84, s84, 7
MM 148,184,252
s_add_u32 s84, s84, 56
s_and_b32 s84, s84, 15
s_lshl_b32 s84, s84, 19
MM 148,188,248
s_add_u32 s84, s84, s88
MM 180,188,244
DM 216,12,84,0
MM 180,184,240
s_waitcnt lgkmcnt(0)
RT 188,207,0
RT 190,207,1024
MM 184,180,240
RT 184,207,8192
RT 186,207,9216
MM 184,176,244
MM 152,176,248
DM 217,12,84,1024
MM 152,180,252
MM 120,180,212
MM 120,176,208
MM 88,176,204
DM 218,12,84,2048
MM 88,180,200
MM 56,180,196
MM 56,176,192
MM 24,176,236
DM 219,12,84,3072
MM 24,180,232
s_waitcnt lgkmcnt(0)
MM 28,188,232
s_waitcnt vmcnt(14) lgkmcnt(0)
s_barrier
MM 28,184,236
RT 176,200,16384
RT 178,200,17408
MM 60,184,192
RT 180,200,24576
RT 182,200,25600
MM 60,188,196
RB 228,208,0
RB 224,209,0
MM 92,188,200
RB 220,209,2048
RB 216,208,2048
MM 92,184,204
RB 232,208,4096
RB 236,209,4096
MM 124,184,208
RB 192,209,6144
RB 196,208,6144
MM 124,188,212
RB 200,208,8192
RB 204,209,8192
MM 156,188,252
RB 208,209,10240
RB 212,208,10240
MM 156,184,248
MM 188,184,244
RT 184,201,16384
RT 186,201,17408
MM 188,188,240
s_waitcnt lgkmcnt(10)
RT 188,201,24576
RT 190,201,25600
MM 0,176,228
s_bfe_u32 s83, s78, 0x4000c
s_mul_i32 s83, s83, 7
s_add_u32 s83, s83, 56
MM 0,180,224
s_and_b32 s83, s83, 15
s_lshl_b32 s83, s83, 7
MM 32,180,220
MM 32,176,216
s_waitcnt lgkmcnt(8)
MM 64,176,232
s_add_u32 m0, s85, 0
MM 64,180,236
MM 96,180,192
s_add_u32 s90, s59, 2048
s_and_b32 s90, s90, 39936
MM 96,176,196
s_waitcnt lgkmcnt(4)
MM 128,176,200
MM 128,180,204
s_barrier
MM 160,180,208
DM 212,8,83,0
MM 160,176,212
s_waitcnt lgkmcnt(0)
RT 180,202,16384
RT 182,202,17408
MM 164,184,212
RT 176,202,24576
RT 178,202,25600
MM 164,188,208
MM 132,188,204
buffer_load_dwordx4 a[112:115], v232, s[16:19], s90 offen offset:0 nt
MM 132,184,200
MM 100,184,196
MM 100,188,192
buffer_load_dwordx4 a[116:119], v233, s[16:19], s90 offen offset:1024 nt
MM 68,188,236
MM 68,184,232
MM 36,184,216
buffer_load_dwordx4 a[120:123], v234, s[16:19], s90 offen offset:2048 nt
MM 36,188,220
MM 4,188,224
RT 188,203,16384
RT 190,203,17408
MM 4,184,228
s_waitcnt lgkmcnt(2)
RT 184,203,24576
RT 186,203,25600
MM 8,180,228
buffer_load_dwordx4 a[124:127], v235, s[16:19], s90 offen offset:3072 nt
MM 8,176,224
s_add_u32 s90, s90, 65536
MM 40,176,220
buffer_load_dwordx4 a[128:131], v232, s[16:19], s90 offen offset:0 nt
MM 40,180,216
MM 72,180,232
MM 72,176,236
buffer_load_dwordx4 a[132:135], v233, s[16:19], s90 offen offset:1024 nt
MM 104,176,192
MM 104,180,196
MM 136,180,200
buffer_load_dwordx4 a[136:139], v234, s[16:19], s90 offen offset:2048 nt
MM 136,176,204
MM 168,176,208
RT 176,204,16384
RT 178,204,17408
MM 168,180,212
s_waitcnt lgkmcnt(2)
RT 180,204,24576
RT 182,204,25600
MM 172,188,212
buffer_load_dwordx4 a[140:143], v235, s[16:19], s90 offen offset:3072 nt
MM 172,184,208
MM 140,184,204
MM 140,188,200
MM 108,188,196
MM 108,184,192
MM 76,184,236
MM 76,188,232
MM 44,188,216
MM 44,184,220
MM 12,184,224
RT 184,205,16384
RT 186,205,17408
MM 12,188,228
s_waitcnt lgkmcnt(2)
RT 188,205,24576
RT 190,205,25600
MM 16,176,228
MM 16,180,224
MM 48,180,220
MM 48,176,216
MM 80,176,232
MM 80,180,236
MM 112,180,192
MM 112,176,196
MM 144,176,200
MM 144,180,204
MM 176,180,208
RT 180,206,16384
RT 182,206,17408
MM 176,176,212
s_waitcnt lgkmcnt(2)
RT 176,206,24576
RT 178,206,25600
MM 180,184,212
MM 180,188,208
MM 148,188,204
MM 148,184,200
MM 116,184,196
MM 116,188,192
MM 84,188,236
DM 213,8,83,1024
MM 84,184,232
MM 52,184,216
MM 52,188,220
MM 20,188,224
DM 214,8,83,2048
MM 20,184,228
s_waitcnt lgkmcnt(0)
s_add_u32 m0, s85, 12288
s_bfe_u32 s83, s78, 0x4000c
MM 24,180,228
s_mul_i32 s83, s83, 7
s_add_u32 s83, s83, 56
s_and_b32 s83, s83, 15
MM 24,176,224
s_lshl_b32 s83, s83, 7
s_add_u32 s83, s83, 196608
RT 188,207,16384
MM 56,176,220
DM 212,8,83,0
MM 56,180,216
RT 190,207,17408
RT 184,207,24576
MM 88,180,232
RT 186,207,25600
MM 88,176,236
MM 120,176,192
DM 213,8,83,1024
MM 120,180,196
s_bfe_u32 s84, s78, 0x4000c
s_mul_i32 s84, s84, 7
s_add_u32 s84, s84, 56
MM 152,180,200
s_and_b32 s84, s84, 15
s_lshl_b32 s84, s84, 19
s_add_u32 s84, s84, s87
MM 152,176,204
MM 184,176,208
DM 214,8,83,2048
MM 184,180,212
s_waitcnt lgkmcnt(0)
s_add_u32 m0, s86, 0
MM 188,188,212
s_waitcnt vmcnt(14) lgkmcnt(0)
s_barrier
MM 188,184,208
RT 176,200,32768
RT 178,200,33792
MM 156,184,204
RT 180,200,40960
RT 182,200,41984
MM 156,188,200
RB 252,208,34816
RB 248,209,34816
MM 124,188,196
RB 244,209,32768
RB 240,208,32768
MM 124,184,192
DM 216,12,84,0
MM 92,184,236
RB 212,208,30720
RB 208,209,30720
MM 92,188,232
RB 204,209,28672
RB 200,208,28672
MM 60,188,216
RB 196,208,26624
RB 192,209,26624
MM 60,184,220
DM 217,12,84,1024
MM 28,184,224
RB 236,209,24576
RB 232,208,24576
MM 28,188,228
s_waitcnt lgkmcnt(8)
RT 184,201,32768
RT 186,201,33792
MM 160,176,252
RT 188,201,40960
RT 190,201,41984
MM 160,180,248
DM 218,12,84,2048
MM 128,180,244
MM 128,176,240
s_waitcnt lgkmcnt(8)
MM 96,176,212
MM 96,180,208
DM 219,12,84,3072
MM 64,180,204
s_add_u32 m0, s86, 8192
s_bfe_u32 s84, s78, 0x4000c
s_mul_i32 s84, s84, 7
MM 64,176,200
s_waitcnt lgkmcnt(4)
s_add_u32 s84, s84, 56
s_and_b32 s84, s84, 15
MM 32,176,196
s_lshl_b32 s84, s84, 19
s_add_u32 s84, s84, s88
MM 32,180,192
s_barrier
MM 0,180,236
DM 216,12,84,0
MM 0,176,232
s_waitcnt lgkmcnt(0)
RT 180,202,32768
RT 182,202,33792
MM 4,184,232
RT 176,202,40960
RT 178,202,41984
MM 4,188,236
MM 36,188,192
DM 217,12,84,1024
MM 36,184,196
MM 68,184,200
MM 68,188,204
s_bfe_u32 s83, s78, 0x40010
s_mul_i32 s83, s83, 7
s_add_u32 s83, s83, 84
MM 100,188,208
DM 218,12,84,2048
MM 100,184,212
s_and_b32 s83, s83, 15
s_lshl_b32 s83, s83, 7
MM 132,184,240
MM 132,188,244
MM 164,188,248
DM 219,12,84,3072
MM 164,184,252
s_waitcnt lgkmcnt(0)
s_add_u32 m0, s85, 24576
RT 188,203,32768
MM 168,180,252
RT 190,203,33792
RT 184,203,40960
MM 168,176,248
RT 186,203,41984
MM 136,176,244
MM 136,180,240
DM 212,8,83,0
MM 104,180,212
MM 104,176,208
MM 72,176,204
MM 72,180,200
MM 40,180,196
DM 213,8,83,1024
MM 40,176,192
MM 8,176,236
RT 176,204,32768
RT 178,204,33792
MM 8,180,232
s_waitcnt lgkmcnt(2)
RT 180,204,40960
RT 182,204,41984
MM 12,188,232
DM 214,8,83,2048
MM 12,184,236
s_add_u32 m0, s85, 36864
s_bfe_u32 s83, s78, 0x40010
s_mul_i32 s83, s83, 7
MM 44,184,192
s_add_u32 s83, s83, 84
s_and_b32 s83, s83, 15
s_lshl_b32 s83, s83, 7
MM 44,188,196
s_add_u32 s83, s83, 196608
MM 76,188,200
DM 212,8,83,0
MM 76,184,204
MM 108,184,208
s_bfe_u32 s84, s78, 0x40010
s_mul_i32 s84, s84, 7
s_add_u32 s84, s84, 84
MM 108,188,212
s_and_b32 s84, s84, 15
s_lshl_b32 s84, s84, 19
s_add_u32 s84, s84, s87
MM 140,188,240
DM 213,8,83,1024
MM 140,184,244
MM 172,184,248
RT 184,205,32768
RT 186,205,33792
MM 172,188,252
s_waitcnt lgkmcnt(2)
RT 188,205,40960
RT 190,205,41984
MM 176,176,252
DM 214,8,83,2048
MM 176,180,248
s_add_u32 m0, s86, 16384
MM 144,180,244
MM 144,176,240
MM 112,176,212
DM 216,12,84,0
MM 112,180,208
MM 80,180,204
MM 80,176,200
MM 48,176,196
DM 217,12,84,1024
MM 48,180,192
MM 16,180,236
RT 180,206,32768
RT 182,206,33792
MM 16,176,232
s_waitcnt lgkmcnt(2)
RT 176,206,40960
RT 178,206,41984
MM 20,184,232
DM 218,12,84,2048
MM 20,188,236
MM 52,188,192
MM 52,184,196
MM 84,184,200
DM 219,12,84,3072
MM 84,188,204
s_add_u32 m0, s86, 24576
s_bfe_u32 s84, s78, 0x40010
s_mul_i32 s84, s84, 7
MM 116,188,208
s_add_u32 s84, s84, 84
s_and_b32 s84, s84, 15
s_lshl_b32 s84, s84, 19
MM 116,184,212
s_add_u32 s84, s84, s88
MM 148,184,240
DM 216,12,84,0
MM 148,188,244
s_add_u32 s90, s59, 2048
s_and_b32 s90, s90, 39936
MM 180,188,248
RT 188,207,32768
RT 190,207,33792
MM 180,184,252
s_waitcnt lgkmcnt(2)
RT 184,207,40960
RT 186,207,41984
MM 184,180,252
DM 217,12,84,1024
MM 184,176,248
MM 152,176,244
MM 152,180,240
MM 120,180,212
buffer_load_dwordx4 a[144:147], v236, s[20:23], s90 offen offset:0 nt
MM 120,176,208
MM 88,176,204
MM 88,180,200
buffer_load_dwordx4 a[148:151], v199, s[20:23], s90 offen offset:1024 nt
MM 56,180,196
MM 56,176,192
MM 24,176,236
buffer_load_dwordx4 a[152:155], v238, s[20:23], s90 offen offset:2048 nt
MM 24,180,232
s_waitcnt lgkmcnt(0)
MM 28,188,232
s_waitcnt vmcnt(15) lgkmcnt(0)
s_barrier
MM 28,184,236
RT 176,200,0
RT 178,200,1024
MM 60,184,192
RT 180,200,8192
RT 182,200,9216
MM 60,188,196
RB 216,208,0
RB 220,209,0
MM 92,188,200
RB 224,209,2048
RB 228,208,2048
MM 92,184,204
buffer_load_dwordx4 a[156:159], v239, s[20:23], s90 offen offset:3072 nt
s_add_u32 s90, s90, 65536
MM 124,184,208
buffer_load_dwordx4 a[160:163], v236, s[20:23], s90 offen offset:0 nt
MM 124,188,212
buffer_load_dwordx4 a[164:167], v199, s[20:23], s90 offen offset:1024 nt
MM 156,188,240
RB 232,208,4096
)ASM"
R"ASM(RB 236,209,4096
MM 156,184,244
RB 192,209,6144
RB 196,208,6144
MM 188,184,248
buffer_load_dwordx4 a[168:171], v238, s[20:23], s90 offen offset:2048 nt
MM 188,188,252
s_waitcnt lgkmcnt(4)
RB 200,208,8192
RB 204,209,8192
MM 0,176,216
RB 208,209,10240
RB 212,208,10240
MM 0,180,220
buffer_load_dwordx4 a[172:175], v239, s[20:23], s90 offen offset:3072 nt
MM 32,180,224
RT 184,201,0
RT 186,201,1024
MM 32,176,228
s_waitcnt lgkmcnt(6)
RT 188,201,8192
RT 190,201,9216
MM 64,176,232
MM 64,180,236
MM 96,180,192
MM 96,176,196
s_waitcnt lgkmcnt(4)
MM 128,176,200
MM 128,180,204
s_barrier
MM 160,180,208
RT 180,202,0
RT 182,202,1024
MM 160,176,212
s_waitcnt lgkmcnt(2)
RT 176,202,8192
RT 178,202,9216
MM 164,184,212
MM 164,188,208
MM 132,188,204
MM 132,184,200
MM 100,184,196
MM 100,188,192
MM 68,188,236
MM 68,184,232
MM 36,184,228
MM 36,188,224
MM 4,188,220
RT 188,203,0
RT 190,203,1024
MM 4,184,216
s_waitcnt lgkmcnt(2)
RT 184,203,8192
RT 186,203,9216
MM 8,180,216
MM 8,176,220
MM 40,176,224
MM 40,180,228
MM 72,180,232
MM 72,176,236
MM 104,176,192
s_bfe_u32 s83, s78, 0x40014
s_mul_i32 s83, s83, 7
s_add_u32 s83, s83, 84
MM 104,180,196
s_and_b32 s83, s83, 15
s_lshl_b32 s83, s83, 7
MM 136,180,200
DM 218,12,84,2048
MM 136,176,204
MM 168,176,208
RT 176,204,0
RT 178,204,1024
MM 168,180,212
s_waitcnt lgkmcnt(2)
RT 180,204,8192
RT 182,204,9216
MM 172,188,212
DM 219,12,84,3072
MM 172,184,208
s_add_u32 m0, s85, 0
MM 140,184,204
MM 140,188,200
MM 108,188,196
DM 212,8,83,0
MM 108,184,192
MM 76,184,236
MM 76,188,232
MM 44,188,228
DM 213,8,83,1024
MM 44,184,224
MM 12,184,220
RT 184,205,0
RT 186,205,1024
MM 12,188,216
s_waitcnt lgkmcnt(2)
RT 188,205,8192
RT 190,205,9216
MM 16,176,216
DM 214,8,83,2048
MM 16,180,220
s_add_u32 m0, s85, 12288
s_bfe_u32 s83, s78, 0x40014
s_mul_i32 s83, s83, 7
MM 48,180,224
s_add_u32 s83, s83, 84
s_and_b32 s83, s83, 15
s_lshl_b32 s83, s83, 7
MM 48,176,228
s_add_u32 s83, s83, 196608
MM 80,176,232
DM 212,8,83,0
MM 80,180,236
MM 112,180,192
s_bfe_u32 s84, s78, 0x40014
s_mul_i32 s84, s84, 7
s_add_u32 s84, s84, 84
MM 112,176,196
s_and_b32 s84, s84, 15
s_lshl_b32 s84, s84, 19
s_add_u32 s84, s84, s87
MM 144,176,200
DM 213,8,83,1024
MM 144,180,204
MM 176,180,208
RT 180,206,0
RT 182,206,1024
MM 176,176,212
s_waitcnt lgkmcnt(2)
RT 176,206,8192
RT 178,206,9216
MM 180,184,212
DM 214,8,83,2048
MM 180,188,208
s_add_u32 m0, s86, 32768
MM 148,188,204
MM 148,184,200
MM 116,184,196
DM 216,12,84,0
MM 116,188,192
MM 84,188,236
MM 84,184,232
MM 52,184,228
DM 217,12,84,1024
MM 52,188,224
MM 20,188,220
RT 188,207,0
RT 190,207,1024
MM 20,184,216
s_waitcnt lgkmcnt(2)
RT 184,207,8192
RT 186,207,9216
MM 24,180,216
DM 218,12,84,2048
MM 24,176,220
MM 56,176,224
MM 56,180,228
MM 88,180,232
DM 219,12,84,3072
MM 88,176,236
s_add_u32 m0, s86, 40960
s_bfe_u32 s84, s78, 0x40014
s_mul_i32 s84, s84, 7
MM 120,176,192
s_add_u32 s84, s84, 84
s_and_b32 s84, s84, 15
s_lshl_b32 s84, s84, 19
MM 120,180,196
s_add_u32 s84, s84, s88
MM 152,180,200
DM 216,12,84,0
MM 152,176,204
MM 184,176,208
MM 184,180,212
s_waitcnt lgkmcnt(0)
MM 188,188,212
s_waitcnt vmcnt(11) lgkmcnt(0)
s_barrier
v_lshrrev_b32 v194, 3, v192
MM 188,184,208
RT 176,200,16384
RT 178,200,17408
MM 156,184,204
RT 180,200,24576
RT 182,200,25600
MM 156,188,200
RB 240,208,34816
RB 244,209,34816
MM 124,188,196
RB 248,209,32768
RB 252,208,32768
MM 124,184,192
DM 217,12,84,1024
MM 92,184,236
RB 212,208,30720
RB 208,209,30720
MM 92,188,232
RB 204,209,28672
RB 200,208,28672
MM 60,188,228
RB 196,208,26624
RB 192,209,26624
MM 60,184,224
DM 218,12,84,2048
MM 28,184,220
RB 236,209,24576
RB 232,208,24576
MM 28,188,216
s_waitcnt lgkmcnt(8)
RT 184,201,16384
RT 186,201,17408
MM 160,176,240
s_bfe_u32 s83, s78, 0x40018
RT 188,201,24576
RT 190,201,25600
MM 160,180,244
DM 219,12,84,3072
MM 128,180,248
s_mul_i32 s83, s83, 7
s_add_u32 s83, s83, 84
s_and_b32 s83, s83, 15
MM 128,176,252
s_waitcnt lgkmcnt(8)
s_lshl_b32 s83, s83, 7
v_add3_u32 v194, v194, s76, 8
MM 96,176,212
s_add_u32 m0, s85, 24576
v_bfe_u32 v195, v194, 0, 1
v_lshlrev_b32 v193, 10, v195
MM 96,180,208
v_bfe_u32 v195, v194, 1, 1
v_lshl_add_u32 v193, v195, 11, v193
MM 64,180,204
v_bfe_u32 v195, v194, 2, 1
v_lshl_add_u32 v193, v195, 12, v193
MM 64,176,200
s_waitcnt lgkmcnt(4)
v_bfe_u32 v195, v194, 3, 1
v_lshl_add_u32 v193, v195, 15, v193
MM 32,176,196
v_bfe_u32 v195, v192, 0, 1
v_lshl_add_u32 v193, v195, 13, v193
MM 32,180,192
s_barrier
v_bfe_u32 v195, v192, 1, 1
v_lshl_add_u32 v193, v195, 14, v193
MM 0,180,236
DM 212,8,83,0
MM 0,176,232
s_waitcnt lgkmcnt(0)
RT 180,202,16384
RT 182,202,17408
MM 4,184,232
RT 176,202,24576
RT 178,202,25600
MM 4,188,236
v_bfe_u32 v195, v192, 2, 1
v_lshl_add_u32 v193, v195, 16, v193
MM 36,188,192
v_add_u32 v194, v194, v192
v_and_b32 v195, 1, v194
MM 36,184,196
DM 213,8,83,1024
MM 68,184,200
v_lshl_add_u32 v193, v195, 7, v193
v_lshrrev_b32 v194, 1, v194
MM 68,188,204
v_bfe_u32 v195, v194, 0, 1
v_lshl_add_u32 v193, v195, 8, v193
MM 100,188,208
v_bfe_u32 v195, v194, 1, 1
v_lshl_add_u32 v193, v195, 9, v193
MM 100,184,212
MM 132,184,252
DM 214,8,83,2048
MM 132,188,248
s_add_u32 m0, s85, 36864
s_bfe_u32 s83, s78, 0x40018
s_mul_i32 s83, s83, 7
MM 164,188,244
s_add_u32 s83, s83, 84
s_and_b32 s83, s83, 15
s_lshl_b32 s83, s83, 7
MM 164,184,240
s_waitcnt lgkmcnt(0)
s_add_u32 s83, s83, 196608
RT 188,203,16384
MM 168,180,240
RT 190,203,17408
RT 184,203,24576
MM 168,176,244
DM 212,8,83,0
MM 136,176,248
RT 186,203,25600
MM 136,180,252
MM 104,180,212
buffer_load_dword v215, v193, s[16:19], 0 offen offset:0
MM 104,176,208
MM 72,176,204
MM 72,180,200
MM 40,180,196
MM 40,176,192
MM 8,176,236
RT 176,204,16384
RT 178,204,17408
MM 8,180,232
s_waitcnt lgkmcnt(2)
RT 180,204,24576
RT 182,204,25600
MM 12,188,232
MM 12,184,236
MM 44,184,192
MM 44,188,196
MM 76,188,200
MM 76,184,204
MM 108,184,208
MM 108,188,212
MM 140,188,252
MM 140,184,248
MM 172,184,244
RT 184,205,16384
RT 186,205,17408
MM 172,188,240
s_waitcnt lgkmcnt(2)
RT 188,205,24576
RT 190,205,25600
MM 176,176,240
MM 176,180,244
MM 144,180,248
MM 144,176,252
MM 112,176,212
MM 112,180,208
MM 80,180,204
MM 80,176,200
MM 48,176,196
MM 48,180,192
MM 16,180,236
RT 180,206,16384
RT 182,206,17408
MM 16,176,232
s_waitcnt lgkmcnt(2)
RT 176,206,24576
RT 178,206,25600
MM 20,184,232
MM 20,188,236
MM 52,188,192
MM 52,184,196
MM 84,184,200
MM 84,188,204
MM 116,188,208
MM 116,184,212
MM 148,184,252
MM 148,188,248
MM 180,188,244
RT 188,207,16384
RT 190,207,17408
MM 180,184,240
s_waitcnt lgkmcnt(2)
RT 184,207,24576
RT 186,207,25600
MM 184,180,240
MM 184,176,244
MM 152,176,248
MM 152,180,252
MM 120,180,212
MM 120,176,208
MM 88,176,204
MM 88,180,200
MM 56,180,196
DM 213,8,83,1024
MM 56,176,192
MM 24,176,236
s_getpc_b64 s[62:63]
L_w4:
s_add_u32 s62, s62, L_c1-L_w4
s_addc_u32 s63, s63, 0
MM 24,180,232
s_waitcnt lgkmcnt(0)
v_min_u32 v196, L_c2-L_c1, v211
s_bfe_u32 s84, s78, 0x40018
MM 28,188,232
s_waitcnt vmcnt(6) lgkmcnt(0)
s_barrier
s_mul_i32 s84, s84, 7
MM 28,184,236
s_add_u32 s84, s84, 84
RT 176,200,32768
RT 178,200,33792
MM 60,184,192
s_and_b32 s84, s84, 15
RT 180,200,40960
RT 182,200,41984
MM 60,188,196
s_lshl_b32 s84, s84, 19
RB 228,208,0
RB 224,209,0
MM 92,188,200
s_add_u32 s84, s84, s87
RB 220,209,2048
RB 216,208,2048
MM 92,184,204
DM 214,8,83,2048
MM 124,184,208
s_add_u32 m0, s86, 0
RB 232,208,4096
RB 236,209,4096
MM 124,188,212
s_mov_b64 exec, 3
global_load_dword v215, v196, s[62:63]
s_mov_b64 exec, -1
MM 156,188,252
RB 192,209,6144
RB 196,208,6144
MM 156,184,248
DM 216,12,84,0
MM 188,184,244
RB 200,208,8192
RB 204,209,8192
MM 188,188,240
s_waitcnt lgkmcnt(6)
RB 208,209,10240
RB 212,208,10240
MM 0,176,228
RT 184,201,32768
RT 186,201,33792
MM 0,180,224
DM 217,12,84,1024
MM 32,180,220
RT 188,201,40960
RT 190,201,41984
MM 32,176,216
s_waitcnt lgkmcnt(8)
MM 64,176,232
MM 64,180,236
DM 218,12,84,2048
MM 96,180,192
MM 96,176,196
s_waitcnt lgkmcnt(4)
MM 128,176,200
MM 128,180,204
DM 219,12,84,3072
MM 160,180,208
s_add_u32 m0, s86, 8192
s_bfe_u32 s84, s78, 0x40018
s_mul_i32 s84, s84, 7
MM 160,176,212
s_waitcnt lgkmcnt(0)
s_add_u32 s84, s84, 84
s_and_b32 s84, s84, 15
MM 164,184,212
s_lshl_b32 s84, s84, 19
s_add_u32 s84, s84, s88
RT 180,202,32768
MM 164,188,208
DM 216,12,84,0
MM 132,188,204
RT 182,202,33792
RT 176,202,40960
MM 132,184,200
RT 178,202,41984
MM 100,184,196
MM 100,188,192
DM 217,12,84,1024
MM 68,188,236
MM 68,184,232
MM 36,184,216
MM 36,188,220
DM 218,12,84,2048
MM 4,188,224
RT 188,203,32768
RT 190,203,33792
MM 4,184,228
s_waitcnt lgkmcnt(2)
RT 184,203,40960
RT 186,203,41984
MM 8,180,228
MM 8,176,224
DM 219,12,84,3072
MM 40,176,220
MM 40,180,216
MM 72,180,232
MM 72,176,236
MM 104,176,192
MM 104,180,196
MM 136,180,200
MM 136,176,204
MM 168,176,208
RT 176,204,32768
RT 178,204,33792
MM 168,180,212
s_waitcnt lgkmcnt(2)
RT 180,204,40960
RT 182,204,41984
MM 172,188,212
MM 172,184,208
MM 140,184,204
buffer_load_dword v215, v193, s[20:23], 0 offen offset:0
MM 140,188,200
MM 108,188,196
MM 108,184,192
MM 76,184,236
MM 76,188,232
MM 44,188,216
MM 44,184,220
MM 12,184,224
RT 184,205,32768
RT 186,205,33792
MM 12,188,228
s_waitcnt lgkmcnt(2)
RT 188,205,40960
RT 190,205,41984
MM 16,176,228
MM 16,180,224
MM 48,180,220
MM 48,176,216
MM 80,176,232
MM 80,180,236
MM 112,180,192
MM 112,176,196
MM 144,176,200
MM 144,180,204
MM 176,180,208
RT 180,206,32768
RT 182,206,33792
MM 176,176,212
s_waitcnt lgkmcnt(2)
RT 176,206,40960
RT 178,206,41984
MM 180,184,212
MM 180,188,208
MM 148,188,204
MM 148,184,200
MM 116,184,196
MM 116,188,192
MM 84,188,236
MM 84,184,232
MM 52,184,216
MM 52,188,220
MM 20,188,224
RT 188,207,32768
RT 190,207,33792
MM 20,184,228
s_waitcnt lgkmcnt(2)
RT 184,207,40960
RT 186,207,41984
MM 24,180,228
MM 24,176,224
MM 56,176,220
MM 56,180,216
MM 88,180,232
MM 88,176,236
MM 120,176,192
MM 120,180,196
MM 152,180,200
MM 152,176,204
MM 184,176,208
MM 184,180,212
s_waitcnt lgkmcnt(0)
MM 188,188,212
s_waitcnt vmcnt(1) lgkmcnt(0)
s_barrier
MM 188,184,208
RT 176,200,0
RT 178,200,1024
MM 156,184,204
RT 180,200,8192
RT 182,200,9216
MM 156,188,200
RB 252,208,34816
RB 248,209,34816
MM 124,188,196
RB 244,209,32768
RB 240,208,32768
MM 124,184,192
RB 212,208,30720
RB 208,209,30720
MM 92,184,236
RB 204,209,28672
RB 200,208,28672
MM 92,188,232
RB 196,208,26624
RB 192,209,26624
MM 60,188,216
RB 236,209,24576
RB 232,208,24576
MM 60,184,220
v_xor_b32 v193, 512, v193
MM 28,184,224
RT 184,201,0
RT 186,201,1024
MM 28,188,228
s_waitcnt lgkmcnt(10)
RT 188,201,8192
RT 190,201,9216
MM 160,176,252
RT 216,202,0
RT 218,202,1024
MM 160,180,248
RT 220,202,8192
RT 222,202,9216
MM 128,180,244
RT 224,203,0
RT 226,203,1024
MM 128,176,240
s_waitcnt lgkmcnt(14)
RT 228,203,8192
RT 230,203,9216
MM 96,176,212
MM 96,180,208
MM 64,180,204
MM 64,176,200
s_waitcnt lgkmcnt(12)
MM 32,176,196
MM 32,180,192
MM 0,180,236
RT 180,204,0
RT 182,204,1024
MM 0,176,232
s_waitcnt lgkmcnt(10)
RT 176,204,8192
RT 178,204,9216
MM 4,184,232
MM 4,188,236
MM 36,188,192
MM 36,184,196
MM 68,184,200
MM 68,188,204
MM 100,188,208
MM 100,184,212
MM 132,184,240
MM 132,188,244
MM 164,188,248
RT 188,205,0
RT 190,205,1024
MM 164,184,252
s_waitcnt lgkmcnt(10)
RT 184,205,8192
RT 186,205,9216
MM 168,216,252
MM 168,220,248
MM 136,220,244
MM 136,216,240
MM 104,216,212
MM 104,220,208
MM 72,220,204
MM 72,216,200
MM 40,216,196
MM 40,220,192
buffer_load_dword v215, v193, s[16:19], 0 offen offset:0
MM 8,220,236
RT 220,206,0
RT 222,206,1024
MM 8,216,232
s_waitcnt lgkmcnt(10)
RT 216,206,8192
RT 218,206,9216
MM 12,224,232
MM 12,228,236
MM 44,228,192
MM 44,224,196
MM 76,224,200
MM 76,228,204
MM 108,228,208
MM 108,224,212
MM 140,224,240
MM 140,228,244
MM 172,228,248
RT 228,207,0
RT 230,207,1024
MM 172,224,252
s_waitcnt lgkmcnt(10)
RT 224,207,8192
RT 226,207,9216
MM 176,180,252
MM 176,176,248
MM 144,176,244
MM 144,180,240
MM 112,180,212
MM 112,176,208
MM 80,176,204
MM 80,180,200
MM 48,180,196
MM 48,176,192
MM 16,176,236
MM 16,180,232
s_waitcnt lgkmcnt(8)
MM 20,188,232
MM 20,184,236
MM 52,184,192
s_waitcnt lgkmcnt(0)
s_barrier
MM 52,188,196
s_waitcnt vmcnt(63)
MM 84,188,200
ds_write_b128 v198, v[240:243] offset:0
MM 84,184,204
MM 116,184,208
MM 116,188,212
ds_write_b128 v198, v[244:247] offset:1024
MM 148,188,240
MM 148,184,244
MM 180,184,248
ds_write_b128 v198, v[248:251] offset:2048
MM 180,188,252
MM 184,220,252
MM 184,216,248
ds_write_b128 v198, v[252:255] offset:3072
MM 152,216,244
MM 152,220,240
MM 120,220,212
ds_write_b128 v198, a[0:3] offset:4096
MM 120,216,208
MM 88,216,204
MM 88,220,200
ds_write_b128 v198, a[4:7] offset:5120
MM 56,220,196
MM 56,216,192
MM 24,216,236
ds_write_b128 v198, a[8:11] offset:6144
MM 24,220,232
MM 28,228,232
MM 28,224,236
ds_write_b128 v198, a[12:15] offset:7168
MM 60,224,192
MM 60,228,196
MM 92,228,200
ds_write_b128 v198, a[16:19] offset:16384
MM 92,224,204
MM 124,224,208
MM 124,228,212
ds_write_b128 v198, a[20:23] offset:17408
MM 156,228,240
MM 156,224,244
MM 188,224,248
MM 188,228,252
L_q3:
L_c1: ; ---- q -> bf16 fragments; attention tiles 0..5
s_nop 7
v_pk_mul_f32 v[0:1], v[0:1], s[68:69]
v_pk_mul_f32 v[2:3], v[2:3], s[68:69]
v_pk_mul_f32 v[4:5], v[4:5], s[68:69]
v_pk_mul_f32 v[6:7], v[6:7], s[68:69]
s_add_u32 s90, s59, 3072
s_and_b32 s90, s90, 39936
s_add_u32 m0, s89, 8192
s_add_u32 s80, s90, 0
buffer_load_dwordx4 v232, s[16:19], s80 offen offset:0 nt lds
CV 0,0,1
CV 1,2,3
CV 2,4,5
CV 3,6,7
ds_write_b128 v198, a[24:27] offset:18432
v_pk_mul_f32 v[8:9], v[8:9], s[68:69]
v_pk_mul_f32 v[10:11], v[10:11], s[68:69]
v_pk_mul_f32 v[12:13], v[12:13], s[68:69]
v_pk_mul_f32 v[14:15], v[14:15], s[68:69]
CV 4,8,9
CV 5,10,11
CV 6,12,13
CV 7,14,15
v_permlane16_swap_b32 v0, v2
v_permlane16_swap_b32 v1, v3
ds_write_b128 v198, a[28:31] offset:19456
v_pk_mul_f32 v[16:17], v[16:17], s[68:69]
v_pk_mul_f32 v[18:19], v[18:19], s[68:69]
v_pk_mul_f32 v[20:21], v[20:21], s[68:69]
v_pk_mul_f32 v[22:23], v[22:23], s[68:69]
CV 8,16,17
CV 9,18,19
CV 10,20,21
CV 11,22,23
v_permlane16_swap_b32 v4, v6
v_permlane16_swap_b32 v5, v7
ds_write_b128 v198, a[32:35] offset:20480
v_pk_mul_f32 v[24:25], v[24:25], s[68:69]
v_pk_mul_f32 v[26:27], v[26:27], s[68:69]
v_pk_mul_f32 v[28:29], v[28:29], s[68:69]
v_pk_mul_f32 v[30:31], v[30:31], s[68:69]
buffer_load_dwordx4 v233, s[16:19], s80 offen offset:1024 nt lds
CV 12,24,25
CV 13,26,27
CV 14,28,29
CV 15,30,31
v_permlane16_swap_b32 v8, v10
v_permlane16_swap_b32 v9, v11
ds_write_b128 v198, a[36:39] offset:21504
v_pk_mul_f32 v[32:33], v[32:33], s[68:69]
v_pk_mul_f32 v[34:35], v[34:35], s[68:69]
v_pk_mul_f32 v[36:37], v[36:37], s[68:69]
v_pk_mul_f32 v[38:39], v[38:39], s[68:69]
CV 16,32,33
CV 17,34,35
CV 18,36,37
CV 19,38,39
v_permlane16_swap_b32 v12, v14
v_permlane16_swap_b32 v13, v15
ds_write_b128 v198, a[40:43] offset:22528
v_pk_mul_f32 v[40:41], v[40:41], s[68:69]
v_pk_mul_f32 v[42:43], v[42:43], s[68:69]
v_pk_mul_f32 v[44:45], v[44:45], s[68:69]
v_pk_mul_f32 v[46:47], v[46:47], s[68:69]
CV 20,40,41
CV 21,42,43
CV 22,44,45
CV 23,46,47
v_permlane16_swap_b32 v16, v18
v_permlane16_swap_b32 v17, v19
ds_write_b128 v198, a[44:47] offset:23552
v_pk_mul_f32 v[48:49], v[48:49], s[68:69]
v_pk_mul_f32 v[50:51], v[50:51], s[68:69]
v_pk_mul_f32 v[52:53], v[52:53], s[68:69]
v_pk_mul_f32 v[54:55], v[54:55], s[68:69]
buffer_load_dwordx4 v234, s[16:19], s80 offen offset:2048 nt lds
CV 24,48,49
CV 25,50,51
CV 26,52,53
CV 27,54,55
v_permlane16_swap_b32 v20, v22
v_permlane16_swap_b32 v21, v23
s_waitcnt vmcnt(63)
ds_write_b128 v198, a[48:51] offset:32768
v_pk_mul_f32 v[56:57], v[56:57], s[68:69]
v_pk_mul_f32 v[58:59], v[58:59], s[68:69]
v_pk_mul_f32 v[60:61], v[60:61], s[68:69]
v_pk_mul_f32 v[62:63], v[62:63], s[68:69]
CV 28,56,57
CV 29,58,59
CV 30,60,61
CV 31,62,63
v_permlane16_swap_b32 v24, v26
v_permlane16_swap_b32 v25, v27
ds_write_b128 v198, a[52:55] offset:33792
v_pk_mul_f32 v[64:65], v[64:65], s[68:69]
v_pk_mul_f32 v[66:67], v[66:67], s[68:69]
v_pk_mul_f32 v[68:69], v[68:69], s[68:69]
v_pk_mul_f32 v[70:71], v[70:71], s[68:69]
CV 32,64,65
CV 33,66,67
CV 34,68,69
CV 35,70,71
v_permlane16_swap_b32 v28, v30
v_permlane16_swap_b32 v29, v31
ds_write_b128 v198, a[56:59] offset:34816
v_pk_mul_f32 v[72:73], v[72:73], s[68:69]
v_pk_mul_f32 v[74:75], v[74:75], s[68:69]
v_pk_mul_f32 v[76:77], v[76:77], s[68:69]
v_pk_mul_f32 v[78:79], v[78:79], s[68:69]
buffer_load_dwordx4 v235, s[16:19], s80 offen offset:3072 nt lds
CV 36,72,73
CV 37,74,75
CV 38,76,77
CV 39,78,79
v_permlane16_swap_b32 v32, v34
v_permlane16_swap_b32 v33, v35
ds_write_b128 v198, a[60:63] offset:35840
v_pk_mul_f32 v[80:81], v[80:81], s[68:69]
v_pk_mul_f32 v[82:83], v[82:83], s[68:69]
v_pk_mul_f32 v[84:85], v[84:85], s[68:69]
v_pk_mul_f32 v[86:87], v[86:87], s[68:69]
CV 40,80,81
CV 41,82,83
CV 42,84,85
CV 43,86,87
v_permlane16_swap_b32 v36, v38
v_permlane16_swap_b32 v37, v39
ds_write_b128 v198, a[64:67] offset:36864
v_pk_mul_f32 v[88:89], v[88:89], s[68:69]
v_pk_mul_f32 v[90:91], v[90:91], s[68:69]
v_pk_mul_f32 v[92:93], v[92:93], s[68:69]
v_pk_mul_f32 v[94:95], v[94:95], s[68:69]
CV 44,88,89
CV 45,90,91
CV 46,92,93
CV 47,94,95
v_permlane16_swap_b32 v40, v42
v_permlane16_swap_b32 v41, v43
ds_write_b128 v198, a[68:71] offset:37888
v_pk_mul_f32 v[96:97], v[96:97], s[68:69]
v_pk_mul_f32 v[98:99], v[98:99], s[68:69]
v_pk_mul_f32 v[100:101], v[100:101], s[68:69]
v_pk_mul_f32 v[102:103], v[102:103], s[68:69]
s_add_u32 m0, s89, 12288
s_add_u32 s80, s90, 65536
buffer_load_dwordx4 v232, s[16:19], s80 offen offset:0 nt lds
CV 48,96,97
CV 49,98,99
CV 50,100,101
CV 51,102,103
v_permlane16_swap_b32 v44, v46
v_permlane16_swap_b32 v45, v47
ds_write_b128 v198, a[72:75] offset:38912
v_pk_mul_f32 v[104:105], v[104:105], s[68:69]
v_pk_mul_f32 v[106:107], v[106:107], s[68:69]
v_pk_mul_f32 v[108:109], v[108:109], s[68:69]
v_pk_mul_f32 v[110:111], v[110:111], s[68:69]
CV 52,104,105
CV 53,106,107
CV 54,108,109
CV 55,110,111
v_permlane16_swap_b32 v48, v50
v_permlane16_swap_b32 v49, v51
ds_write_b128 v198, a[76:79] offset:39936
v_pk_mul_f32 v[112:113], v[112:113], s[68:69]
v_pk_mul_f32 v[114:115], v[114:115], s[68:69]
v_pk_mul_f32 v[116:117], v[116:117], s[68:69]
v_pk_mul_f32 v[118:119], v[118:119], s[68:69]
CV 56,112,113
CV 57,114,115
CV 58,116,117
CV 59,118,119
v_permlane16_swap_b32 v52, v54
v_permlane16_swap_b32 v53, v55
ds_write_b128 v198, a[80:83] offset:24576
v_pk_mul_f32 v[120:121], v[120:121], s[68:69]
v_pk_mul_f32 v[122:123], v[122:123], s[68:69]
v_pk_mul_f32 v[124:125], v[124:125], s[68:69]
v_pk_mul_f32 v[126:127], v[126:127], s[68:69]
buffer_load_dwordx4 v233, s[16:19], s80 offen offset:1024 nt lds
CV 60,120,121
CV 61,122,123
CV 62,124,125
CV 63,126,127
v_permlane16_swap_b32 v56, v58
v_permlane16_swap_b32 v57, v59
ds_write_b128 v198, a[84:87] offset:25600
v_pk_mul_f32 v[128:129], v[128:129], s[68:69]
v_pk_mul_f32 v[130:131], v[130:131], s[68:69]
v_pk_mul_f32 v[132:133], v[132:133], s[68:69]
v_pk_mul_f32 v[134:135], v[134:135], s[68:69]
CV 64,128,129
CV 65,130,131
CV 66,132,133
CV 67,134,135
v_permlane16_swap_b32 v60, v62
v_permlane16_swap_b32 v61, v63
ds_write_b128 v198, a[88:91] offset:26624
v_pk_mul_f32 v[136:137], v[136:137], s[68:69]
v_pk_mul_f32 v[138:139], v[138:139], s[68:69]
v_pk_mul_f32 v[140:141], v[140:141], s[68:69]
v_pk_mul_f32 v[142:143], v[142:143], s[68:69]
CV 68,136,137
CV 69,138,139
CV 70,140,141
CV 71,142,143
v_permlane16_swap_b32 v64, v66
v_permlane16_swap_b32 v65, v67
ds_write_b128 v198, a[92:95] offset:27648
v_pk_mul_f32 v[144:145], v[144:145], s[68:69]
v_pk_mul_f32 v[146:147], v[146:147], s[68:69]
v_pk_mul_f32 v[148:149], v[148:149], s[68:69]
v_pk_mul_f32 v[150:151], v[150:151], s[68:69]
buffer_load_dwordx4 v234, s[16:19], s80 offen offset:2048 nt lds
CV 72,144,145
CV 73,146,147
CV 74,148,149
CV 75,150,151
v_permlane16_swap_b32 v68, v70
v_permlane16_swap_b32 v69, v71
ds_write_b128 v198, a[96:99] offset:28672
v_pk_mul_f32 v[152:153], v[152:153], s[68:69]
v_pk_mul_f32 v[154:155], v[154:155], s[68:69]
v_pk_mul_f32 v[156:157], v[156:157], s[68:69]
v_pk_mul_f32 v[158:159], v[158:159], s[68:69]
CV 76,152,153
CV 77,154,155
CV 78,156,157
CV 79,158,159
v_permlane16_swap_b32 v72, v74
v_permlane16_swap_b32 v73, v75
ds_write_b128 v198, a[100:103] offset:29696
v_pk_mul_f32 v[160:161], v[160:161], s[68:69]
v_pk_mul_f32 v[162:163], v[162:163], s[68:69]
v_pk_mul_f32 v[164:165], v[164:165], s[68:69]
v_pk_mul_f32 v[166:167], v[166:167], s[68:69]
CV 80,160,161
CV 81,162,163
CV 82,164,165
CV 83,166,167
v_permlane16_swap_b32 v76, v78
v_permlane16_swap_b32 v77, v79
ds_write_b128 v198, a[104:107] offset:30720
v_pk_mul_f32 v[168:169], v[168:169], s[68:69]
v_pk_mul_f32 v[170:171], v[170:171], s[68:69]
v_pk_mul_f32 v[172:173], v[172:173], s[68:69]
v_pk_mul_f32 v[174:175], v[174:175], s[68:69]
buffer_load_dwordx4 v235, s[16:19], s80 offen offset:3072 nt lds
CV 84,168,169
CV 85,170,171
CV 86,172,173
CV 87,174,175
v_permlane16_swap_b32 v80, v82
v_permlane16_swap_b32 v81, v83
ds_write_b128 v198, a[108:111] offset:31744
v_pk_mul_f32 v[176:177], v[176:177], s[68:69]
v_pk_mul_f32 v[178:179], v[178:179], s[68:69]
v_pk_mul_f32 v[180:181], v[180:181], s[68:69]
v_pk_mul_f32 v[182:183], v[182:183], s[68:69]
CV 88,176,177
CV 89,178,179
CV 90,180,181
)ASM"
R"ASM(CV 91,182,183
v_permlane16_swap_b32 v84, v86
v_permlane16_swap_b32 v85, v87
v_pk_mul_f32 v[184:185], v[184:185], s[68:69]
v_pk_mul_f32 v[186:187], v[186:187], s[68:69]
v_pk_mul_f32 v[188:189], v[188:189], s[68:69]
v_pk_mul_f32 v[190:191], v[190:191], s[68:69]
CV 92,184,185
CV 93,186,187
CV 94,188,189
CV 95,190,191
v_permlane16_swap_b32 v88, v90
v_permlane16_swap_b32 v89, v91
s_nop 1
v_permlane16_swap_b32 v92, v94
v_permlane16_swap_b32 v93, v95
s_waitcnt lgkmcnt(0)
RB 192,220,0
RB 196,220,4096
RB 200,221,0
RB 204,221,4096
RB 208,222,0
RB 212,222,4096
RB 216,223,0
RB 220,223,4096
s_lshl_b32 s95, s55, 15
v_lshrrev_b32 v194, 4, v192
v_lshlrev_b32 v195, 1, v194
v_and_b32 v193, 15, v192
v_xor_b32 v193, v193, v195
v_lshlrev_b32 v193, 4, v193
v_lshl_add_u32 v193, v194, 11, v193
s_add_u32 s81, s95, 0
v_add_u32 v216, s81, v193
v_xor_b32 v193, 128, v193
s_add_u32 s81, s95, 7168
v_add_u32 v217, s81, v193
v_xor_b32 v193, 128, v193
s_add_u32 s81, s95, 14336
v_add_u32 v218, s81, v193
v_xor_b32 v193, 128, v193
s_add_u32 s81, s95, 21504
v_add_u32 v219, s81, v193
s_lshl_b32 s95, s70, 14
v_lshrrev_b32 v194, 4, v192
v_lshlrev_b32 v195, 1, v194
v_and_b32 v193, 15, v192
v_xor_b32 v193, v193, v195
v_lshlrev_b32 v193, 4, v193
v_lshl_add_u32 v193, v194, 11, v193
s_add_u32 s81, s95, 0
v_add_u32 v213, s81, v193
v_xor_b32 v193, 128, v193
s_add_u32 s81, s95, 7168
v_add_u32 v214, s81, v193
s_waitcnt lgkmcnt(0)
v_mfma_f32_16x16x32_bf16 v[144:147], a[192:195], v[0:3], 0
s_waitcnt vmcnt(63)
ds_write_b128 v198, a[112:115] offset:0
MS 144,200,4
MS 144,208,8
ds_write_b128 v198, a[116:119] offset:1024
MS 144,216,12
v_mfma_f32_16x16x32_bf16 v[152:155], a[216:219], v[28:31], 0
ds_write_b128 v198, a[120:123] offset:2048
MS 152,208,24
MS 152,200,20
ds_write_b128 v198, a[124:127] offset:3072
MS 152,192,16
v_mfma_f32_16x16x32_bf16 v[156:159], a[196:199], v[16:19], 0
s_add_u32 s90, s59, 3072
ds_write_b128 v198, a[128:131] offset:4096
MS 156,204,20
s_and_b32 s90, s90, 39936
MS 156,212,24
buffer_load_dwordx4 v[120:123], v236, s[20:23], s90 offen offset:0 nt
MS 156,220,28
ds_write_b128 v198, a[132:135] offset:5120
v_mfma_f32_16x16x32_bf16 v[148:151], a[220:223], v[12:15], 0
MS 148,212,8
ds_write_b128 v198, a[136:139] offset:6144
MS 148,204,4
MS 148,196,0
ds_write_b128 v198, a[140:143] offset:7168
s_nop 7
v_max3_f32 v246, v144, v145, v146
v_max3_f32 v246, v246, v147, v148
v_max3_f32 v246, v246, v149, v150
v_max_f32 v246, v246, v151
v_mov_b32 v247, v246
s_nop 1
v_permlane16_swap_b32 v246, v247
v_max_f32 v246, v246, v247
RT 224,224,16384
v_mov_b32 v247, v246
s_nop 1
v_permlane32_swap_b32 v246, v247
RT 226,224,20480
v_max_f32 v246, v246, v247
v_sub_f32 v144, v144, v246
v_sub_f32 v145, v145, v246
RT 228,225,16384
v_sub_f32 v146, v146, v246
v_sub_f32 v147, v147, v246
v_sub_f32 v148, v148, v246
v_sub_f32 v149, v149, v246
v_sub_f32 v150, v150, v246
v_sub_f32 v151, v151, v246
v_sub_f32 v96, 0, v246
v_sub_f32 v97, 0, v246
buffer_load_dwordx4 v[124:127], v199, s[20:23], s90 offen offset:1024 nt
v_sub_f32 v98, 0, v246
v_sub_f32 v99, 0, v246
v_max3_f32 v246, v152, v153, v154
RT 230,225,20480
v_max3_f32 v246, v246, v155, v156
v_max3_f32 v246, v246, v157, v158
v_max_f32 v246, v246, v159
v_mov_b32 v247, v246
RT 232,226,16384
s_nop 1
v_permlane16_swap_b32 v246, v247
RT 234,226,20480
v_max_f32 v246, v246, v247
v_mov_b32 v247, v246
s_nop 1
RT 236,227,16384
v_permlane32_swap_b32 v246, v247
v_max_f32 v246, v246, v247
v_sub_f32 v152, v152, v246
RT 238,227,20480
v_sub_f32 v153, v153, v246
v_sub_f32 v154, v154, v246
v_sub_f32 v155, v155, v246
v_sub_f32 v156, v156, v246
v_sub_f32 v157, v157, v246
v_sub_f32 v158, v158, v246
v_sub_f32 v159, v159, v246
v_sub_f32 v100, 0, v246
RT 240,228,16384
v_sub_f32 v101, 0, v246
v_sub_f32 v102, 0, v246
v_sub_f32 v103, 0, v246
RT 242,228,20480
EX 144
EX 145
RT 244,229,16384
EX 146
EX 147
v_mfma_f32_16x16x32_bf16 v[160:163], a[192:195], v[32:35], 0
CV 176,144,145
EX 148
MS 160,200,36
RT 246,229,20480
EX 149
MS 160,208,40
buffer_load_dwordx4 v[128:131], v238, s[20:23], s90 offen offset:2048 nt
MS 160,216,44
RT 248,230,16384
CV 177,146,147
v_mfma_f32_16x16x32_bf16 v[168:171], a[216:219], v[60:63], 0
RT 250,230,20480
EX 150
MS 168,208,56
RT 252,231,16384
EX 151
MS 168,200,52
RT 254,231,20480
CV 178,148,149
MS 168,192,48
EX 152
v_mfma_f32_16x16x32_bf16 v[172:175], a[196:199], v[48:51], 0
EX 153
CV 179,150,151
MS 172,204,52
EX 154
MS 172,212,56
EX 155
CV 180,152,153
MS 172,220,60
EX 156
v_mfma_f32_16x16x32_bf16 v[164:167], a[220:223], v[44:47], 0
EX 157
CV 181,154,155
MS 164,212,40
buffer_load_dwordx4 v[132:135], v239, s[20:23], s90 offen offset:3072 nt
MS 164,204,36
EX 158
EX 159
MS 164,196,32
s_waitcnt lgkmcnt(0)
CV 182,156,157
CV 183,158,159
v_mfma_f32_16x16x32_bf16 a[0:3], a[224:227], v[176:179], 0
v_add_f32 v240, v144, v145
v_add_f32 v241, v152, v153
v_add_f32 v240, v240, v146
v_add_f32 v241, v241, v154
v_add_f32 v240, v240, v147
v_mfma_f32_16x16x32_bf16 a[32:35], a[224:227], v[180:183], 0
v_add_f32 v241, v241, v155
v_max3_f32 v246, v160, v161, v162
v_max3_f32 v246, v246, v163, v164
v_max3_f32 v246, v246, v165, v166
v_max_f32 v246, v246, v167
v_mfma_f32_16x16x32_bf16 a[36:39], a[228:231], v[180:183], 0
v_mov_b32 v247, v246
v_add_f32 v240, v240, v148
s_nop 1
v_permlane16_swap_b32 v246, v247
v_mfma_f32_16x16x32_bf16 a[4:7], a[228:231], v[176:179], 0
v_max_f32 v246, v246, v247
v_mov_b32 v247, v246
s_nop 1
v_permlane32_swap_b32 v246, v247
v_mfma_f32_16x16x32_bf16 a[8:11], a[232:235], v[176:179], 0
v_max_f32 v246, v246, v247
v_sub_f32 v160, v160, v246
v_sub_f32 v161, v161, v246
v_sub_f32 v162, v162, v246
v_mfma_f32_16x16x32_bf16 a[40:43], a[232:235], v[180:183], 0
v_sub_f32 v163, v163, v246
v_sub_f32 v164, v164, v246
v_sub_f32 v165, v165, v246
v_sub_f32 v166, v166, v246
v_sub_f32 v167, v167, v246
v_mfma_f32_16x16x32_bf16 a[44:47], a[236:239], v[180:183], 0
v_sub_f32 v104, 0, v246
v_sub_f32 v105, 0, v246
v_sub_f32 v106, 0, v246
v_sub_f32 v107, 0, v246
v_max3_f32 v246, v168, v169, v170
v_mfma_f32_16x16x32_bf16 a[12:15], a[236:239], v[176:179], 0
v_max3_f32 v246, v246, v171, v172
v_max3_f32 v246, v246, v173, v174
v_max_f32 v246, v246, v175
v_mov_b32 v247, v246
s_nop 1
v_mfma_f32_16x16x32_bf16 a[16:19], a[240:243], v[176:179], 0
v_permlane16_swap_b32 v246, v247
v_max_f32 v246, v246, v247
v_mov_b32 v247, v246
v_mfma_f32_16x16x32_bf16 a[48:51], a[240:243], v[180:183], 0
s_add_u32 s90, s90, 65536
v_add_f32 v241, v241, v156
v_add_f32 v240, v240, v149
s_nop 1
v_permlane32_swap_b32 v246, v247
v_max_f32 v246, v246, v247
v_mfma_f32_16x16x32_bf16 a[52:55], a[244:247], v[180:183], 0
buffer_load_dwordx4 v[136:139], v236, s[20:23], s90 offen offset:0 nt
v_sub_f32 v168, v168, v246
v_sub_f32 v169, v169, v246
v_sub_f32 v170, v170, v246
v_mfma_f32_16x16x32_bf16 a[20:23], a[244:247], v[176:179], 0
v_add_f32 v241, v241, v157
v_sub_f32 v171, v171, v246
v_sub_f32 v172, v172, v246
v_sub_f32 v173, v173, v246
v_sub_f32 v174, v174, v246
v_mfma_f32_16x16x32_bf16 a[24:27], a[248:251], v[176:179], 0
s_waitcnt vmcnt(47)
ds_write_b128 v198, a[144:147] offset:16384
v_add_f32 v240, v240, v150
v_sub_f32 v175, v175, v246
v_sub_f32 v108, 0, v246
v_mfma_f32_16x16x32_bf16 a[56:59], a[248:251], v[180:183], 0
v_add_f32 v241, v241, v158
v_sub_f32 v109, 0, v246
v_sub_f32 v110, 0, v246
v_sub_f32 v111, 0, v246
EX 160
v_mfma_f32_16x16x32_bf16 a[60:63], a[252:255], v[180:183], 0
ds_write_b128 v198, a[148:151] offset:17408
v_add_f32 v240, v240, v151
EX 161
v_mfma_f32_16x16x32_bf16 a[28:31], a[252:255], v[176:179], 0
v_add_f32 v241, v241, v159
EX 162
EX 163
v_mfma_f32_16x16x32_bf16 v[144:147], a[192:195], v[64:67], 0
ds_write_b128 v198, a[152:155] offset:18432
CV 184,160,161
MS 144,200,68
EX 164
EX 165
MS 144,208,72
ds_write_b128 v198, a[156:159] offset:19456
CV 185,162,163
MS 144,216,76
EX 166
v_mfma_f32_16x16x32_bf16 v[152:155], a[216:219], v[92:95], 0
s_barrier
RB 216,223,32768
EX 167
MS 152,208,88
CV 186,164,165
EX 168
MS 152,200,84
RB 208,222,32768
EX 169
MS 152,192,80
buffer_load_dwordx4 v[140:143], v199, s[20:23], s90 offen offset:1024 nt
v_mfma_f32_16x16x32_bf16 v[156:159], a[196:199], v[80:83], 0
RB 200,221,32768
CV 187,166,167
MS 156,204,84
EX 170
EX 171
MS 156,212,88
RB 192,220,32768
CV 188,168,169
MS 156,220,92
EX 172
v_mfma_f32_16x16x32_bf16 v[148:151], a[220:223], v[76:79], 0
RB 220,223,36864
EX 173
MS 148,212,72
CV 189,170,171
EX 174
MS 148,204,68
RB 212,222,36864
EX 175
MS 148,196,64
CV 190,172,173
CV 191,174,175
v_mfma_f32_16x16x32_bf16 a[64:67], a[224:227], v[184:187], 0
RB 204,221,36864
v_add_f32 v242, v160, v161
v_add_f32 v243, v168, v169
v_add_f32 v242, v242, v162
v_add_f32 v243, v243, v170
v_mfma_f32_16x16x32_bf16 a[96:99], a[224:227], v[188:191], 0
buffer_load_dwordx4 v[248:251], v238, s[20:23], s90 offen offset:2048 nt
v_max3_f32 v246, v144, v145, v146
v_max3_f32 v246, v246, v147, v148
v_max3_f32 v246, v246, v149, v150
v_max_f32 v246, v246, v151
v_mfma_f32_16x16x32_bf16 a[100:103], a[228:231], v[188:191], 0
RB 196,220,36864
v_mov_b32 v247, v246
s_nop 1
v_permlane16_swap_b32 v246, v247
v_mfma_f32_16x16x32_bf16 a[68:71], a[228:231], v[184:187], 0
v_max_f32 v246, v246, v247
v_mov_b32 v247, v246
s_nop 1
v_permlane32_swap_b32 v246, v247
v_mfma_f32_16x16x32_bf16 a[72:75], a[232:235], v[184:187], 0
ds_write_b128 v198, a[160:163] offset:20480
v_max_f32 v246, v246, v247
v_sub_f32 v144, v144, v246
v_sub_f32 v145, v145, v246
v_sub_f32 v146, v146, v246
v_mfma_f32_16x16x32_bf16 a[104:107], a[232:235], v[188:191], 0
v_sub_f32 v147, v147, v246
v_sub_f32 v148, v148, v246
v_sub_f32 v149, v149, v246
v_sub_f32 v150, v150, v246
v_sub_f32 v151, v151, v246
v_sub_f32 v112, 0, v246
v_mfma_f32_16x16x32_bf16 a[108:111], a[236:239], v[188:191], 0
ds_write_b128 v198, a[164:167] offset:21504
v_add_f32 v242, v242, v163
v_sub_f32 v113, 0, v246
v_sub_f32 v114, 0, v246
v_sub_f32 v115, 0, v246
v_mfma_f32_16x16x32_bf16 a[76:79], a[236:239], v[184:187], 0
v_add_f32 v243, v243, v171
v_max3_f32 v246, v152, v153, v154
v_max3_f32 v246, v246, v155, v156
v_max3_f32 v246, v246, v157, v158
v_max_f32 v246, v246, v159
v_mfma_f32_16x16x32_bf16 a[80:83], a[240:243], v[184:187], 0
ds_write_b128 v198, a[168:171] offset:22528
v_add_f32 v242, v242, v164
v_mov_b32 v247, v246
s_nop 1
v_mfma_f32_16x16x32_bf16 a[112:115], a[240:243], v[188:191], 0
v_add_f32 v243, v243, v172
v_permlane16_swap_b32 v246, v247
v_max_f32 v246, v246, v247
v_mov_b32 v247, v246
s_nop 1
v_mfma_f32_16x16x32_bf16 a[116:119], a[244:247], v[188:191], 0
ds_write_b128 v198, a[172:175] offset:23552
v_add_f32 v242, v242, v165
v_permlane32_swap_b32 v246, v247
v_max_f32 v246, v246, v247
v_sub_f32 v152, v152, v246
v_mfma_f32_16x16x32_bf16 a[84:87], a[244:247], v[184:187], 0
v_add_f32 v243, v243, v173
v_sub_f32 v153, v153, v246
v_sub_f32 v154, v154, v246
v_sub_f32 v155, v155, v246
v_sub_f32 v156, v156, v246
v_mfma_f32_16x16x32_bf16 a[88:91], a[248:251], v[184:187], 0
buffer_load_dwordx4 v[252:255], v239, s[20:23], s90 offen offset:3072 nt
v_add_f32 v242, v242, v166
v_sub_f32 v157, v157, v246
v_sub_f32 v158, v158, v246
v_mfma_f32_16x16x32_bf16 a[120:123], a[248:251], v[188:191], 0
v_add_f32 v243, v243, v174
v_sub_f32 v159, v159, v246
v_sub_f32 v116, 0, v246
v_sub_f32 v117, 0, v246
v_sub_f32 v118, 0, v246
v_sub_f32 v119, 0, v246
v_mfma_f32_16x16x32_bf16 a[124:127], a[252:255], v[188:191], 0
v_add_f32 v242, v242, v167
v_add_f32 v243, v243, v175
EX 144
v_mfma_f32_16x16x32_bf16 a[92:95], a[252:255], v[184:187], 0
s_waitcnt lgkmcnt(0)
EX 145
EX 146
EX 147
v_mfma_f32_16x16x32_bf16 v[160:163], a[192:195], v[0:3], v[96:99]
CV 176,144,145
EX 148
MS 160,200,4
EX 149
MS 160,208,8
s_add_u32 s90, s59, 4096
s_and_b32 s90, s90, 39936
CV 177,146,147
EX 150
MS 160,216,12
s_add_u32 m0, s89, 32768
s_add_u32 s80, s90, 0
EX 151
v_mfma_f32_16x16x32_bf16 v[168:171], a[216:219], v[28:31], v[100:103]
s_waitcnt vmcnt(4)
buffer_load_dwordx4 v232, s[16:19], s80 offen offset:0 nt lds
MS 168,208,24
CV 178,148,149
EX 152
MS 168,200,20
EX 153
MS 168,192,16
CV 179,150,151
EX 154
v_mfma_f32_16x16x32_bf16 v[172:175], a[196:199], v[16:19], v[100:103]
EX 155
MS 172,204,20
CV 180,152,153
EX 156
MS 172,212,24
s_waitcnt vmcnt(4)
buffer_load_dwordx4 v233, s[16:19], s80 offen offset:1024 nt lds
MS 172,220,28
EX 157
v_mfma_f32_16x16x32_bf16 v[164:167], a[220:223], v[12:15], v[96:99]
CV 181,154,155
EX 158
MS 164,212,8
EX 159
MS 164,204,4
CV 182,156,157
CV 183,158,159
MS 164,196,0
v_mfma_f32_16x16x32_bf16 a[128:131], a[224:227], v[176:179], 0
v_add_f32 v244, v144, v145
v_add_f32 v245, v152, v153
v_add_f32 v244, v244, v146
v_mfma_f32_16x16x32_bf16 a[160:163], a[224:227], v[180:183], 0
s_waitcnt vmcnt(4)
buffer_load_dwordx4 v234, s[16:19], s80 offen offset:2048 nt lds
v_add_f32 v245, v245, v154
v_mfma_f32_16x16x32_bf16 a[164:167], a[228:231], v[180:183], 0
RT 224,224,24576
v_max3_f32 v246, v160, v161, v162
v_max3_f32 v246, v246, v163, v164
v_mfma_f32_16x16x32_bf16 a[132:135], a[228:231], v[176:179], 0
RT 226,224,28672
v_max3_f32 v246, v246, v165, v166
v_max3_f32 v246, v246, v167, v168
v_mfma_f32_16x16x32_bf16 a[136:139], a[232:235], v[176:179], 0
RT 228,225,24576
v_max3_f32 v246, v246, v169, v170
v_mfma_f32_16x16x32_bf16 a[168:171], a[232:235], v[180:183], 0
v_max3_f32 v246, v246, v171, v172
v_max3_f32 v246, v246, v173, v174
v_max_f32 v246, v246, v175
v_mfma_f32_16x16x32_bf16 a[172:175], a[236:239], v[180:183], 0
RT 230,225,28672
v_cmp_lt_f32 vcc, s91, v246
v_add_f32 v244, v244, v147
v_mfma_f32_16x16x32_bf16 a[140:143], a[236:239], v[176:179], 0
RT 232,226,24576
v_add_f32 v245, v245, v155
v_add_f32 v244, v244, v148
v_mfma_f32_16x16x32_bf16 a[144:147], a[240:243], v[176:179], 0
s_cbranch_vccnz L_tr1a
L_back1a:
RT 234,226,28672
v_add_f32 v245, v245, v156
v_mfma_f32_16x16x32_bf16 a[176:179], a[240:243], v[180:183], 0
EX 160
EX 161
v_mfma_f32_16x16x32_bf16 a[180:183], a[244:247], v[180:183], 0
s_waitcnt vmcnt(4)
buffer_load_dwordx4 v235, s[16:19], s80 offen offset:3072 nt lds
v_add_f32 v244, v244, v149
v_mfma_f32_16x16x32_bf16 a[148:151], a[244:247], v[176:179], 0
RT 236,227,24576
v_add_f32 v245, v245, v157
v_mfma_f32_16x16x32_bf16 a[152:155], a[248:251], v[176:179], 0
RT 238,227,28672
v_add_f32 v244, v244, v150
EX 162
v_mfma_f32_16x16x32_bf16 a[184:187], a[248:251], v[180:183], 0
RT 240,228,24576
v_add_f32 v245, v245, v158
v_mfma_f32_16x16x32_bf16 a[188:191], a[252:255], v[180:183], 0
s_add_u32 m0, s89, 36864
RT 242,228,28672
v_add_f32 v244, v244, v151
EX 163
v_mfma_f32_16x16x32_bf16 a[156:159], a[252:255], v[176:179], 0
s_add_u32 s80, s90, 65536
RT 244,229,24576
v_add_f32 v245, v245, v159
v_mfma_f32_16x16x32_bf16 v[144:147], a[192:195], v[32:35], v[104:107]
s_waitcnt vmcnt(4)
buffer_load_dwordx4 v232, s[16:19], s80 offen offset:0 nt lds
CV 184,160,161
MS 144,200,36
RT 246,229,28672
EX 164
MS 144,208,40
RT 248,230,24576
EX 165
MS 144,216,44
RT 250,230,28672
CV 185,162,163
v_mfma_f32_16x16x32_bf16 v[152:155], a[216:219], v[60:63], v[108:111]
EX 166
MS 152,208,56
RT 252,231,24576
EX 167
MS 152,200,52
RT 254,231,28672
CV 186,164,165
MS 152,192,48
EX 168
v_mfma_f32_16x16x32_bf16 v[156:159], a[196:199], v[48:51], v[108:111]
EX 169
CV 187,166,167
MS 156,204,52
EX 170
MS 156,212,56
s_waitcnt vmcnt(4)
buffer_load_dwordx4 v233, s[16:19], s80 offen offset:1024 nt lds
EX 171
MS 156,220,60
CV 188,168,169
EX 172
v_mfma_f32_16x16x32_bf16 v[148:151], a[220:223], v[44:47], v[104:107]
EX 173
MS 148,212,40
CV 189,170,171
EX 174
MS 148,204,36
EX 175
MS 148,196,32
s_waitcnt lgkmcnt(0)
CV 190,172,173
CV 191,174,175
MP 0,224,184
s_waitcnt vmcnt(6)
ds_write_b128 v198, v[120:123] offset:24576
v_add_f32 v240, v240, v160
v_add_f32 v241, v241, v168
MP 32,224,188
v_add_f32 v240, v240, v161
v_add_f32 v241, v241, v169
v_max3_f32 v246, v144, v145, v146
MP 36,228,188
s_waitcnt vmcnt(4)
buffer_load_dwordx4 v234, s[16:19], s80 offen offset:2048 nt lds
MP 4,228,184
ds_write_b128 v198, v[124:127] offset:25600
v_max3_f32 v246, v246, v147, v148
v_max3_f32 v246, v246, v149, v150
MP 8,232,184
v_max3_f32 v246, v246, v151, v152
v_max3_f32 v246, v246, v153, v154
v_max3_f32 v246, v246, v155, v156
MP 40,232,188
ds_write_b128 v198, v[128:131] offset:26624
v_max3_f32 v246, v246, v157, v158
v_max_f32 v246, v246, v159
MP 44,236,188
v_cmp_lt_f32 vcc, s91, v246
v_add_f32 v240, v240, v162
v_add_f32 v241, v241, v170
MP 12,236,184
ds_write_b128 v198, v[132:135] offset:27648
v_add_f32 v240, v240, v163
v_add_f32 v241, v241, v171
MP 16,240,184
s_cbranch_vccnz L_tr1b
L_back1b:
v_add_f32 v240, v240, v164
v_add_f32 v241, v241, v172
MP 48,240,188
ds_write_b128 v198, v[136:139] offset:28672
v_add_f32 v240, v240, v165
EX 144
MP 52,244,188
s_waitcnt vmcnt(4)
buffer_load_dwordx4 v235, s[16:19], s80 offen offset:3072 nt lds
MP 20,244,184
ds_write_b128 v198, v[140:143] offset:29696
v_add_f32 v241, v241, v173
MP 24,248,184
v_add_f32 v240, v240, v166
v_add_f32 v241, v241, v174
EX 145
MP 56,248,188
ds_write_b128 v198, v[248:251] offset:30720
v_add_f32 v240, v240, v167
MP 60,252,188
EX 146
EX 147
MP 28,252,184
ds_write_b128 v198, v[252:255] offset:31744
v_add_f32 v241, v241, v175
v_mfma_f32_16x16x32_bf16 v[160:163], a[192:195], v[64:67], v[112:115]
CV 176,144,145
EX 148
MS 160,200,68
s_add_u32 s90, s59, 4096
EX 149
MS 160,208,72
s_and_b32 s90, s90, 39936
s_waitcnt lgkmcnt(0)
CV 177,146,147
EX 150
MS 160,216,76
s_waitcnt vmcnt(4)
buffer_load_dwordx4 v[120:123], v236, s[20:23], s90 offen offset:0 nt
v_mfma_f32_16x16x32_bf16 v[168:171], a[216:219], v[92:95], v[116:119]
s_barrier
RB 216,223,0
EX 151
MS 168,208,88
CV 178,148,149
EX 152
MS 168,200,84
RB 208,222,0
EX 153
MS 168,192,80
CV 179,150,151
EX 154
v_mfma_f32_16x16x32_bf16 v[172:175], a[196:199], v[80:83], v[116:119]
RB 200,221,0
EX 155
MS 172,204,84
CV 180,152,153
EX 156
MS 172,212,88
RB 192,220,0
EX 157
MS 172,220,92
s_waitcnt vmcnt(4)
buffer_load_dwordx4 v[124:127], v199, s[20:23], s90 offen offset:1024 nt
v_mfma_f32_16x16x32_bf16 v[164:167], a[220:223], v[76:79], v[112:115]
RB 220,223,4096
CV 181,154,155
MS 164,212,72
EX 158
MS 164,204,68
RB 212,222,4096
EX 159
MS 164,196,64
CV 182,156,157
CV 183,158,159
MP 64,224,176
RB 204,221,4096
v_add_f32 v242, v242, v144
MP 96,224,180
v_add_f32 v243, v243, v152
v_add_f32 v242, v242, v145
v_add_f32 v243, v243, v153
MP 100,228,180
RB 196,220,4096
v_max3_f32 v246, v160, v161, v162
MP 68,228,176
s_waitcnt vmcnt(4)
buffer_load_dwordx4 v[128:131], v238, s[20:23], s90 offen offset:2048 nt
v_max3_f32 v246, v246, v163, v164
MP 72,232,176
v_max3_f32 v246, v246, v165, v166
v_max3_f32 v246, v246, v167, v168
MP 104,232,180
v_max3_f32 v246, v246, v169, v170
v_max3_f32 v246, v246, v171, v172
v_max3_f32 v246, v246, v173, v174
MP 108,236,180
v_max_f32 v246, v246, v175
v_cmp_lt_f32 vcc, s91, v246
MP 76,236,176
v_add_f32 v242, v242, v146
v_add_f32 v243, v243, v154
v_add_f32 v242, v242, v147
MP 80,240,176
s_cbranch_vccnz L_tr1c
L_back1c:
v_add_f32 v243, v243, v155
v_add_f32 v242, v242, v148
MP 112,240,180
v_add_f32 v243, v243, v156
v_add_f32 v242, v242, v149
MP 116,244,180
s_waitcnt vmcnt(4)
buffer_load_dwordx4 v[132:135], v239, s[20:23], s90 offen offset:3072 nt
MP 84,244,176
v_add_f32 v243, v243, v157
v_add_f32 v242, v242, v150
EX 160
MP 88,248,176
EX 161
MP 120,248,180
v_add_f32 v243, v243, v158
v_add_f32 v242, v242, v151
MP 124,252,180
EX 162
MP 92,252,176
s_waitcnt lgkmcnt(0)
v_add_f32 v243, v243, v159
EX 163
v_mfma_f32_16x16x32_bf16 v[144:147], a[192:195], v[0:3], v[96:99]
CV 184,160,161
EX 164
MS 144,200,4
s_add_u32 s90, s90, 65536
EX 165
MS 144,208,8
s_waitcnt vmcnt(4)
buffer_load_dwordx4 v[136:139], v236, s[20:23], s90 offen offset:0 nt
MS 144,216,12
CV 185,162,163
EX 166
v_mfma_f32_16x16x32_bf16 v[152:155], a[216:219], v[28:31], v[100:103]
EX 167
MS 152,208,24
CV 186,164,165
EX 168
MS 152,200,20
EX 169
MS 152,192,16
CV 187,166,167
EX 170
v_mfma_f32_16x16x32_bf16 v[156:159], a[196:199], v[16:19], v[100:103]
EX 171
MS 156,204,20
CV 188,168,169
EX 172
MS 156,212,24
s_waitcnt vmcnt(4)
buffer_load_dwordx4 v[140:143], v199, s[20:23], s90 offen offset:1024 nt
MS 156,220,28
EX 173
v_mfma_f32_16x16x32_bf16 v[148:151], a[220:223], v[12:15], v[96:99]
CV 189,170,171
EX 174
MS 148,212,8
EX 175
MS 148,204,4
CV 190,172,173
CV 191,174,175
MS 148,196,0
MP 128,224,184
v_add_f32 v244, v244, v160
v_add_f32 v245, v245, v168
v_add_f32 v244, v244, v161
MP 160,224,188
s_waitcnt vmcnt(4)
buffer_load_dwordx4 v[248:251], v238, s[20:23], s90 offen offset:2048 nt
v_add_f32 v245, v245, v169
MP 164,228,188
RT 224,224,16384
v_max3_f32 v246, v144, v145, v146
v_max3_f32 v246, v246, v147, v148
MP 132,228,184
RT 226,224,20480
v_max3_f32 v246, v246, v149, v150
v_max3_f32 v246, v246, v151, v152
MP 136,232,184
RT 228,225,16384
v_max3_f32 v246, v246, v153, v154
v_max3_f32 v246, v246, v155, v156
MP 168,232,188
v_max3_f32 v246, v246, v157, v158
v_max_f32 v246, v246, v159
v_cmp_lt_f32 vcc, s91, v246
MP 172,236,188
RT 230,225,20480
v_add_f32 v244, v244, v162
v_add_f32 v245, v245, v170
MP 140,236,184
s_cbranch_vccnz L_tr2a
L_back2a:
RT 232,226,16384
v_add_f32 v244, v244, v163
v_add_f32 v245, v245, v171
MP 144,240,184
RT 234,226,20480
v_add_f32 v244, v244, v164
EX 144
MP 176,240,188
v_add_f32 v245, v245, v172
v_add_f32 v244, v244, v165
MP 180,244,188
s_waitcnt vmcnt(4)
buffer_load_dwordx4 v[252:255], v239, s[20:23], s90 offen offset:3072 nt
EX 145
MP 148,244,184
RT 236,227,16384
v_add_f32 v245, v245, v173
MP 152,248,184
RT 238,227,20480
v_add_f32 v244, v244, v166
EX 146
MP 184,248,188
RT 240,228,16384
v_add_f32 v245, v245, v174
MP 188,252,188
s_add_u32 s90, s59, 6144
s_and_b32 s90, s90, 39936
RT 242,228,20480
v_add_f32 v244, v244, v167
EX 147
MP 156,252,184
s_add_u32 m0, s89, 0
s_add_u32 s80, s90, 0
RT 244,229,16384
v_add_f32 v245, v245, v175
v_mfma_f32_16x16x32_bf16 v[160:163], a[192:195], v[32:35], v[104:107]
s_waitcnt vmcnt(4)
buffer_load_dwordx4 v232, s[16:19], s80 offen offset:0 nt lds
CV 176,144,145
MS 160,200,36
RT 246,229,20480
EX 148
MS 160,208,40
RT 248,230,16384
EX 149
MS 160,216,44
RT 250,230,20480
CV 177,146,147
v_mfma_f32_16x16x32_bf16 v[168:171], a[216:219], v[60:63], v[108:111]
EX 150
MS 168,208,56
RT 252,231,16384
EX 151
MS 168,200,52
RT 254,231,20480
CV 178,148,149
MS 168,192,48
EX 152
v_mfma_f32_16x16x32_bf16 v[172:175], a[196:199], v[48:51], v[108:111]
EX 153
CV 179,150,151
MS 172,204,52
EX 154
MS 172,212,56
s_waitcnt vmcnt(4)
buffer_load_dwordx4 v233, s[16:19], s80 offen offset:1024 nt lds
EX 155
MS 172,220,60
CV 180,152,153
EX 156
v_mfma_f32_16x16x32_bf16 v[164:167], a[220:223], v[44:47], v[104:107]
EX 157
MS 164,212,40
CV 181,154,155
EX 158
MS 164,204,36
EX 159
MS 164,196,32
s_waitcnt lgkmcnt(0)
CV 182,156,157
CV 183,158,159
MP 0,224,176
v_add_f32 v240, v240, v144
v_add_f32 v241, v241, v152
MP 32,224,180
v_add_f32 v240, v240, v145
v_add_f32 v241, v241, v153
v_add_f32 v240, v240, v146
MP 36,228,180
s_waitcnt vmcnt(4)
buffer_load_dwordx4 v234, s[16:19], s80 offen offset:2048 nt lds
MP 4,228,176
v_max3_f32 v246, v160, v161, v162
v_max3_f32 v246, v246, v163, v164
v_max3_f32 v246, v246, v165, v166
MP 8,232,176
v_max3_f32 v246, v246, v167, v168
v_max3_f32 v246, v246, v169, v170
MP 40,232,180
v_max3_f32 v246, v246, v171, v172
v_max3_f32 v246, v246, v173, v174
v_max_f32 v246, v246, v175
MP 44,236,180
v_cmp_lt_f32 vcc, s91, v246
v_add_f32 v241, v241, v154
MP 12,236,176
v_add_f32 v240, v240, v147
v_add_f32 v241, v241, v155
v_add_f32 v240, v240, v148
MP 16,240,176
s_cbranch_vccnz L_tr2b
L_back2b:
EX 160
MP 48,240,180
v_add_f32 v241, v241, v156
v_add_f32 v240, v240, v149
MP 52,244,180
s_waitcnt vmcnt(4)
buffer_load_dwordx4 v235, s[16:19], s80 offen offset:3072 nt lds
MP 20,244,176
v_add_f32 v241, v241, v157
v_add_f32 v240, v240, v150
EX 161
MP 24,248,176
s_waitcnt vmcnt(4)
ds_write_b128 v198, v[120:123] offset:16384
v_add_f32 v241, v241, v158
MP 56,248,180
EX 162
MP 60,252,180
ds_write_b128 v198, v[124:127] offset:17408
v_add_f32 v240, v240, v151
MP 28,252,176
v_add_f32 v241, v241, v159
EX 163
v_mfma_f32_16x16x32_bf16 v[144:147], a[192:195], v[64:67], v[112:115]
ds_write_b128 v198, v[128:131] offset:18432
CV 184,160,161
MS 144,200,68
s_add_u32 m0, s89, 4096
EX 164
EX 165
MS 144,208,72
s_add_u32 s80, s90, 65536
ds_write_b128 v198, v[132:135] offset:19456
CV 185,162,163
MS 144,216,76
s_waitcnt vmcnt(4)
buffer_load_dwordx4 v232, s[16:19], s80 offen offset:0 nt lds
EX 166
v_mfma_f32_16x16x32_bf16 v[152:155], a[216:219], v[92:95], v[116:119]
s_waitcnt vmcnt(29)
s_barrier
RB 216,223,8192
EX 167
MS 152,208,88
CV 186,164,165
EX 168
MS 152,200,84
RB 208,222,8192
EX 169
MS 152,192,80
CV 187,166,167
EX 170
v_mfma_f32_16x16x32_bf16 v[156:159], a[196:199], v[80:83], v[116:119]
RB 200,221,8192
EX 171
MS 156,204,84
CV 188,168,169
EX 172
MS 156,212,88
RB 192,220,8192
EX 173
MS 156,220,92
s_waitcnt vmcnt(4)
buffer_load_dwordx4 v233, s[16:19], s80 offen offset:1024 nt lds
v_mfma_f32_16x16x32_bf16 v[148:151], a[220:223], v[76:79], v[112:115]
RB 220,223,12288
CV 189,170,171
MS 148,212,72
EX 174
MS 148,204,68
RB 212,222,12288
EX 175
MS 148,196,64
CV 190,172,173
CV 191,174,175
MP 64,224,184
RB 204,221,12288
v_add_f32 v242, v242, v160
v_add_f32 v243, v243, v168
MP 96,224,188
v_add_f32 v242, v242, v161
v_add_f32 v243, v243, v169
MP 100,228,188
)ASM"
R"ASM(RB 196,220,12288
v_max3_f32 v246, v144, v145, v146
v_max3_f32 v246, v246, v147, v148
MP 68,228,184
s_waitcnt vmcnt(4)
buffer_load_dwordx4 v234, s[16:19], s80 offen offset:2048 nt lds
v_max3_f32 v246, v246, v149, v150
MP 72,232,184
ds_write_b128 v198, v[136:139] offset:20480
v_max3_f32 v246, v246, v151, v152
MP 104,232,188
v_max3_f32 v246, v246, v153, v154
v_max3_f32 v246, v246, v155, v156
v_max3_f32 v246, v246, v157, v158
MP 108,236,188
ds_write_b128 v198, v[140:143] offset:21504
v_max_f32 v246, v246, v159
v_cmp_lt_f32 vcc, s91, v246
MP 76,236,184
v_add_f32 v242, v242, v162
v_add_f32 v243, v243, v170
v_add_f32 v242, v242, v163
MP 80,240,184
s_cbranch_vccnz L_tr2c
L_back2c:
ds_write_b128 v198, v[248:251] offset:22528
v_add_f32 v243, v243, v171
MP 112,240,188
v_add_f32 v242, v242, v164
v_add_f32 v243, v243, v172
MP 116,244,188
s_waitcnt vmcnt(4)
buffer_load_dwordx4 v235, s[16:19], s80 offen offset:3072 nt lds
v_add_f32 v242, v242, v165
MP 84,244,184
ds_write_b128 v198, v[252:255] offset:23552
v_add_f32 v243, v243, v173
EX 144
MP 88,248,184
v_add_f32 v242, v242, v166
v_add_f32 v243, v243, v174
MP 120,248,188
EX 145
MP 124,252,188
v_add_f32 v242, v242, v167
v_add_f32 v243, v243, v175
EX 146
MP 92,252,184
s_waitcnt lgkmcnt(0)
EX 147
v_mfma_f32_16x16x32_bf16 v[160:163], a[192:195], v[0:3], v[96:99]
CV 176,144,145
EX 148
MS 160,200,4
EX 149
MS 160,208,8
s_add_u32 s90, s59, 5120
s_and_b32 s90, s90, 39936
CV 177,146,147
EX 150
MS 160,216,12
s_add_u32 m0, s89, 8192
s_add_u32 s80, s90, 0
EX 151
v_mfma_f32_16x16x32_bf16 v[168:171], a[216:219], v[28:31], v[100:103]
s_waitcnt vmcnt(4)
buffer_load_dwordx4 v232, s[16:19], s80 offen offset:0 nt lds
MS 168,208,24
CV 178,148,149
EX 152
MS 168,200,20
EX 153
MS 168,192,16
CV 179,150,151
EX 154
v_mfma_f32_16x16x32_bf16 v[172:175], a[196:199], v[16:19], v[100:103]
EX 155
MS 172,204,20
CV 180,152,153
EX 156
MS 172,212,24
EX 157
MS 172,220,28
s_waitcnt vmcnt(4)
buffer_load_dwordx4 v233, s[16:19], s80 offen offset:1024 nt lds
v_mfma_f32_16x16x32_bf16 v[164:167], a[220:223], v[12:15], v[96:99]
CV 181,154,155
EX 158
MS 164,212,8
EX 159
MS 164,204,4
CV 182,156,157
CV 183,158,159
MS 164,196,0
MP 128,224,176
v_add_f32 v244, v244, v144
v_add_f32 v245, v245, v152
v_add_f32 v244, v244, v145
MP 160,224,180
v_add_f32 v245, v245, v153
v_add_f32 v244, v244, v146
v_add_f32 v245, v245, v154
MP 164,228,180
RT 224,224,24576
v_max3_f32 v246, v160, v161, v162
v_max3_f32 v246, v246, v163, v164
MP 132,228,176
RT 226,224,28672
v_max3_f32 v246, v246, v165, v166
v_max3_f32 v246, v246, v167, v168
MP 136,232,176
s_waitcnt vmcnt(4)
buffer_load_dwordx4 v234, s[16:19], s80 offen offset:2048 nt lds
v_max3_f32 v246, v246, v169, v170
MP 168,232,180
RT 228,225,24576
v_max3_f32 v246, v246, v171, v172
v_max3_f32 v246, v246, v173, v174
MP 172,236,180
RT 230,225,28672
v_max_f32 v246, v246, v175
v_cmp_lt_f32 vcc, s91, v246
MP 140,236,176
RT 232,226,24576
v_add_f32 v244, v244, v147
v_add_f32 v245, v245, v155
MP 144,240,176
s_cbranch_vccnz L_tr3a
L_back3a:
RT 234,226,28672
v_add_f32 v244, v244, v148
EX 160
MP 176,240,180
v_add_f32 v245, v245, v156
v_add_f32 v244, v244, v149
MP 180,244,180
RT 236,227,24576
v_add_f32 v245, v245, v157
EX 161
MP 148,244,176
RT 238,227,28672
v_add_f32 v244, v244, v150
MP 152,248,176
s_waitcnt vmcnt(4)
buffer_load_dwordx4 v235, s[16:19], s80 offen offset:3072 nt lds
EX 162
MP 184,248,180
RT 240,228,24576
v_add_f32 v245, v245, v158
MP 188,252,180
RT 242,228,28672
v_add_f32 v244, v244, v151
EX 163
MP 156,252,176
RT 244,229,24576
v_add_f32 v245, v245, v159
v_mfma_f32_16x16x32_bf16 v[144:147], a[192:195], v[32:35], v[104:107]
CV 184,160,161
EX 164
MS 144,200,36
RT 246,229,28672
EX 165
MS 144,208,40
RT 248,230,24576
CV 185,162,163
MS 144,216,44
s_add_u32 m0, s89, 12288
RT 250,230,28672
EX 166
v_mfma_f32_16x16x32_bf16 v[152:155], a[216:219], v[60:63], v[108:111]
s_add_u32 s80, s90, 65536
EX 167
MS 152,208,56
s_waitcnt vmcnt(4)
buffer_load_dwordx4 v232, s[16:19], s80 offen offset:0 nt lds
MS 152,200,52
RT 252,231,24576
CV 186,164,165
MS 152,192,48
RT 254,231,28672
EX 168
v_mfma_f32_16x16x32_bf16 v[156:159], a[196:199], v[48:51], v[108:111]
EX 169
CV 187,166,167
MS 156,204,52
EX 170
MS 156,212,56
EX 171
CV 188,168,169
MS 156,220,60
EX 172
v_mfma_f32_16x16x32_bf16 v[148:151], a[220:223], v[44:47], v[104:107]
EX 173
CV 189,170,171
MS 148,212,40
s_waitcnt vmcnt(4)
buffer_load_dwordx4 v233, s[16:19], s80 offen offset:1024 nt lds
MS 148,204,36
EX 174
EX 175
MS 148,196,32
s_waitcnt lgkmcnt(0)
CV 190,172,173
CV 191,174,175
MP 0,224,184
v_add_f32 v240, v240, v160
v_add_f32 v241, v241, v168
MP 32,224,188
v_add_f32 v240, v240, v161
v_add_f32 v241, v241, v169
v_add_f32 v240, v240, v162
MP 36,228,188
v_max3_f32 v246, v144, v145, v146
v_max3_f32 v246, v246, v147, v148
MP 4,228,184
v_max3_f32 v246, v246, v149, v150
v_max3_f32 v246, v246, v151, v152
MP 8,232,184
v_max3_f32 v246, v246, v153, v154
v_max3_f32 v246, v246, v155, v156
v_max3_f32 v246, v246, v157, v158
MP 40,232,188
v_max_f32 v246, v246, v159
v_cmp_lt_f32 vcc, s91, v246
MP 44,236,188
s_waitcnt vmcnt(4)
buffer_load_dwordx4 v234, s[16:19], s80 offen offset:2048 nt lds
MP 12,236,184
s_cbranch_vccnz L_tr3b
L_back3b:
v_add_f32 v241, v241, v170
v_add_f32 v240, v240, v163
v_add_f32 v241, v241, v171
MP 16,240,184
v_add_f32 v240, v240, v164
v_add_f32 v241, v241, v172
MP 48,240,188
EX 144
MP 52,244,188
v_add_f32 v240, v240, v165
v_add_f32 v241, v241, v173
MP 20,244,184
EX 145
MP 24,248,184
v_add_f32 v240, v240, v166
v_add_f32 v241, v241, v174
EX 146
MP 56,248,188
EX 147
MP 60,252,188
v_add_f32 v240, v240, v167
v_add_f32 v241, v241, v175
MP 28,252,184
s_waitcnt vmcnt(4)
buffer_load_dwordx4 v235, s[16:19], s80 offen offset:3072 nt lds
v_mfma_f32_16x16x32_bf16 v[160:163], a[192:195], v[64:67], v[112:115]
CV 176,144,145
EX 148
MS 160,200,68
EX 149
MS 160,208,72
CV 177,146,147
EX 150
MS 160,216,76
EX 151
v_mfma_f32_16x16x32_bf16 v[168:171], a[216:219], v[92:95], v[116:119]
s_waitcnt vmcnt(24)
s_barrier
RB 216,223,32768
CV 178,148,149
MS 168,208,88
s_add_u32 m0, s89, 24576
EX 152
EX 153
MS 168,200,84
s_add_u32 s80, s90, 0
RB 208,222,32768
CV 179,150,151
MS 168,192,80
s_waitcnt vmcnt(4)
buffer_load_dwordx4 v236, s[20:23], s80 offen offset:0 nt lds
v_mfma_f32_16x16x32_bf16 v[172:175], a[196:199], v[80:83], v[116:119]
RB 200,221,32768
EX 154
MS 172,204,84
EX 155
MS 172,212,88
RB 192,220,32768
CV 180,152,153
MS 172,220,92
EX 156
EX 157
v_mfma_f32_16x16x32_bf16 v[164:167], a[220:223], v[76:79], v[112:115]
RB 220,223,36864
CV 181,154,155
MS 164,212,72
EX 158
MS 164,204,68
RB 212,222,36864
EX 159
MS 164,196,64
CV 182,156,157
CV 183,158,159
MP 64,224,176
RB 204,221,36864
v_add_f32 v242, v242, v144
MP 96,224,180
s_waitcnt vmcnt(4)
buffer_load_dwordx4 v199, s[20:23], s80 offen offset:1024 nt lds
v_add_f32 v243, v243, v152
MP 100,228,180
RB 196,220,36864
v_max3_f32 v246, v160, v161, v162
MP 68,228,176
v_max3_f32 v246, v246, v163, v164
v_max3_f32 v246, v246, v165, v166
v_max3_f32 v246, v246, v167, v168
MP 72,232,176
v_max3_f32 v246, v246, v169, v170
v_max3_f32 v246, v246, v171, v172
MP 104,232,180
v_max3_f32 v246, v246, v173, v174
v_max_f32 v246, v246, v175
v_cmp_lt_f32 vcc, s91, v246
MP 108,236,180
v_add_f32 v242, v242, v145
v_add_f32 v243, v243, v153
MP 76,236,176
s_cbranch_vccnz L_tr3c
L_back3c:
v_add_f32 v242, v242, v146
v_add_f32 v243, v243, v154
v_add_f32 v242, v242, v147
MP 80,240,176
v_add_f32 v243, v243, v155
v_add_f32 v242, v242, v148
MP 112,240,180
v_add_f32 v243, v243, v156
v_add_f32 v242, v242, v149
MP 116,244,180
s_waitcnt vmcnt(4)
buffer_load_dwordx4 v238, s[20:23], s80 offen offset:2048 nt lds
MP 84,244,176
v_add_f32 v243, v243, v157
v_add_f32 v242, v242, v150
EX 160
MP 88,248,176
EX 161
MP 120,248,180
v_add_f32 v243, v243, v158
v_add_f32 v242, v242, v151
MP 124,252,180
EX 162
MP 92,252,176
s_waitcnt lgkmcnt(0)
v_add_f32 v243, v243, v159
EX 163
v_mfma_f32_16x16x32_bf16 v[144:147], a[192:195], v[0:3], v[96:99]
CV 184,160,161
EX 164
MS 144,200,4
EX 165
MS 144,208,8
s_waitcnt vmcnt(4)
buffer_load_dwordx4 v239, s[20:23], s80 offen offset:3072 nt lds
MS 144,216,12
CV 185,162,163
EX 166
v_mfma_f32_16x16x32_bf16 v[152:155], a[216:219], v[28:31], v[100:103]
EX 167
MS 152,208,24
CV 186,164,165
EX 168
MS 152,200,20
EX 169
MS 152,192,16
CV 187,166,167
EX 170
v_mfma_f32_16x16x32_bf16 v[156:159], a[196:199], v[16:19], v[100:103]
EX 171
MS 156,204,20
s_add_u32 m0, s89, 28672
CV 188,168,169
EX 172
MS 156,212,24
s_add_u32 s80, s90, 65536
EX 173
MS 156,220,28
s_waitcnt vmcnt(4)
buffer_load_dwordx4 v236, s[20:23], s80 offen offset:0 nt lds
v_mfma_f32_16x16x32_bf16 v[148:151], a[220:223], v[12:15], v[96:99]
CV 189,170,171
EX 174
MS 148,212,8
EX 175
MS 148,204,4
CV 190,172,173
CV 191,174,175
MS 148,196,0
MP 128,224,184
v_add_f32 v244, v244, v160
v_add_f32 v245, v245, v168
v_add_f32 v244, v244, v161
MP 160,224,188
v_add_f32 v245, v245, v169
v_add_f32 v244, v244, v162
v_add_f32 v245, v245, v170
MP 164,228,188
RT 224,224,16384
v_max3_f32 v246, v144, v145, v146
v_max3_f32 v246, v246, v147, v148
MP 132,228,184
RT 226,224,20480
v_max3_f32 v246, v246, v149, v150
v_max3_f32 v246, v246, v151, v152
MP 136,232,184
s_waitcnt vmcnt(4)
buffer_load_dwordx4 v199, s[20:23], s80 offen offset:1024 nt lds
v_max3_f32 v246, v246, v153, v154
MP 168,232,188
RT 228,225,16384
v_max3_f32 v246, v246, v155, v156
v_max3_f32 v246, v246, v157, v158
MP 172,236,188
RT 230,225,20480
v_max_f32 v246, v246, v159
v_cmp_lt_f32 vcc, s91, v246
MP 140,236,184
RT 232,226,16384
v_add_f32 v244, v244, v163
v_add_f32 v245, v245, v171
MP 144,240,184
s_cbranch_vccnz L_tr4a
L_back4a:
RT 234,226,20480
v_add_f32 v244, v244, v164
MP 176,240,188
v_add_f32 v245, v245, v172
v_add_f32 v244, v244, v165
EX 144
MP 180,244,188
RT 236,227,16384
v_add_f32 v245, v245, v173
MP 148,244,184
RT 238,227,20480
v_add_f32 v244, v244, v166
EX 145
MP 152,248,184
EX 146
MP 184,248,188
s_waitcnt vmcnt(4)
buffer_load_dwordx4 v238, s[20:23], s80 offen offset:2048 nt lds
v_add_f32 v245, v245, v174
MP 188,252,188
RT 240,228,16384
v_add_f32 v244, v244, v167
EX 147
MP 156,252,184
RT 242,228,20480
v_add_f32 v245, v245, v175
v_mfma_f32_16x16x32_bf16 v[160:163], a[192:195], v[32:35], v[104:107]
RT 244,229,16384
CV 176,144,145
MS 160,200,36
RT 246,229,20480
EX 148
MS 160,208,40
RT 248,230,16384
EX 149
MS 160,216,44
RT 250,230,20480
CV 177,146,147
v_mfma_f32_16x16x32_bf16 v[168:171], a[216:219], v[60:63], v[108:111]
EX 150
MS 168,208,56
RT 252,231,16384
EX 151
MS 168,200,52
s_waitcnt vmcnt(4)
buffer_load_dwordx4 v239, s[20:23], s80 offen offset:3072 nt lds
CV 178,148,149
MS 168,192,48
RT 254,231,20480
EX 152
v_mfma_f32_16x16x32_bf16 v[172:175], a[196:199], v[48:51], v[108:111]
EX 153
MS 172,204,52
CV 179,150,151
EX 154
MS 172,212,56
EX 155
MS 172,220,60
CV 180,152,153
EX 156
v_mfma_f32_16x16x32_bf16 v[164:167], a[220:223], v[44:47], v[104:107]
EX 157
MS 164,212,40
CV 181,154,155
EX 158
MS 164,204,36
EX 159
MS 164,196,32
s_waitcnt lgkmcnt(0)
CV 182,156,157
CV 183,158,159
MP 0,224,176
s_add_u32 s90, s59, 6144
s_and_b32 s90, s90, 39936
v_add_f32 v240, v240, v144
v_add_f32 v241, v241, v152
MP 32,224,180
s_add_u32 m0, s89, 16384
s_add_u32 s80, s90, 0
v_max3_f32 v246, v160, v161, v162
v_max3_f32 v246, v246, v163, v164
v_max3_f32 v246, v246, v165, v166
MP 36,228,180
s_waitcnt vmcnt(4)
buffer_load_dwordx4 v236, s[20:23], s80 offen offset:0 nt lds
MP 4,228,176
v_max3_f32 v246, v246, v167, v168
v_max3_f32 v246, v246, v169, v170
v_max3_f32 v246, v246, v171, v172
MP 8,232,176
v_max3_f32 v246, v246, v173, v174
v_max_f32 v246, v246, v175
MP 40,232,180
v_cmp_lt_f32 vcc, s91, v246
v_add_f32 v240, v240, v145
v_add_f32 v241, v241, v153
MP 44,236,180
v_add_f32 v240, v240, v146
v_add_f32 v241, v241, v154
MP 12,236,176
s_cbranch_vccnz L_tr4b
L_back4b:
v_add_f32 v240, v240, v147
v_add_f32 v241, v241, v155
MP 16,240,176
s_waitcnt vmcnt(4)
buffer_load_dwordx4 v199, s[20:23], s80 offen offset:1024 nt lds
v_add_f32 v240, v240, v148
MP 48,240,180
v_add_f32 v241, v241, v156
v_add_f32 v240, v240, v149
MP 52,244,180
EX 160
MP 20,244,176
v_add_f32 v241, v241, v157
v_add_f32 v240, v240, v150
EX 161
MP 24,248,176
EX 162
MP 56,248,180
v_add_f32 v241, v241, v158
v_add_f32 v240, v240, v151
MP 60,252,180
s_waitcnt vmcnt(4)
buffer_load_dwordx4 v238, s[20:23], s80 offen offset:2048 nt lds
MP 28,252,176
v_add_f32 v241, v241, v159
EX 163
v_mfma_f32_16x16x32_bf16 v[144:147], a[192:195], v[64:67], v[112:115]
CV 184,160,161
EX 164
MS 144,200,68
EX 165
CV 185,162,163
MS 144,208,72
EX 166
MS 144,216,76
s_waitcnt vmcnt(4)
buffer_load_dwordx4 v239, s[20:23], s80 offen offset:3072 nt lds
EX 167
v_mfma_f32_16x16x32_bf16 v[152:155], a[216:219], v[92:95], v[116:119]
s_waitcnt vmcnt(12)
s_barrier
RB 216,223,8192
CV 186,164,165
MS 152,208,88
EX 168
MS 152,200,84
RB 208,222,8192
EX 169
MS 152,192,80
s_add_u32 m0, s89, 20480
CV 187,166,167
EX 170
v_mfma_f32_16x16x32_bf16 v[156:159], a[196:199], v[80:83], v[116:119]
s_add_u32 s80, s90, 65536
RB 200,221,8192
EX 171
MS 156,204,84
s_waitcnt vmcnt(4)
buffer_load_dwordx4 v236, s[20:23], s80 offen offset:0 nt lds
CV 188,168,169
MS 156,212,88
RB 192,220,8192
EX 172
MS 156,220,92
EX 173
v_mfma_f32_16x16x32_bf16 v[148:151], a[220:223], v[76:79], v[112:115]
RB 220,223,12288
CV 189,170,171
MS 148,212,72
EX 174
EX 175
MS 148,204,68
RB 212,222,12288
CV 190,172,173
MS 148,196,64
s_waitcnt vmcnt(4)
buffer_load_dwordx4 v199, s[20:23], s80 offen offset:1024 nt lds
CV 191,174,175
MP 64,224,184
RB 204,221,12288
v_add_f32 v242, v242, v160
MP 96,224,188
v_add_f32 v243, v243, v168
v_add_f32 v242, v242, v161
v_add_f32 v243, v243, v169
MP 100,228,188
RB 196,220,12288
v_max3_f32 v246, v144, v145, v146
MP 68,228,184
v_max3_f32 v246, v246, v147, v148
v_max3_f32 v246, v246, v149, v150
v_max3_f32 v246, v246, v151, v152
MP 72,232,184
v_max3_f32 v246, v246, v153, v154
v_max3_f32 v246, v246, v155, v156
MP 104,232,188
s_waitcnt vmcnt(4)
buffer_load_dwordx4 v238, s[20:23], s80 offen offset:2048 nt lds
v_max3_f32 v246, v246, v157, v158
MP 108,236,188
v_max_f32 v246, v246, v159
v_cmp_lt_f32 vcc, s91, v246
MP 76,236,184
v_add_f32 v242, v242, v162
v_add_f32 v243, v243, v170
v_add_f32 v242, v242, v163
MP 80,240,184
s_cbranch_vccnz L_tr4c
L_back4c:
v_add_f32 v243, v243, v171
v_add_f32 v242, v242, v164
MP 112,240,188
v_add_f32 v243, v243, v172
v_add_f32 v242, v242, v165
MP 116,244,188
EX 144
MP 84,244,184
s_waitcnt vmcnt(4)
buffer_load_dwordx4 v239, s[20:23], s80 offen offset:3072 nt lds
v_add_f32 v243, v243, v173
MP 88,248,184
v_add_f32 v242, v242, v166
v_add_f32 v243, v243, v174
MP 120,248,188
EX 145
EX 146
MP 124,252,188
v_add_f32 v242, v242, v167
v_add_f32 v243, v243, v175
MP 92,252,184
s_waitcnt lgkmcnt(0)
EX 147
v_mfma_f32_16x16x32_bf16 v[160:163], a[192:195], v[0:3], v[96:99]
CV 176,144,145
EX 148
MS 160,200,4
EX 149
MS 160,208,8
s_add_u32 s90, s59, 7168
s_and_b32 s90, s90, 39936
CV 177,146,147
EX 150
MS 160,216,12
s_add_u32 m0, s89, 8192
s_add_u32 s80, s90, 0
EX 151
v_mfma_f32_16x16x32_bf16 v[168:171], a[216:219], v[28:31], v[100:103]
s_waitcnt vmcnt(4)
buffer_load_dwordx4 v232, s[16:19], s80 offen offset:0 nt lds
MS 168,208,24
CV 178,148,149
EX 152
MS 168,200,20
EX 153
MS 168,192,16
CV 179,150,151
EX 154
v_mfma_f32_16x16x32_bf16 v[172:175], a[196:199], v[16:19], v[100:103]
s_waitcnt vmcnt(4)
buffer_load_dwordx4 v233, s[16:19], s80 offen offset:1024 nt lds
MS 172,204,20
EX 155
MS 172,212,24
CV 180,152,153
EX 156
MS 172,220,28
EX 157
v_mfma_f32_16x16x32_bf16 v[164:167], a[220:223], v[12:15], v[96:99]
CV 181,154,155
EX 158
MS 164,212,8
EX 159
MS 164,204,4
s_waitcnt vmcnt(4)
buffer_load_dwordx4 v234, s[16:19], s80 offen offset:2048 nt lds
MS 164,196,0
CV 182,156,157
CV 183,158,159
MP 128,224,176
v_add_f32 v244, v244, v144
v_add_f32 v245, v245, v152
v_add_f32 v244, v244, v145
MP 160,224,180
v_add_f32 v245, v245, v153
v_add_f32 v244, v244, v146
v_add_f32 v245, v245, v154
MP 164,228,180
s_waitcnt vmcnt(11)
RT 224,224,24576
v_max3_f32 v246, v160, v161, v162
v_max3_f32 v246, v246, v163, v164
MP 132,228,176
RT 226,224,28672
v_max3_f32 v246, v246, v165, v166
v_max3_f32 v246, v246, v167, v168
MP 136,232,176
s_waitcnt vmcnt(4)
buffer_load_dwordx4 v235, s[16:19], s80 offen offset:3072 nt lds
v_max3_f32 v246, v246, v169, v170
MP 168,232,180
RT 228,225,24576
v_max3_f32 v246, v246, v171, v172
v_max3_f32 v246, v246, v173, v174
MP 172,236,180
RT 230,225,28672
v_max_f32 v246, v246, v175
v_cmp_lt_f32 vcc, s91, v246
MP 140,236,176
RT 232,226,24576
v_add_f32 v244, v244, v147
v_add_f32 v245, v245, v155
MP 144,240,176
s_cbranch_vccnz L_tr5a
L_back5a:
s_add_u32 m0, s89, 12288
RT 234,226,28672
v_add_f32 v244, v244, v148
EX 160
MP 176,240,180
s_add_u32 s80, s90, 65536
v_add_f32 v245, v245, v156
v_add_f32 v244, v244, v149
MP 180,244,180
s_waitcnt vmcnt(4)
buffer_load_dwordx4 v232, s[16:19], s80 offen offset:0 nt lds
EX 161
MP 148,244,176
RT 236,227,24576
v_add_f32 v245, v245, v157
MP 152,248,176
RT 238,227,28672
v_add_f32 v244, v244, v150
EX 162
MP 184,248,180
RT 240,228,24576
v_add_f32 v245, v245, v158
MP 188,252,180
RT 242,228,28672
v_add_f32 v244, v244, v151
EX 163
MP 156,252,176
RT 244,229,24576
v_add_f32 v245, v245, v159
v_mfma_f32_16x16x32_bf16 v[144:147], a[192:195], v[32:35], v[104:107]
s_waitcnt vmcnt(4)
buffer_load_dwordx4 v233, s[16:19], s80 offen offset:1024 nt lds
CV 184,160,161
MS 144,200,36
RT 246,229,28672
EX 164
MS 144,208,40
RT 248,230,24576
EX 165
MS 144,216,44
RT 250,230,28672
CV 185,162,163
v_mfma_f32_16x16x32_bf16 v[152:155], a[216:219], v[60:63], v[108:111]
EX 166
MS 152,208,56
RT 252,231,24576
EX 167
MS 152,200,52
s_waitcnt vmcnt(4)
buffer_load_dwordx4 v234, s[16:19], s80 offen offset:2048 nt lds
CV 186,164,165
MS 152,192,48
RT 254,231,28672
EX 168
v_mfma_f32_16x16x32_bf16 v[156:159], a[196:199], v[48:51], v[108:111]
EX 169
MS 156,204,52
CV 187,166,167
EX 170
MS 156,212,56
EX 171
CV 188,168,169
MS 156,220,60
EX 172
v_mfma_f32_16x16x32_bf16 v[148:151], a[220:223], v[44:47], v[104:107]
s_waitcnt vmcnt(4)
buffer_load_dwordx4 v235, s[16:19], s80 offen offset:3072 nt lds
MS 148,212,40
EX 173
CV 189,170,171
MS 148,204,36
EX 174
EX 175
MS 148,196,32
s_waitcnt lgkmcnt(0)
CV 190,172,173
CV 191,174,175
MP 0,224,184
s_add_u32 m0, s89, 24576
v_add_f32 v240, v240, v160
v_add_f32 v241, v241, v168
MP 32,224,188
s_add_u32 s80, s90, 0
v_add_f32 v240, v240, v161
v_add_f32 v241, v241, v169
v_max3_f32 v246, v144, v145, v146
MP 36,228,188
s_waitcnt vmcnt(4)
buffer_load_dwordx4 v236, s[20:23], s80 offen offset:0 nt lds
MP 4,228,184
v_max3_f32 v246, v246, v147, v148
v_max3_f32 v246, v246, v149, v150
v_max3_f32 v246, v246, v151, v152
MP 8,232,184
v_max3_f32 v246, v246, v153, v154
v_max3_f32 v246, v246, v155, v156
MP 40,232,188
v_max3_f32 v246, v246, v157, v158
v_max_f32 v246, v246, v159
v_cmp_lt_f32 vcc, s91, v246
MP 44,236,188
v_add_f32 v240, v240, v162
v_add_f32 v241, v241, v170
MP 12,236,184
s_cbranch_vccnz L_tr5b
L_back5b:
v_add_f32 v240, v240, v163
v_add_f32 v241, v241, v171
MP 16,240,184
s_waitcnt vmcnt(4)
buffer_load_dwordx4 v199, s[20:23], s80 offen offset:1024 nt lds
v_add_f32 v240, v240, v164
MP 48,240,188
v_add_f32 v241, v241, v172
v_add_f32 v240, v240, v165
MP 52,244,188
EX 144
MP 20,244,184
v_add_f32 v241, v241, v173
v_add_f32 v240, v240, v166
EX 145
MP 24,248,184
EX 146
MP 56,248,188
v_add_f32 v241, v241, v174
v_add_f32 v240, v240, v167
MP 60,252,188
s_waitcnt vmcnt(4)
buffer_load_dwordx4 v238, s[20:23], s80 offen offset:2048 nt lds
MP 28,252,184
v_add_f32 v241, v241, v175
EX 147
v_mfma_f32_16x16x32_bf16 v[160:163], a[192:195], v[64:67], v[112:115]
CV 176,144,145
EX 148
MS 160,200,68
EX 149
CV 177,146,147
MS 160,208,72
EX 150
MS 160,216,76
s_waitcnt vmcnt(4)
buffer_load_dwordx4 v239, s[20:23], s80 offen offset:3072 nt lds
EX 151
v_mfma_f32_16x16x32_bf16 v[168:171], a[216:219], v[92:95], v[116:119]
s_waitcnt vmcnt(36)
s_barrier
RB 216,223,0
CV 178,148,149
MS 168,208,88
EX 152
MS 168,200,84
RB 208,222,0
EX 153
MS 168,192,80
s_add_u32 m0, s89, 28672
CV 179,150,151
EX 154
v_mfma_f32_16x16x32_bf16 v[172:175], a[196:199], v[80:83], v[116:119]
s_add_u32 s80, s90, 65536
RB 200,221,0
EX 155
MS 172,204,84
s_waitcnt vmcnt(4)
buffer_load_dwordx4 v236, s[20:23], s80 offen offset:0 nt lds
CV 180,152,153
MS 172,212,88
RB 192,220,0
EX 156
MS 172,220,92
EX 157
v_mfma_f32_16x16x32_bf16 v[164:167], a[220:223], v[76:79], v[112:115]
RB 220,223,4096
CV 181,154,155
MS 164,212,72
EX 158
EX 159
MS 164,204,68
RB 212,222,4096
CV 182,156,157
MS 164,196,64
s_waitcnt vmcnt(4)
buffer_load_dwordx4 v199, s[20:23], s80 offen offset:1024 nt lds
CV 183,158,159
MP 64,224,176
RB 204,221,4096
v_add_f32 v242, v242, v144
MP 96,224,180
v_add_f32 v243, v243, v152
v_add_f32 v242, v242, v145
v_add_f32 v243, v243, v153
MP 100,228,180
RB 196,220,4096
v_max3_f32 v246, v160, v161, v162
MP 68,228,176
v_max3_f32 v246, v246, v163, v164
v_max3_f32 v246, v246, v165, v166
v_max3_f32 v246, v246, v167, v168
MP 72,232,176
v_max3_f32 v246, v246, v169, v170
v_max3_f32 v246, v246, v171, v172
MP 104,232,180
s_waitcnt vmcnt(4)
buffer_load_dwordx4 v238, s[20:23], s80 offen offset:2048 nt lds
v_max3_f32 v246, v246, v173, v174
MP 108,236,180
v_max_f32 v246, v246, v175
v_cmp_lt_f32 vcc, s91, v246
MP 76,236,176
v_add_f32 v242, v242, v146
v_add_f32 v243, v243, v154
v_add_f32 v242, v242, v147
MP 80,240,176
s_cbranch_vccnz L_tr5c
L_back5c:
v_add_f32 v243, v243, v155
v_add_f32 v242, v242, v148
MP 112,240,180
v_add_f32 v243, v243, v156
v_add_f32 v242, v242, v149
MP 116,244,180
EX 160
MP 84,244,176
s_waitcnt vmcnt(4)
buffer_load_dwordx4 v239, s[20:23], s80 offen offset:3072 nt lds
v_add_f32 v243, v243, v157
MP 88,248,176
v_add_f32 v242, v242, v150
v_add_f32 v243, v243, v158
MP 120,248,180
s_sub_u32 s90, s90, 39936
EX 161
EX 162
MP 124,252,180
s_and_b32 s90, s90, 39936
v_add_f32 v242, v242, v151
v_add_f32 v243, v243, v159
MP 92,252,176
s_waitcnt lgkmcnt(0)
EX 163
s_getpc_b64 s[62:63]
L_w5:
s_add_u32 s62, s62, L_c2-L_w5
s_addc_u32 s63, s63, 0
v_min_u32 v196, L_c3-L_c2, v211
s_mov_b64 exec, 3
global_load_dword v215, v196, s[62:63]
s_mov_b64 exec, -1
s_mov_b32 s79, 0
s_cmp_lg_u32 s77, 0
)ASM"
R"ASM(s_cbranch_scc1 L_ns
v_lshlrev_b32 v193, 2, v192
buffer_load_dword v194, v193, s[40:43], 0 offen offset:2560 sc1
s_waitcnt vmcnt(0)
buffer_store_dword v194, v193, s[40:43], 0 offen offset:2816 sc1
L_ns:
L_aloop: ; ---- attention tiles 6..13
v_mfma_f32_16x16x32_bf16 v[144:147], a[192:195], v[0:3], v[96:99]
s_add_u32 m0, s89, 0
CV 184,160,161
EX 164
MS 144,200,4
s_add_u32 s80, s90, 0
EX 165
MS 144,208,8
s_waitcnt vmcnt(5)
buffer_load_dwordx4 v232, s[16:19], s80 offen offset:0 nt lds
MS 144,216,12
CV 185,162,163
EX 166
v_mfma_f32_16x16x32_bf16 v[152:155], a[216:219], v[28:31], v[100:103]
EX 167
MS 152,208,24
CV 186,164,165
EX 168
MS 152,200,20
EX 169
MS 152,192,16
CV 187,166,167
EX 170
v_mfma_f32_16x16x32_bf16 v[156:159], a[196:199], v[16:19], v[100:103]
s_waitcnt vmcnt(5)
buffer_load_dwordx4 v233, s[16:19], s80 offen offset:1024 nt lds
MS 156,204,20
EX 171
MS 156,212,24
CV 188,168,169
EX 172
MS 156,220,28
EX 173
v_mfma_f32_16x16x32_bf16 v[148:151], a[220:223], v[12:15], v[96:99]
CV 189,170,171
EX 174
MS 148,212,8
EX 175
MS 148,204,4
s_waitcnt vmcnt(5)
buffer_load_dwordx4 v234, s[16:19], s80 offen offset:2048 nt lds
MS 148,196,0
CV 190,172,173
CV 191,174,175
MP 128,224,184
v_add_f32 v244, v244, v160
v_add_f32 v245, v245, v168
v_add_f32 v244, v244, v161
MP 160,224,188
v_add_f32 v245, v245, v169
v_add_f32 v244, v244, v162
v_add_f32 v245, v245, v170
MP 164,228,188
s_waitcnt vmcnt(19)
RT 224,224,16384
v_max3_f32 v246, v144, v145, v146
v_max3_f32 v246, v246, v147, v148
MP 132,228,184
RT 226,224,20480
v_max3_f32 v246, v246, v149, v150
v_max3_f32 v246, v246, v151, v152
MP 136,232,184
s_waitcnt vmcnt(5)
buffer_load_dwordx4 v235, s[16:19], s80 offen offset:3072 nt lds
v_max3_f32 v246, v246, v153, v154
MP 168,232,188
RT 228,225,16384
v_max3_f32 v246, v246, v155, v156
v_max3_f32 v246, v246, v157, v158
MP 172,236,188
RT 230,225,20480
v_max_f32 v246, v246, v159
v_cmp_lt_f32 vcc, s91, v246
MP 140,236,184
RT 232,226,16384
v_add_f32 v244, v244, v163
v_add_f32 v245, v245, v171
MP 144,240,184
s_cbranch_vccnz L_trL6a
L_backL6a:
s_add_u32 m0, s89, 4096
RT 234,226,20480
v_add_f32 v244, v244, v164
EX 144
MP 176,240,188
s_add_u32 s80, s90, 65536
v_add_f32 v245, v245, v172
v_add_f32 v244, v244, v165
MP 180,244,188
s_waitcnt vmcnt(5)
buffer_load_dwordx4 v232, s[16:19], s80 offen offset:0 nt lds
EX 145
MP 148,244,184
RT 236,227,16384
v_add_f32 v245, v245, v173
MP 152,248,184
RT 238,227,20480
v_add_f32 v244, v244, v166
EX 146
MP 184,248,188
RT 240,228,16384
v_add_f32 v245, v245, v174
MP 188,252,188
RT 242,228,20480
v_add_f32 v244, v244, v167
EX 147
MP 156,252,184
RT 244,229,16384
v_add_f32 v245, v245, v175
v_mfma_f32_16x16x32_bf16 v[160:163], a[192:195], v[32:35], v[104:107]
s_waitcnt vmcnt(5)
buffer_load_dwordx4 v233, s[16:19], s80 offen offset:1024 nt lds
CV 176,144,145
MS 160,200,36
RT 246,229,20480
EX 148
MS 160,208,40
RT 248,230,16384
EX 149
MS 160,216,44
RT 250,230,20480
CV 177,146,147
v_mfma_f32_16x16x32_bf16 v[168:171], a[216:219], v[60:63], v[108:111]
EX 150
MS 168,208,56
RT 252,231,16384
EX 151
MS 168,200,52
s_waitcnt vmcnt(5)
buffer_load_dwordx4 v234, s[16:19], s80 offen offset:2048 nt lds
CV 178,148,149
MS 168,192,48
RT 254,231,20480
EX 152
v_mfma_f32_16x16x32_bf16 v[172:175], a[196:199], v[48:51], v[108:111]
EX 153
MS 172,204,52
CV 179,150,151
EX 154
MS 172,212,56
EX 155
CV 180,152,153
MS 172,220,60
EX 156
v_mfma_f32_16x16x32_bf16 v[164:167], a[220:223], v[44:47], v[104:107]
s_waitcnt vmcnt(5)
buffer_load_dwordx4 v235, s[16:19], s80 offen offset:3072 nt lds
MS 164,212,40
EX 157
CV 181,154,155
MS 164,204,36
EX 158
EX 159
MS 164,196,32
s_waitcnt lgkmcnt(0)
CV 182,156,157
CV 183,158,159
MP 0,224,176
s_add_u32 m0, s89, 16384
v_add_f32 v240, v240, v144
v_add_f32 v241, v241, v152
MP 32,224,180
s_add_u32 s80, s90, 0
v_add_f32 v240, v240, v145
v_add_f32 v241, v241, v153
v_max3_f32 v246, v160, v161, v162
MP 36,228,180
s_waitcnt vmcnt(5)
buffer_load_dwordx4 v236, s[20:23], s80 offen offset:0 nt lds
MP 4,228,176
v_max3_f32 v246, v246, v163, v164
v_max3_f32 v246, v246, v165, v166
v_max3_f32 v246, v246, v167, v168
MP 8,232,176
v_max3_f32 v246, v246, v169, v170
v_max3_f32 v246, v246, v171, v172
MP 40,232,180
v_max3_f32 v246, v246, v173, v174
v_max_f32 v246, v246, v175
v_cmp_lt_f32 vcc, s91, v246
MP 44,236,180
v_add_f32 v240, v240, v146
v_add_f32 v241, v241, v154
MP 12,236,176
s_cbranch_vccnz L_trL6b
L_backL6b:
v_add_f32 v240, v240, v147
v_add_f32 v241, v241, v155
MP 16,240,176
s_waitcnt vmcnt(5)
buffer_load_dwordx4 v199, s[20:23], s80 offen offset:1024 nt lds
v_add_f32 v240, v240, v148
MP 48,240,180
v_add_f32 v241, v241, v156
v_add_f32 v240, v240, v149
MP 52,244,180
EX 160
MP 20,244,176
v_add_f32 v241, v241, v157
v_add_f32 v240, v240, v150
EX 161
MP 24,248,176
EX 162
MP 56,248,180
v_add_f32 v241, v241, v158
v_add_f32 v240, v240, v151
MP 60,252,180
s_waitcnt vmcnt(5)
buffer_load_dwordx4 v238, s[20:23], s80 offen offset:2048 nt lds
MP 28,252,176
v_add_f32 v241, v241, v159
EX 163
v_mfma_f32_16x16x32_bf16 v[144:147], a[192:195], v[64:67], v[112:115]
CV 184,160,161
EX 164
MS 144,200,68
EX 165
CV 185,162,163
MS 144,208,72
EX 166
MS 144,216,76
s_waitcnt vmcnt(5)
buffer_load_dwordx4 v239, s[20:23], s80 offen offset:3072 nt lds
EX 167
v_mfma_f32_16x16x32_bf16 v[152:155], a[216:219], v[92:95], v[116:119]
s_waitcnt vmcnt(20)
s_barrier
RB 216,223,8192
CV 186,164,165
MS 152,208,88
EX 168
MS 152,200,84
RB 208,222,8192
EX 169
MS 152,192,80
s_add_u32 m0, s89, 20480
CV 187,166,167
EX 170
v_mfma_f32_16x16x32_bf16 v[156:159], a[196:199], v[80:83], v[116:119]
s_add_u32 s80, s90, 65536
RB 200,221,8192
EX 171
MS 156,204,84
s_waitcnt vmcnt(5)
buffer_load_dwordx4 v236, s[20:23], s80 offen offset:0 nt lds
CV 188,168,169
MS 156,212,88
RB 192,220,8192
EX 172
MS 156,220,92
EX 173
v_mfma_f32_16x16x32_bf16 v[148:151], a[220:223], v[76:79], v[112:115]
RB 220,223,12288
CV 189,170,171
MS 148,212,72
EX 174
EX 175
MS 148,204,68
RB 212,222,12288
CV 190,172,173
MS 148,196,64
s_waitcnt vmcnt(5)
buffer_load_dwordx4 v199, s[20:23], s80 offen offset:1024 nt lds
CV 191,174,175
MP 64,224,184
RB 204,221,12288
v_add_f32 v242, v242, v160
MP 96,224,188
v_add_f32 v243, v243, v168
v_add_f32 v242, v242, v161
v_add_f32 v243, v243, v169
MP 100,228,188
RB 196,220,12288
v_max3_f32 v246, v144, v145, v146
MP 68,228,184
v_max3_f32 v246, v246, v147, v148
v_max3_f32 v246, v246, v149, v150
v_max3_f32 v246, v246, v151, v152
MP 72,232,184
v_max3_f32 v246, v246, v153, v154
v_max3_f32 v246, v246, v155, v156
MP 104,232,188
s_waitcnt vmcnt(5)
buffer_load_dwordx4 v238, s[20:23], s80 offen offset:2048 nt lds
v_max3_f32 v246, v246, v157, v158
MP 108,236,188
v_max_f32 v246, v246, v159
v_cmp_lt_f32 vcc, s91, v246
MP 76,236,184
v_add_f32 v242, v242, v162
v_add_f32 v243, v243, v170
v_add_f32 v242, v242, v163
MP 80,240,184
s_cbranch_vccnz L_trL6c
L_backL6c:
v_add_f32 v243, v243, v171
v_add_f32 v242, v242, v164
MP 112,240,188
v_add_f32 v243, v243, v172
v_add_f32 v242, v242, v165
MP 116,244,188
EX 144
MP 84,244,184
s_waitcnt vmcnt(5)
buffer_load_dwordx4 v239, s[20:23], s80 offen offset:3072 nt lds
v_add_f32 v243, v243, v173
MP 88,248,184
v_add_f32 v242, v242, v166
v_add_f32 v243, v243, v174
MP 120,248,188
s_sub_u32 s90, s90, 39936
EX 145
EX 146
MP 124,252,188
s_and_b32 s90, s90, 39936
v_add_f32 v242, v242, v167
v_add_f32 v243, v243, v175
MP 92,252,184
s_waitcnt lgkmcnt(0)
EX 147
v_mfma_f32_16x16x32_bf16 v[160:163], a[192:195], v[0:3], v[96:99]
s_add_u32 m0, s89, 8192
CV 176,144,145
EX 148
MS 160,200,4
s_add_u32 s80, s90, 0
EX 149
MS 160,208,8
s_waitcnt vmcnt(5)
buffer_load_dwordx4 v232, s[16:19], s80 offen offset:0 nt lds
MS 160,216,12
CV 177,146,147
EX 150
v_mfma_f32_16x16x32_bf16 v[168:171], a[216:219], v[28:31], v[100:103]
EX 151
MS 168,208,24
CV 178,148,149
EX 152
MS 168,200,20
EX 153
MS 168,192,16
CV 179,150,151
EX 154
v_mfma_f32_16x16x32_bf16 v[172:175], a[196:199], v[16:19], v[100:103]
s_waitcnt vmcnt(5)
buffer_load_dwordx4 v233, s[16:19], s80 offen offset:1024 nt lds
MS 172,204,20
EX 155
MS 172,212,24
CV 180,152,153
EX 156
MS 172,220,28
EX 157
v_mfma_f32_16x16x32_bf16 v[164:167], a[220:223], v[12:15], v[96:99]
CV 181,154,155
EX 158
MS 164,212,8
EX 159
MS 164,204,4
s_waitcnt vmcnt(5)
buffer_load_dwordx4 v234, s[16:19], s80 offen offset:2048 nt lds
MS 164,196,0
CV 182,156,157
CV 183,158,159
MP 128,224,176
v_add_f32 v244, v244, v144
v_add_f32 v245, v245, v152
v_add_f32 v244, v244, v145
MP 160,224,180
v_add_f32 v245, v245, v153
v_add_f32 v244, v244, v146
v_add_f32 v245, v245, v154
MP 164,228,180
s_waitcnt vmcnt(19)
RT 224,224,24576
v_max3_f32 v246, v160, v161, v162
v_max3_f32 v246, v246, v163, v164
MP 132,228,176
RT 226,224,28672
v_max3_f32 v246, v246, v165, v166
v_max3_f32 v246, v246, v167, v168
MP 136,232,176
s_waitcnt vmcnt(5)
buffer_load_dwordx4 v235, s[16:19], s80 offen offset:3072 nt lds
v_max3_f32 v246, v246, v169, v170
MP 168,232,180
RT 228,225,24576
v_max3_f32 v246, v246, v171, v172
v_max3_f32 v246, v246, v173, v174
MP 172,236,180
RT 230,225,28672
v_max_f32 v246, v246, v175
v_cmp_lt_f32 vcc, s91, v246
MP 140,236,176
RT 232,226,24576
v_add_f32 v244, v244, v147
v_add_f32 v245, v245, v155
MP 144,240,176
s_cbranch_vccnz L_trL7a
L_backL7a:
s_add_u32 m0, s89, 12288
RT 234,226,28672
v_add_f32 v244, v244, v148
EX 160
MP 176,240,180
s_add_u32 s80, s90, 65536
v_add_f32 v245, v245, v156
v_add_f32 v244, v244, v149
MP 180,244,180
s_waitcnt vmcnt(5)
buffer_load_dwordx4 v232, s[16:19], s80 offen offset:0 nt lds
EX 161
MP 148,244,176
RT 236,227,24576
v_add_f32 v245, v245, v157
MP 152,248,176
RT 238,227,28672
v_add_f32 v244, v244, v150
EX 162
MP 184,248,180
RT 240,228,24576
v_add_f32 v245, v245, v158
MP 188,252,180
RT 242,228,28672
v_add_f32 v244, v244, v151
EX 163
MP 156,252,176
RT 244,229,24576
v_add_f32 v245, v245, v159
v_mfma_f32_16x16x32_bf16 v[144:147], a[192:195], v[32:35], v[104:107]
s_waitcnt vmcnt(5)
buffer_load_dwordx4 v233, s[16:19], s80 offen offset:1024 nt lds
CV 184,160,161
MS 144,200,36
RT 246,229,28672
EX 164
MS 144,208,40
RT 248,230,24576
EX 165
MS 144,216,44
RT 250,230,28672
CV 185,162,163
v_mfma_f32_16x16x32_bf16 v[152:155], a[216:219], v[60:63], v[108:111]
EX 166
MS 152,208,56
RT 252,231,24576
EX 167
MS 152,200,52
s_waitcnt vmcnt(5)
buffer_load_dwordx4 v234, s[16:19], s80 offen offset:2048 nt lds
CV 186,164,165
MS 152,192,48
RT 254,231,28672
EX 168
v_mfma_f32_16x16x32_bf16 v[156:159], a[196:199], v[48:51], v[108:111]
EX 169
MS 156,204,52
CV 187,166,167
EX 170
MS 156,212,56
EX 171
CV 188,168,169
MS 156,220,60
EX 172
v_mfma_f32_16x16x32_bf16 v[148:151], a[220:223], v[44:47], v[104:107]
s_waitcnt vmcnt(5)
buffer_load_dwordx4 v235, s[16:19], s80 offen offset:3072 nt lds
MS 148,212,40
EX 173
CV 189,170,171
MS 148,204,36
EX 174
EX 175
MS 148,196,32
s_waitcnt lgkmcnt(0)
CV 190,172,173
CV 191,174,175
MP 0,224,184
s_add_u32 m0, s89, 24576
v_add_f32 v240, v240, v160
v_add_f32 v241, v241, v168
MP 32,224,188
s_add_u32 s80, s90, 0
v_add_f32 v240, v240, v161
v_add_f32 v241, v241, v169
v_max3_f32 v246, v144, v145, v146
MP 36,228,188
s_waitcnt vmcnt(5)
buffer_load_dwordx4 v236, s[20:23], s80 offen offset:0 nt lds
MP 4,228,184
v_max3_f32 v246, v246, v147, v148
v_max3_f32 v246, v246, v149, v150
v_max3_f32 v246, v246, v151, v152
MP 8,232,184
v_max3_f32 v246, v246, v153, v154
v_max3_f32 v246, v246, v155, v156
MP 40,232,188
v_max3_f32 v246, v246, v157, v158
v_max_f32 v246, v246, v159
v_cmp_lt_f32 vcc, s91, v246
MP 44,236,188
v_add_f32 v240, v240, v162
v_add_f32 v241, v241, v170
MP 12,236,184
s_cbranch_vccnz L_trL7b
L_backL7b:
v_add_f32 v240, v240, v163
v_add_f32 v241, v241, v171
MP 16,240,184
s_waitcnt vmcnt(5)
buffer_load_dwordx4 v199, s[20:23], s80 offen offset:1024 nt lds
v_add_f32 v240, v240, v164
MP 48,240,188
v_add_f32 v241, v241, v172
v_add_f32 v240, v240, v165
MP 52,244,188
EX 144
MP 20,244,184
v_add_f32 v241, v241, v173
v_add_f32 v240, v240, v166
EX 145
MP 24,248,184
EX 146
MP 56,248,188
v_add_f32 v241, v241, v174
v_add_f32 v240, v240, v167
MP 60,252,188
s_waitcnt vmcnt(5)
buffer_load_dwordx4 v238, s[20:23], s80 offen offset:2048 nt lds
MP 28,252,184
v_add_f32 v241, v241, v175
EX 147
v_mfma_f32_16x16x32_bf16 v[160:163], a[192:195], v[64:67], v[112:115]
CV 176,144,145
EX 148
MS 160,200,68
EX 149
CV 177,146,147
MS 160,208,72
EX 150
MS 160,216,76
s_waitcnt vmcnt(5)
buffer_load_dwordx4 v239, s[20:23], s80 offen offset:3072 nt lds
EX 151
v_mfma_f32_16x16x32_bf16 v[168:171], a[216:219], v[92:95], v[116:119]
s_waitcnt vmcnt(20)
s_barrier
RB 216,223,0
CV 178,148,149
MS 168,208,88
EX 152
MS 168,200,84
RB 208,222,0
EX 153
MS 168,192,80
s_add_u32 m0, s89, 28672
CV 179,150,151
EX 154
v_mfma_f32_16x16x32_bf16 v[172:175], a[196:199], v[80:83], v[116:119]
s_add_u32 s80, s90, 65536
RB 200,221,0
EX 155
MS 172,204,84
s_waitcnt vmcnt(5)
buffer_load_dwordx4 v236, s[20:23], s80 offen offset:0 nt lds
CV 180,152,153
MS 172,212,88
RB 192,220,0
EX 156
MS 172,220,92
EX 157
v_mfma_f32_16x16x32_bf16 v[164:167], a[220:223], v[76:79], v[112:115]
RB 220,223,4096
CV 181,154,155
MS 164,212,72
EX 158
EX 159
MS 164,204,68
RB 212,222,4096
CV 182,156,157
MS 164,196,64
s_waitcnt vmcnt(5)
buffer_load_dwordx4 v199, s[20:23], s80 offen offset:1024 nt lds
CV 183,158,159
MP 64,224,176
RB 204,221,4096
v_add_f32 v242, v242, v144
MP 96,224,180
v_add_f32 v243, v243, v152
v_add_f32 v242, v242, v145
v_add_f32 v243, v243, v153
MP 100,228,180
RB 196,220,4096
v_max3_f32 v246, v160, v161, v162
MP 68,228,176
v_max3_f32 v246, v246, v163, v164
v_max3_f32 v246, v246, v165, v166
v_max3_f32 v246, v246, v167, v168
MP 72,232,176
v_max3_f32 v246, v246, v169, v170
v_max3_f32 v246, v246, v171, v172
MP 104,232,180
s_waitcnt vmcnt(5)
buffer_load_dwordx4 v238, s[20:23], s80 offen offset:2048 nt lds
v_max3_f32 v246, v246, v173, v174
MP 108,236,180
v_max_f32 v246, v246, v175
v_cmp_lt_f32 vcc, s91, v246
MP 76,236,176
v_add_f32 v242, v242, v146
v_add_f32 v243, v243, v154
v_add_f32 v242, v242, v147
MP 80,240,176
s_cbranch_vccnz L_trL7c
L_backL7c:
v_add_f32 v243, v243, v155
v_add_f32 v242, v242, v148
MP 112,240,180
v_add_f32 v243, v243, v156
v_add_f32 v242, v242, v149
MP 116,244,180
EX 160
MP 84,244,176
s_waitcnt vmcnt(5)
buffer_load_dwordx4 v239, s[20:23], s80 offen offset:3072 nt lds
v_add_f32 v243, v243, v157
MP 88,248,176
v_add_f32 v242, v242, v150
v_add_f32 v243, v243, v158
MP 120,248,180
s_sub_u32 s90, s90, 39936
EX 161
EX 162
MP 124,252,180
s_and_b32 s90, s90, 39936
v_add_f32 v242, v242, v151
v_add_f32 v243, v243, v159
MP 92,252,176
s_waitcnt lgkmcnt(0)
EX 163
s_add_u32 s79, s79, 1
s_cmp_lt_u32 s79, 4
s_cbranch_scc1 L_aloop
L_c2: ; ---- attention tiles 14, 15, normalisation, o stores
v_mfma_f32_16x16x32_bf16 v[144:147], a[192:195], v[0:3], v[96:99]
CV 184,160,161
EX 164
MS 144,200,4
EX 165
MS 144,208,8
CV 185,162,163
EX 166
MS 144,216,12
EX 167
v_mfma_f32_16x16x32_bf16 v[152:155], a[216:219], v[28:31], v[100:103]
CV 186,164,165
EX 168
MS 152,208,24
EX 169
MS 152,200,20
CV 187,166,167
EX 170
MS 152,192,16
EX 171
v_mfma_f32_16x16x32_bf16 v[156:159], a[196:199], v[16:19], v[100:103]
CV 188,168,169
EX 172
MS 156,204,20
EX 173
MS 156,212,24
CV 189,170,171
EX 174
MS 156,220,28
EX 175
v_mfma_f32_16x16x32_bf16 v[148:151], a[220:223], v[12:15], v[96:99]
CV 190,172,173
CV 191,174,175
MS 148,212,8
MS 148,204,4
MS 148,196,0
MP 128,224,184
v_add_f32 v244, v244, v160
v_add_f32 v245, v245, v168
v_add_f32 v244, v244, v161
MP 160,224,188
v_add_f32 v245, v245, v169
v_add_f32 v244, v244, v162
v_add_f32 v245, v245, v170
MP 164,228,188
s_waitcnt vmcnt(16)
RT 224,224,16384
v_max3_f32 v246, v144, v145, v146
MP 132,228,184
RT 226,224,20480
v_max3_f32 v246, v246, v147, v148
v_max3_f32 v246, v246, v149, v150
MP 136,232,184
RT 228,225,16384
v_max3_f32 v246, v246, v151, v152
v_max3_f32 v246, v246, v153, v154
MP 168,232,188
v_max3_f32 v246, v246, v155, v156
v_max3_f32 v246, v246, v157, v158
v_max_f32 v246, v246, v159
MP 172,236,188
RT 230,225,20480
v_cmp_lt_f32 vcc, s91, v246
MP 140,236,184
RT 232,226,16384
v_add_f32 v244, v244, v163
v_add_f32 v245, v245, v171
MP 144,240,184
s_cbranch_vccnz L_tr14a
L_back14a:
RT 234,226,20480
v_add_f32 v244, v244, v164
MP 176,240,188
v_add_f32 v245, v245, v172
v_add_f32 v244, v244, v165
EX 144
MP 180,244,188
RT 236,227,16384
v_add_f32 v245, v245, v173
MP 148,244,184
RT 238,227,20480
v_add_f32 v244, v244, v166
EX 145
MP 152,248,184
EX 146
MP 184,248,188
RT 240,228,16384
v_add_f32 v245, v245, v174
MP 188,252,188
RT 242,228,20480
v_add_f32 v244, v244, v167
EX 147
MP 156,252,184
RT 244,229,16384
v_add_f32 v245, v245, v175
v_mfma_f32_16x16x32_bf16 v[160:163], a[192:195], v[32:35], v[104:107]
CV 176,144,145
EX 148
MS 160,200,36
RT 246,229,20480
EX 149
MS 160,208,40
RT 248,230,16384
CV 177,146,147
MS 160,216,44
RT 250,230,20480
EX 150
v_mfma_f32_16x16x32_bf16 v[168:171], a[216:219], v[60:63], v[108:111]
EX 151
MS 168,208,56
RT 252,231,16384
CV 178,148,149
MS 168,200,52
RT 254,231,20480
EX 152
MS 168,192,48
EX 153
v_mfma_f32_16x16x32_bf16 v[172:175], a[196:199], v[48:51], v[108:111]
CV 179,150,151
EX 154
MS 172,204,52
EX 155
MS 172,212,56
CV 180,152,153
EX 156
MS 172,220,60
EX 157
v_mfma_f32_16x16x32_bf16 v[164:167], a[220:223], v[44:47], v[104:107]
CV 181,154,155
EX 158
MS 164,212,40
EX 159
MS 164,204,36
CV 182,156,157
CV 183,158,159
MS 164,196,32
s_waitcnt lgkmcnt(0)
MP 0,224,176
s_bfe_u32 s93, s78, 0x4001c
s_lshl1_add_u32 s93, s93, s99
v_add_f32 v240, v240, v144
v_add_f32 v241, v241, v152
MP 32,224,180
s_add_u32 m0, s97, 32768
s_lshl_b32 s81, s93, 16
v_max3_f32 v246, v160, v161, v162
v_max3_f32 v246, v246, v163, v164
v_max3_f32 v246, v246, v165, v166
MP 36,228,180
DM 216,32,81,0
MP 4,228,176
v_max3_f32 v246, v246, v167, v168
v_max3_f32 v246, v246, v169, v170
v_max3_f32 v246, v246, v171, v172
MP 8,232,176
v_max3_f32 v246, v246, v173, v174
v_max_f32 v246, v246, v175
MP 40,232,180
v_cmp_lt_f32 vcc, s91, v246
v_add_f32 v240, v240, v145
v_add_f32 v241, v241, v153
MP 44,236,180
v_add_f32 v240, v240, v146
v_add_f32 v241, v241, v154
MP 12,236,176
s_cbranch_vccnz L_tr14b
L_back14b:
v_add_f32 v240, v240, v147
v_add_f32 v241, v241, v155
MP 16,240,176
DM 217,32,81,1024
v_add_f32 v240, v240, v148
MP 48,240,180
v_add_f32 v241, v241, v156
v_add_f32 v240, v240, v149
MP 52,244,180
EX 160
MP 20,244,176
v_add_f32 v241, v241, v157
v_add_f32 v240, v240, v150
EX 161
MP 24,248,176
EX 162
MP 56,248,180
v_add_f32 v241, v241, v158
v_add_f32 v240, v240, v151
MP 60,252,180
DM 218,32,81,2048
MP 28,252,176
v_add_f32 v241, v241, v159
EX 163
v_mfma_f32_16x16x32_bf16 v[144:147], a[192:195], v[64:67], v[112:115]
CV 184,160,161
EX 164
MS 144,200,68
EX 165
CV 185,162,163
MS 144,208,72
EX 166
MS 144,216,76
DM 219,32,81,3072
EX 167
v_mfma_f32_16x16x32_bf16 v[152:155], a[216:219], v[92:95], v[116:119]
s_waitcnt vmcnt(12)
s_barrier
RB 216,223,8192
CV 186,164,165
MS 152,208,88
EX 168
MS 152,200,84
s_bfe_u32 s93, s78, 0x4001c
RB 208,222,8192
EX 169
MS 152,192,80
s_lshl1_add_u32 s93, s93, s99
s_xor_b32 s93, s93, 1
CV 187,166,167
EX 170
v_mfma_f32_16x16x32_bf16 v[156:159], a[196:199], v[80:83], v[116:119]
s_add_u32 m0, s97, 73728
s_lshl_b32 s81, s93, 16
RB 200,221,8192
EX 171
MS 156,204,84
DM 216,32,81,0
CV 188,168,169
MS 156,212,88
RB 192,220,8192
EX 172
MS 156,220,92
EX 173
v_mfma_f32_16x16x32_bf16 v[148:151], a[220:223], v[76:79], v[112:115]
RB 220,223,12288
CV 189,170,171
MS 148,212,72
EX 174
EX 175
MS 148,204,68
RB 212,222,12288
CV 190,172,173
MS 148,196,64
DM 217,32,81,1024
CV 191,174,175
MP 64,224,184
RB 204,221,12288
v_add_f32 v242, v242, v160
MP 96,224,188
v_add_f32 v243, v243, v168
v_add_f32 v242, v242, v161
v_add_f32 v243, v243, v169
MP 100,228,188
RB 196,220,12288
v_max3_f32 v246, v144, v145, v146
MP 68,228,184
v_max3_f32 v246, v246, v147, v148
v_max3_f32 v246, v246, v149, v150
v_max3_f32 v246, v246, v151, v152
MP 72,232,184
v_max3_f32 v246, v246, v153, v154
v_max3_f32 v246, v246, v155, v156
MP 104,232,188
DM 218,32,81,2048
v_max3_f32 v246, v246, v157, v158
MP 108,236,188
v_max_f32 v246, v246, v159
v_cmp_lt_f32 vcc, s91, v246
MP 76,236,184
v_add_f32 v242, v242, v162
v_add_f32 v243, v243, v170
v_add_f32 v242, v242, v163
MP 80,240,184
s_cbranch_vccnz L_tr14c
L_back14c:
v_add_f32 v243, v243, v171
v_add_f32 v242, v242, v164
MP 112,240,188
v_add_f32 v243, v243, v172
v_add_f32 v242, v242, v165
MP 116,244,188
EX 144
MP 84,244,184
DM 219,32,81,3072
v_add_f32 v243, v243, v173
MP 88,248,184
v_add_f32 v242, v242, v166
v_add_f32 v243, v243, v174
MP 120,248,188
EX 145
EX 146
MP 124,252,188
v_add_f32 v242, v242, v167
v_add_f32 v243, v243, v175
MP 92,252,184
s_waitcnt lgkmcnt(0)
EX 147
v_mfma_f32_16x16x32_bf16 v[160:163], a[192:195], v[0:3], v[96:99]
CV 176,144,145
EX 148
MS 160,200,4
EX 149
MS 160,208,8
CV 177,146,147
EX 150
MS 160,216,12
EX 151
v_mfma_f32_16x16x32_bf16 v[168:171], a[216:219], v[28:31], v[100:103]
CV 178,148,149
EX 152
MS 168,208,24
EX 153
MS 168,200,20
CV 179,150,151
EX 154
MS 168,192,16
EX 155
v_mfma_f32_16x16x32_bf16 v[172:175], a[196:199], v[16:19], v[100:103]
CV 180,152,153
EX 156
MS 172,204,20
EX 157
MS 172,212,24
CV 181,154,155
EX 158
MS 172,220,28
EX 159
v_mfma_f32_16x16x32_bf16 v[164:167], a[220:223], v[12:15], v[96:99]
CV 182,156,157
CV 183,158,159
MS 164,212,8
MS 164,204,4
MS 164,196,0
MP 128,224,176
v_add_f32 v244, v244, v144
v_add_f32 v245, v245, v152
v_add_f32 v244, v244, v145
MP 160,224,180
v_add_f32 v245, v245, v153
v_add_f32 v244, v244, v146
v_add_f32 v245, v245, v154
MP 164,228,180
s_waitcnt vmcnt(8)
RT 224,224,24576
v_max3_f32 v246, v160, v161, v162
MP 132,228,176
RT 226,224,28672
v_max3_f32 v246, v246, v163, v164
v_max3_f32 v246, v246, v165, v166
MP 136,232,176
RT 228,225,24576
)ASM"
R"ASM(v_max3_f32 v246, v246, v167, v168
v_max3_f32 v246, v246, v169, v170
MP 168,232,180
v_max3_f32 v246, v246, v171, v172
v_max3_f32 v246, v246, v173, v174
v_max_f32 v246, v246, v175
MP 172,236,180
RT 230,225,28672
v_cmp_lt_f32 vcc, s91, v246
MP 140,236,176
RT 232,226,24576
v_add_f32 v244, v244, v147
v_add_f32 v245, v245, v155
MP 144,240,176
s_cbranch_vccnz L_tr15a
L_back15a:
RT 234,226,28672
v_add_f32 v244, v244, v148
MP 176,240,180
v_add_f32 v245, v245, v156
v_add_f32 v244, v244, v149
EX 160
MP 180,244,180
RT 236,227,24576
v_add_f32 v245, v245, v157
MP 148,244,176
RT 238,227,28672
v_add_f32 v244, v244, v150
EX 161
MP 152,248,176
EX 162
MP 184,248,180
RT 240,228,24576
v_add_f32 v245, v245, v158
MP 188,252,180
RT 242,228,28672
v_add_f32 v244, v244, v151
EX 163
MP 156,252,176
RT 244,229,24576
v_add_f32 v245, v245, v159
v_mfma_f32_16x16x32_bf16 v[144:147], a[192:195], v[32:35], v[104:107]
CV 184,160,161
EX 164
MS 144,200,36
RT 246,229,28672
EX 165
MS 144,208,40
RT 248,230,24576
CV 185,162,163
MS 144,216,44
RT 250,230,28672
EX 166
v_mfma_f32_16x16x32_bf16 v[152:155], a[216:219], v[60:63], v[108:111]
EX 167
MS 152,208,56
RT 252,231,24576
CV 186,164,165
MS 152,200,52
RT 254,231,28672
EX 168
MS 152,192,48
EX 169
v_mfma_f32_16x16x32_bf16 v[156:159], a[196:199], v[48:51], v[108:111]
CV 187,166,167
EX 170
MS 156,204,52
EX 171
MS 156,212,56
CV 188,168,169
EX 172
MS 156,220,60
EX 173
v_mfma_f32_16x16x32_bf16 v[148:151], a[220:223], v[44:47], v[104:107]
CV 189,170,171
EX 174
MS 148,212,40
buffer_load_dword v215, off, s[40:43], s77 offset:2816 sc1
MS 148,204,36
EX 175
MS 148,196,32
s_waitcnt lgkmcnt(0)
CV 190,172,173
CV 191,174,175
MP 0,224,184
v_add_f32 v240, v240, v160
v_add_f32 v241, v241, v168
MP 32,224,188
v_add_f32 v240, v240, v161
v_add_f32 v241, v241, v169
MP 36,228,188
v_add_f32 v240, v240, v162
v_max3_f32 v246, v144, v145, v146
MP 4,228,184
v_max3_f32 v246, v246, v147, v148
v_max3_f32 v246, v246, v149, v150
MP 8,232,184
v_max3_f32 v246, v246, v151, v152
v_max3_f32 v246, v246, v153, v154
MP 40,232,188
v_max3_f32 v246, v246, v155, v156
v_max3_f32 v246, v246, v157, v158
MP 44,236,188
v_max_f32 v246, v246, v159
v_cmp_lt_f32 vcc, s91, v246
MP 12,236,184
v_add_f32 v241, v241, v170
v_add_f32 v240, v240, v163
v_add_f32 v241, v241, v171
MP 16,240,184
s_cbranch_vccnz L_tr15b
L_back15b:
v_add_f32 v240, v240, v164
v_add_f32 v241, v241, v172
MP 48,240,188
EX 144
MP 52,244,188
v_add_f32 v240, v240, v165
v_add_f32 v241, v241, v173
MP 20,244,184
EX 145
MP 24,248,184
v_add_f32 v240, v240, v166
v_add_f32 v241, v241, v174
MP 56,248,188
EX 146
MP 60,252,188
v_add_f32 v240, v240, v167
v_add_f32 v241, v241, v175
MP 28,252,184
EX 147
v_mfma_f32_16x16x32_bf16 v[160:163], a[192:195], v[64:67], v[112:115]
v_mov_b32 v247, v240
s_nop 1
v_permlane16_swap_b32 v240, v247
v_add_f32 v240, v240, v247
v_mov_b32 v247, v240
s_nop 1
v_permlane32_swap_b32 v240, v247
v_add_f32 v240, v240, v247
v_rcp_f32 v0, v240
v_mov_b32 v247, v241
s_nop 1
v_permlane16_swap_b32 v241, v247
v_add_f32 v241, v241, v247
v_mov_b32 v247, v241
s_nop 1
v_permlane32_swap_b32 v241, v247
v_add_f32 v241, v241, v247
v_rcp_f32 v2, v241
MS 160,200,68
s_mov_b32 s80, 0
v_accvgpr_read_b32 v12, a0
v_accvgpr_read_b32 v13, a1
v_accvgpr_read_b32 v14, a2
v_accvgpr_read_b32 v15, a3
v_accvgpr_read_b32 v16, a4
v_accvgpr_read_b32 v17, a5
v_accvgpr_read_b32 v18, a6
v_accvgpr_read_b32 v19, a7
v_mul_f32 v12, v12, v0
v_mul_f32 v13, v13, v0
v_mul_f32 v14, v14, v0
v_mul_f32 v15, v15, v0
v_mul_f32 v16, v16, v0
v_mul_f32 v17, v17, v0
v_mul_f32 v18, v18, v0
v_mul_f32 v19, v19, v0
CV 20,12,13
CV 21,14,15
CV 22,16,17
CV 23,18,19
MS 160,208,72
buffer_store_dwordx4 v[20:23], v237, s[24:27], s80 offen offset:0 sc1
MS 160,216,76
CV 176,144,145
EX 148
v_mfma_f32_16x16x32_bf16 v[168:171], a[216:219], v[92:95], v[116:119]
EX 149
MS 168,208,88
v_accvgpr_read_b32 v24, a32
v_accvgpr_read_b32 v25, a33
v_accvgpr_read_b32 v26, a34
v_accvgpr_read_b32 v27, a35
v_accvgpr_read_b32 v28, a36
v_accvgpr_read_b32 v29, a37
v_accvgpr_read_b32 v30, a38
v_accvgpr_read_b32 v31, a39
v_mul_f32 v24, v24, v2
v_mul_f32 v25, v25, v2
v_mul_f32 v26, v26, v2
v_mul_f32 v27, v27, v2
v_mul_f32 v28, v28, v2
v_mul_f32 v29, v29, v2
v_mul_f32 v30, v30, v2
v_mul_f32 v31, v31, v2
CV 32,24,25
CV 33,26,27
CV 34,28,29
CV 35,30,31
MS 168,200,84
buffer_store_dwordx4 v[32:35], v237, s[24:27], s80 offen offset:1024 sc1
MS 168,192,80
CV 177,146,147
EX 150
v_mfma_f32_16x16x32_bf16 v[172:175], a[196:199], v[80:83], v[116:119]
EX 151
CV 178,148,149
EX 152
EX 153
CV 179,150,151
MS 172,204,84
s_mov_b32 s80, 6144
v_accvgpr_read_b32 v36, a8
v_accvgpr_read_b32 v37, a9
v_accvgpr_read_b32 v38, a10
v_accvgpr_read_b32 v39, a11
v_accvgpr_read_b32 v40, a12
v_accvgpr_read_b32 v41, a13
v_accvgpr_read_b32 v42, a14
v_accvgpr_read_b32 v43, a15
v_mul_f32 v36, v36, v0
v_mul_f32 v37, v37, v0
v_mul_f32 v38, v38, v0
v_mul_f32 v39, v39, v0
v_mul_f32 v40, v40, v0
v_mul_f32 v41, v41, v0
v_mul_f32 v42, v42, v0
v_mul_f32 v43, v43, v0
CV 44,36,37
CV 45,38,39
CV 46,40,41
CV 47,42,43
MS 172,212,88
buffer_store_dwordx4 v[44:47], v237, s[24:27], s80 offen offset:0 sc1
MS 172,220,92
EX 154
EX 155
v_mfma_f32_16x16x32_bf16 v[164:167], a[220:223], v[76:79], v[112:115]
CV 180,152,153
EX 156
EX 157
CV 181,154,155
EX 158
MS 164,212,72
v_accvgpr_read_b32 v48, a40
v_accvgpr_read_b32 v49, a41
v_accvgpr_read_b32 v50, a42
v_accvgpr_read_b32 v51, a43
v_accvgpr_read_b32 v52, a44
v_accvgpr_read_b32 v53, a45
v_accvgpr_read_b32 v54, a46
v_accvgpr_read_b32 v55, a47
v_mul_f32 v48, v48, v2
v_mul_f32 v49, v49, v2
v_mul_f32 v50, v50, v2
v_mul_f32 v51, v51, v2
v_mul_f32 v52, v52, v2
v_mul_f32 v53, v53, v2
v_mul_f32 v54, v54, v2
v_mul_f32 v55, v55, v2
CV 56,48,49
CV 57,50,51
CV 58,52,53
CV 59,54,55
MS 164,204,68
buffer_store_dwordx4 v[56:59], v237, s[24:27], s80 offen offset:1024 sc1
MS 164,196,64
EX 159
CV 182,156,157
CV 183,158,159
MP 64,224,176
v_add_f32 v242, v242, v144
v_add_f32 v243, v243, v152
v_add_f32 v242, v242, v145
v_add_f32 v243, v243, v153
v_add_f32 v242, v242, v146
v_add_f32 v243, v243, v154
v_add_f32 v242, v242, v147
v_max3_f32 v246, v160, v161, v162
MP 96,224,180
s_mov_b32 s80, 12288
v_accvgpr_read_b32 v12, a16
v_accvgpr_read_b32 v13, a17
v_accvgpr_read_b32 v14, a18
v_accvgpr_read_b32 v15, a19
v_accvgpr_read_b32 v16, a20
v_accvgpr_read_b32 v17, a21
v_accvgpr_read_b32 v18, a22
v_accvgpr_read_b32 v19, a23
v_mul_f32 v12, v12, v0
v_mul_f32 v13, v13, v0
v_mul_f32 v14, v14, v0
v_mul_f32 v15, v15, v0
v_mul_f32 v16, v16, v0
v_mul_f32 v17, v17, v0
v_mul_f32 v18, v18, v0
v_mul_f32 v19, v19, v0
CV 20,12,13
CV 21,14,15
CV 22,16,17
CV 23,18,19
MP 100,228,180
buffer_store_dwordx4 v[20:23], v237, s[24:27], s80 offen offset:0 sc1
MP 68,228,176
v_max3_f32 v246, v246, v163, v164
v_max3_f32 v246, v246, v165, v166
MP 72,232,176
v_max3_f32 v246, v246, v167, v168
v_max3_f32 v246, v246, v169, v170
v_max3_f32 v246, v246, v171, v172
v_max3_f32 v246, v246, v173, v174
v_max_f32 v246, v246, v175
v_cmp_lt_f32 vcc, s91, v246
MP 104,232,180
v_accvgpr_read_b32 v24, a48
v_accvgpr_read_b32 v25, a49
v_accvgpr_read_b32 v26, a50
v_accvgpr_read_b32 v27, a51
v_accvgpr_read_b32 v28, a52
v_accvgpr_read_b32 v29, a53
v_accvgpr_read_b32 v30, a54
v_accvgpr_read_b32 v31, a55
v_mul_f32 v24, v24, v2
v_mul_f32 v25, v25, v2
v_mul_f32 v26, v26, v2
v_mul_f32 v27, v27, v2
v_mul_f32 v28, v28, v2
v_mul_f32 v29, v29, v2
v_mul_f32 v30, v30, v2
v_mul_f32 v31, v31, v2
CV 32,24,25
CV 33,26,27
CV 34,28,29
CV 35,30,31
MP 108,236,180
s_cbranch_vccnz L_tr15c
L_back15c:
buffer_store_dwordx4 v[32:35], v237, s[24:27], s80 offen offset:1024 sc1
MP 76,236,176
v_add_f32 v243, v243, v155
v_add_f32 v242, v242, v148
MP 80,240,176
EX 160
EX 161
EX 162
MP 112,240,180
s_mov_b32 s80, 18432
v_accvgpr_read_b32 v36, a24
v_accvgpr_read_b32 v37, a25
v_accvgpr_read_b32 v38, a26
v_accvgpr_read_b32 v39, a27
v_accvgpr_read_b32 v40, a28
v_accvgpr_read_b32 v41, a29
v_accvgpr_read_b32 v42, a30
v_accvgpr_read_b32 v43, a31
v_mul_f32 v36, v36, v0
v_mul_f32 v37, v37, v0
v_mul_f32 v38, v38, v0
v_mul_f32 v39, v39, v0
v_mul_f32 v40, v40, v0
v_mul_f32 v41, v41, v0
v_mul_f32 v42, v42, v0
v_mul_f32 v43, v43, v0
CV 44,36,37
CV 45,38,39
CV 46,40,41
CV 47,42,43
MP 116,244,180
buffer_store_dwordx4 v[44:47], v237, s[24:27], s80 offen offset:0 sc1
MP 84,244,176
v_add_f32 v243, v243, v156
v_add_f32 v242, v242, v149
MP 88,248,176
v_add_f32 v243, v243, v157
v_add_f32 v242, v242, v150
EX 163
v_add_f32 v243, v243, v158
MP 120,248,180
v_accvgpr_read_b32 v48, a56
v_accvgpr_read_b32 v49, a57
v_accvgpr_read_b32 v50, a58
v_accvgpr_read_b32 v51, a59
v_accvgpr_read_b32 v52, a60
v_accvgpr_read_b32 v53, a61
v_accvgpr_read_b32 v54, a62
v_accvgpr_read_b32 v55, a63
v_mul_f32 v48, v48, v2
v_mul_f32 v49, v49, v2
v_mul_f32 v50, v50, v2
v_mul_f32 v51, v51, v2
v_mul_f32 v52, v52, v2
v_mul_f32 v53, v53, v2
v_mul_f32 v54, v54, v2
v_mul_f32 v55, v55, v2
CV 56,48,49
CV 57,50,51
CV 58,52,53
CV 59,54,55
MP 124,252,180
buffer_store_dwordx4 v[56:59], v237, s[24:27], s80 offen offset:1024 sc1
MP 92,252,176
v_add_f32 v242, v242, v151
v_add_f32 v243, v243, v159
v_mov_b32 v247, v242
s_nop 1
v_permlane16_swap_b32 v242, v247
v_add_f32 v242, v242, v247
v_mov_b32 v247, v242
s_nop 1
v_permlane32_swap_b32 v242, v247
v_add_f32 v242, v242, v247
v_rcp_f32 v4, v242
v_mov_b32 v247, v243
s_nop 1
v_permlane16_swap_b32 v243, v247
v_add_f32 v243, v243, v247
v_mov_b32 v247, v243
s_nop 1
v_permlane32_swap_b32 v243, v247
v_add_f32 v243, v243, v247
v_rcp_f32 v6, v243
s_mov_b32 s80, 2048
v_accvgpr_read_b32 v12, a64
v_accvgpr_read_b32 v13, a65
v_accvgpr_read_b32 v14, a66
v_accvgpr_read_b32 v15, a67
v_accvgpr_read_b32 v16, a68
v_accvgpr_read_b32 v17, a69
v_accvgpr_read_b32 v18, a70
v_accvgpr_read_b32 v19, a71
v_mul_f32 v12, v12, v4
v_mul_f32 v13, v13, v4
v_mul_f32 v14, v14, v4
v_mul_f32 v15, v15, v4
v_mul_f32 v16, v16, v4
v_mul_f32 v17, v17, v4
v_mul_f32 v18, v18, v4
v_mul_f32 v19, v19, v4
CV 20,12,13
CV 21,14,15
CV 22,16,17
CV 23,18,19
buffer_store_dwordx4 v[20:23], v237, s[24:27], s80 offen offset:0 sc1
CV 184,160,161
EX 164
EX 165
v_accvgpr_read_b32 v24, a96
v_accvgpr_read_b32 v25, a97
v_accvgpr_read_b32 v26, a98
v_accvgpr_read_b32 v27, a99
v_accvgpr_read_b32 v28, a100
v_accvgpr_read_b32 v29, a101
v_accvgpr_read_b32 v30, a102
v_accvgpr_read_b32 v31, a103
v_mul_f32 v24, v24, v6
v_mul_f32 v25, v25, v6
v_mul_f32 v26, v26, v6
v_mul_f32 v27, v27, v6
v_mul_f32 v28, v28, v6
v_mul_f32 v29, v29, v6
v_mul_f32 v30, v30, v6
v_mul_f32 v31, v31, v6
CV 32,24,25
CV 33,26,27
CV 34,28,29
CV 35,30,31
buffer_store_dwordx4 v[32:35], v237, s[24:27], s80 offen offset:1024 sc1
CV 185,162,163
EX 166
EX 167
CV 186,164,165
EX 168
EX 169
CV 187,166,167
s_mov_b32 s80, 8192
v_accvgpr_read_b32 v36, a72
v_accvgpr_read_b32 v37, a73
v_accvgpr_read_b32 v38, a74
v_accvgpr_read_b32 v39, a75
v_accvgpr_read_b32 v40, a76
v_accvgpr_read_b32 v41, a77
v_accvgpr_read_b32 v42, a78
v_accvgpr_read_b32 v43, a79
v_mul_f32 v36, v36, v4
v_mul_f32 v37, v37, v4
v_mul_f32 v38, v38, v4
v_mul_f32 v39, v39, v4
v_mul_f32 v40, v40, v4
v_mul_f32 v41, v41, v4
v_mul_f32 v42, v42, v4
v_mul_f32 v43, v43, v4
CV 44,36,37
CV 45,38,39
CV 46,40,41
CV 47,42,43
buffer_store_dwordx4 v[44:47], v237, s[24:27], s80 offen offset:0 sc1
EX 170
EX 171
CV 188,168,169
EX 172
EX 173
CV 189,170,171
EX 174
v_accvgpr_read_b32 v48, a104
v_accvgpr_read_b32 v49, a105
v_accvgpr_read_b32 v50, a106
v_accvgpr_read_b32 v51, a107
v_accvgpr_read_b32 v52, a108
v_accvgpr_read_b32 v53, a109
v_accvgpr_read_b32 v54, a110
v_accvgpr_read_b32 v55, a111
v_mul_f32 v48, v48, v6
v_mul_f32 v49, v49, v6
v_mul_f32 v50, v50, v6
v_mul_f32 v51, v51, v6
v_mul_f32 v52, v52, v6
v_mul_f32 v53, v53, v6
v_mul_f32 v54, v54, v6
v_mul_f32 v55, v55, v6
CV 56,48,49
CV 57,50,51
CV 58,52,53
CV 59,54,55
buffer_store_dwordx4 v[56:59], v237, s[24:27], s80 offen offset:1024 sc1
EX 175
CV 190,172,173
CV 191,174,175
MP 128,224,184
v_add_f32 v244, v244, v160
v_add_f32 v245, v245, v168
MP 160,224,188
s_mov_b32 s80, 14336
v_accvgpr_read_b32 v12, a80
v_accvgpr_read_b32 v13, a81
v_accvgpr_read_b32 v14, a82
v_accvgpr_read_b32 v15, a83
v_accvgpr_read_b32 v16, a84
v_accvgpr_read_b32 v17, a85
v_accvgpr_read_b32 v18, a86
v_accvgpr_read_b32 v19, a87
v_mul_f32 v12, v12, v4
v_mul_f32 v13, v13, v4
v_mul_f32 v14, v14, v4
v_mul_f32 v15, v15, v4
v_mul_f32 v16, v16, v4
v_mul_f32 v17, v17, v4
v_mul_f32 v18, v18, v4
v_mul_f32 v19, v19, v4
CV 20,12,13
CV 21,14,15
CV 22,16,17
CV 23,18,19
MP 164,228,188
buffer_store_dwordx4 v[20:23], v237, s[24:27], s80 offen offset:0 sc1
MP 132,228,184
v_add_f32 v244, v244, v161
v_add_f32 v245, v245, v169
MP 136,232,184
v_add_f32 v244, v244, v162
v_add_f32 v245, v245, v170
MP 168,232,188
v_accvgpr_read_b32 v24, a112
v_accvgpr_read_b32 v25, a113
v_accvgpr_read_b32 v26, a114
v_accvgpr_read_b32 v27, a115
v_accvgpr_read_b32 v28, a116
v_accvgpr_read_b32 v29, a117
v_accvgpr_read_b32 v30, a118
v_accvgpr_read_b32 v31, a119
v_mul_f32 v24, v24, v6
v_mul_f32 v25, v25, v6
v_mul_f32 v26, v26, v6
v_mul_f32 v27, v27, v6
v_mul_f32 v28, v28, v6
v_mul_f32 v29, v29, v6
v_mul_f32 v30, v30, v6
v_mul_f32 v31, v31, v6
CV 32,24,25
CV 33,26,27
CV 34,28,29
CV 35,30,31
MP 172,236,188
buffer_store_dwordx4 v[32:35], v237, s[24:27], s80 offen offset:1024 sc1
MP 140,236,184
v_add_f32 v244, v244, v163
v_add_f32 v245, v245, v171
MP 144,240,184
v_add_f32 v244, v244, v164
v_add_f32 v245, v245, v172
MP 176,240,188
s_mov_b32 s80, 20480
v_accvgpr_read_b32 v36, a88
v_accvgpr_read_b32 v37, a89
v_accvgpr_read_b32 v38, a90
v_accvgpr_read_b32 v39, a91
v_accvgpr_read_b32 v40, a92
v_accvgpr_read_b32 v41, a93
v_accvgpr_read_b32 v42, a94
v_accvgpr_read_b32 v43, a95
v_mul_f32 v36, v36, v4
v_mul_f32 v37, v37, v4
v_mul_f32 v38, v38, v4
v_mul_f32 v39, v39, v4
v_mul_f32 v40, v40, v4
v_mul_f32 v41, v41, v4
v_mul_f32 v42, v42, v4
v_mul_f32 v43, v43, v4
CV 44,36,37
CV 45,38,39
CV 46,40,41
CV 47,42,43
MP 180,244,188
buffer_store_dwordx4 v[44:47], v237, s[24:27], s80 offen offset:0 sc1
MP 148,244,184
v_add_f32 v244, v244, v165
v_add_f32 v245, v245, v173
MP 152,248,184
v_add_f32 v244, v244, v166
v_add_f32 v245, v245, v174
MP 184,248,188
v_accvgpr_read_b32 v48, a120
v_accvgpr_read_b32 v49, a121
v_accvgpr_read_b32 v50, a122
v_accvgpr_read_b32 v51, a123
v_accvgpr_read_b32 v52, a124
v_accvgpr_read_b32 v53, a125
v_accvgpr_read_b32 v54, a126
v_accvgpr_read_b32 v55, a127
v_mul_f32 v48, v48, v6
v_mul_f32 v49, v49, v6
v_mul_f32 v50, v50, v6
v_mul_f32 v51, v51, v6
v_mul_f32 v52, v52, v6
v_mul_f32 v53, v53, v6
v_mul_f32 v54, v54, v6
v_mul_f32 v55, v55, v6
CV 56,48,49
CV 57,50,51
CV 58,52,53
CV 59,54,55
MP 188,252,188
buffer_store_dwordx4 v[56:59], v237, s[24:27], s80 offen offset:1024 sc1
MP 156,252,184
v_add_f32 v244, v244, v167
v_add_f32 v245, v245, v175
v_mov_b32 v247, v244
s_nop 1
v_permlane16_swap_b32 v244, v247
v_add_f32 v244, v244, v247
v_mov_b32 v247, v244
s_nop 1
v_permlane32_swap_b32 v244, v247
v_add_f32 v244, v244, v247
v_rcp_f32 v8, v244
v_mov_b32 v247, v245
s_nop 1
v_permlane16_swap_b32 v245, v247
v_add_f32 v245, v245, v247
v_mov_b32 v247, v245
s_nop 1
v_permlane32_swap_b32 v245, v247
v_add_f32 v245, v245, v247
v_rcp_f32 v10, v245
s_mov_b32 s80, 4096
v_accvgpr_read_b32 v12, a128
v_accvgpr_read_b32 v13, a129
v_accvgpr_read_b32 v14, a130
v_accvgpr_read_b32 v15, a131
v_accvgpr_read_b32 v16, a132
v_accvgpr_read_b32 v17, a133
v_accvgpr_read_b32 v18, a134
v_accvgpr_read_b32 v19, a135
v_pk_mul_f32 v[12:13], v[12:13], v[8:9] op_sel_hi:[1,0]
v_pk_mul_f32 v[14:15], v[14:15], v[8:9] op_sel_hi:[1,0]
v_pk_mul_f32 v[16:17], v[16:17], v[8:9] op_sel_hi:[1,0]
v_pk_mul_f32 v[18:19], v[18:19], v[8:9] op_sel_hi:[1,0]
CV 20,12,13
CV 21,14,15
CV 22,16,17
CV 23,18,19
buffer_store_dwordx4 v[20:23], v237, s[24:27], s80 offen offset:0 sc1
v_accvgpr_read_b32 v24, a160
v_accvgpr_read_b32 v25, a161
v_accvgpr_read_b32 v26, a162
v_accvgpr_read_b32 v27, a163
v_accvgpr_read_b32 v28, a164
v_accvgpr_read_b32 v29, a165
v_accvgpr_read_b32 v30, a166
v_accvgpr_read_b32 v31, a167
v_pk_mul_f32 v[24:25], v[24:25], v[10:11] op_sel_hi:[1,0]
v_pk_mul_f32 v[26:27], v[26:27], v[10:11] op_sel_hi:[1,0]
v_pk_mul_f32 v[28:29], v[28:29], v[10:11] op_sel_hi:[1,0]
v_pk_mul_f32 v[30:31], v[30:31], v[10:11] op_sel_hi:[1,0]
CV 32,24,25
CV 33,26,27
CV 34,28,29
CV 35,30,31
buffer_store_dwordx4 v[32:35], v237, s[24:27], s80 offen offset:1024 sc1
s_mov_b32 s80, 10240
v_accvgpr_read_b32 v36, a136
v_accvgpr_read_b32 v37, a137
v_accvgpr_read_b32 v38, a138
v_accvgpr_read_b32 v39, a139
v_accvgpr_read_b32 v40, a140
v_accvgpr_read_b32 v41, a141
v_accvgpr_read_b32 v42, a142
v_accvgpr_read_b32 v43, a143
v_pk_mul_f32 v[36:37], v[36:37], v[8:9] op_sel_hi:[1,0]
v_pk_mul_f32 v[38:39], v[38:39], v[8:9] op_sel_hi:[1,0]
v_pk_mul_f32 v[40:41], v[40:41], v[8:9] op_sel_hi:[1,0]
v_pk_mul_f32 v[42:43], v[42:43], v[8:9] op_sel_hi:[1,0]
CV 44,36,37
CV 45,38,39
CV 46,40,41
CV 47,42,43
buffer_store_dwordx4 v[44:47], v237, s[24:27], s80 offen offset:0 sc1
v_accvgpr_read_b32 v48, a168
v_accvgpr_read_b32 v49, a169
v_accvgpr_read_b32 v50, a170
v_accvgpr_read_b32 v51, a171
v_accvgpr_read_b32 v52, a172
v_accvgpr_read_b32 v53, a173
v_accvgpr_read_b32 v54, a174
v_accvgpr_read_b32 v55, a175
v_pk_mul_f32 v[48:49], v[48:49], v[10:11] op_sel_hi:[1,0]
v_pk_mul_f32 v[50:51], v[50:51], v[10:11] op_sel_hi:[1,0]
v_pk_mul_f32 v[52:53], v[52:53], v[10:11] op_sel_hi:[1,0]
v_pk_mul_f32 v[54:55], v[54:55], v[10:11] op_sel_hi:[1,0]
CV 56,48,49
CV 57,50,51
CV 58,52,53
CV 59,54,55
buffer_store_dwordx4 v[56:59], v237, s[24:27], s80 offen offset:1024 sc1
s_mov_b32 s80, 16384
v_accvgpr_read_b32 v12, a144
v_accvgpr_read_b32 v13, a145
v_accvgpr_read_b32 v14, a146
v_accvgpr_read_b32 v15, a147
v_accvgpr_read_b32 v16, a148
v_accvgpr_read_b32 v17, a149
v_accvgpr_read_b32 v18, a150
v_accvgpr_read_b32 v19, a151
v_pk_mul_f32 v[12:13], v[12:13], v[8:9] op_sel_hi:[1,0]
v_pk_mul_f32 v[14:15], v[14:15], v[8:9] op_sel_hi:[1,0]
v_pk_mul_f32 v[16:17], v[16:17], v[8:9] op_sel_hi:[1,0]
v_pk_mul_f32 v[18:19], v[18:19], v[8:9] op_sel_hi:[1,0]
CV 20,12,13
CV 21,14,15
CV 22,16,17
CV 23,18,19
buffer_store_dwordx4 v[20:23], v237, s[24:27], s80 offen offset:0 sc1
v_accvgpr_read_b32 v24, a176
v_accvgpr_read_b32 v25, a177
v_accvgpr_read_b32 v26, a178
v_accvgpr_read_b32 v27, a179
v_accvgpr_read_b32 v28, a180
v_accvgpr_read_b32 v29, a181
v_accvgpr_read_b32 v30, a182
v_accvgpr_read_b32 v31, a183
v_pk_mul_f32 v[24:25], v[24:25], v[10:11] op_sel_hi:[1,0]
v_pk_mul_f32 v[26:27], v[26:27], v[10:11] op_sel_hi:[1,0]
v_pk_mul_f32 v[28:29], v[28:29], v[10:11] op_sel_hi:[1,0]
v_pk_mul_f32 v[30:31], v[30:31], v[10:11] op_sel_hi:[1,0]
CV 32,24,25
CV 33,26,27
CV 34,28,29
CV 35,30,31
buffer_store_dwordx4 v[32:35], v237, s[24:27], s80 offen offset:1024 sc1
s_mov_b32 s80, 22528
v_accvgpr_read_b32 v36, a152
v_accvgpr_read_b32 v37, a153
v_accvgpr_read_b32 v38, a154
v_accvgpr_read_b32 v39, a155
v_accvgpr_read_b32 v40, a156
v_accvgpr_read_b32 v41, a157
v_accvgpr_read_b32 v42, a158
v_accvgpr_read_b32 v43, a159
v_pk_mul_f32 v[36:37], v[36:37], v[8:9] op_sel_hi:[1,0]
v_pk_mul_f32 v[38:39], v[38:39], v[8:9] op_sel_hi:[1,0]
v_pk_mul_f32 v[40:41], v[40:41], v[8:9] op_sel_hi:[1,0]
v_pk_mul_f32 v[42:43], v[42:43], v[8:9] op_sel_hi:[1,0]
CV 44,36,37
CV 45,38,39
CV 46,40,41
CV 47,42,43
buffer_store_dwordx4 v[44:47], v237, s[24:27], s80 offen offset:0 sc1
v_accvgpr_read_b32 v48, a184
v_accvgpr_read_b32 v49, a185
v_accvgpr_read_b32 v50, a186
v_accvgpr_read_b32 v51, a187
v_accvgpr_read_b32 v52, a188
v_accvgpr_read_b32 v53, a189
v_accvgpr_read_b32 v54, a190
v_accvgpr_read_b32 v55, a191
v_pk_mul_f32 v[48:49], v[48:49], v[10:11] op_sel_hi:[1,0]
v_pk_mul_f32 v[50:51], v[50:51], v[10:11] op_sel_hi:[1,0]
v_pk_mul_f32 v[52:53], v[52:53], v[10:11] op_sel_hi:[1,0]
v_pk_mul_f32 v[54:55], v[54:55], v[10:11] op_sel_hi:[1,0]
CV 56,48,49
CV 57,50,51
CV 58,52,53
CV 59,54,55
buffer_store_dwordx4 v[56:59], v237, s[24:27], s80 offen offset:1024 sc1
s_lshl_b32 s82, s56, 2
s_mul_i32 s80, s56, 81920
s_add_u32 s80, s80, 16384
v_lshrrev_b32 v193, 2, v192
v_and_b32 v193, 3, v193
v_lshrrev_b32 v194, 4, v192
v_lshl_add_u32 v195, v194, 2, v193
v_lshlrev_b32 v195, 8, v195
v_and_b32 v194, 1, v194
v_lshl_add_u32 v193, v194, 2, v193
v_xor_b32 v193, s82, v193
v_and_b32 v194, 3, v192
v_lshl_add_u32 v195, v194, 3, v195
v_add_u32 v195, 0, v195
v_xor_b32 v194, 0, v193
v_lshl_add_u32 v200, v194, 5, v195
v_xor_b32 v194, 1, v193
v_lshl_add_u32 v201, v194, 5, v195
v_xor_b32 v194, 2, v193
v_lshl_add_u32 v202, v194, 5, v195
v_xor_b32 v194, 3, v193
v_lshl_add_u32 v203, v194, 5, v195
v_xor_b32 v194, 4, v193
v_lshl_add_u32 v204, v194, 5, v195
v_xor_b32 v194, 5, v193
v_lshl_add_u32 v205, v194, 5, v195
v_xor_b32 v194, 6, v193
v_lshl_add_u32 v206, v194, 5, v195
v_xor_b32 v194, 7, v193
v_lshl_add_u32 v207, v194, 5, v195
v_add_u32 v220, 81920, v200
v_add_u32 v228, s80, v200
v_add_u32 v221, 81920, v201
v_add_u32 v229, s80, v201
v_add_u32 v222, 81920, v202
v_add_u32 v230, s80, v202
v_add_u32 v223, 81920, v203
v_add_u32 v231, s80, v203
v_add_u32 v224, 81920, v204
v_add_u32 v232, s80, v204
v_add_u32 v225, 81920, v205
v_add_u32 v233, s80, v205
v_add_u32 v226, 81920, v206
v_add_u32 v234, s80, v206
v_add_u32 v227, 81920, v207
v_add_u32 v235, s80, v207
s_mul_i32 s80, s55, 12288
s_add_u32 s80, s80, 40960
s_mul_i32 s81, s56, 81920
s_add_u32 s93, s80, s81
v_add_u32 v208, s93, v237
s_sub_u32 s81, 81920, s81
s_add_u32 s93, s80, s81
v_add_u32 v209, s93, v237
v_and_b32 v195, 15, v192
v_lshlrev_b32 v195, 12, v195
v_and_b32 v194, 16, v192
v_lshl_add_u32 v195, v194, 1, v195
v_lshrrev_b32 v194, 5, v192
v_lshl_add_u32 v195, v194, 4, v195
s_mul_i32 s80, s55, 196608
s_xor_b32 s82, s56, 1
s_lshl_b32 s82, s82, 7
s_add_u32 s80, s80, s82
v_add_u32 v212, s80, v195
v_mov_b32 v195, 1
s_mov_b32 s92, 0
s_waitcnt vmcnt(0)
L_hs: ; ---- hand-off: response must equal challenge, then arrive
v_cmp_ne_u32 vcc, v215, v197
s_cbranch_vccnz L_hs_slow
L_hs_ok:
s_mov_b64 exec, 1
buffer_atomic_add v195, off, s[40:43], 0
s_mov_b64 exec, -1
s_barrier
s_bfe_u32 s93, s78, 0x4001c
s_lshl1_add_u32 s93, s93, s98
s_add_u32 m0, s96, 0
s_lshl_b32 s81, s93, 16
DM 213,32,81,0
DM 214,32,81,1024
s_bfe_u32 s93, s78, 0x4001c
s_lshl1_add_u32 s93, s93, s98
s_xor_b32 s93, s93, 1
s_add_u32 m0, s96, 8192
s_lshl_b32 s81, s93, 16
DM 213,32,81,0
DM 214,32,81,1024
s_bfe_u32 s93, s78, 0x40000
s_lshl1_add_u32 s93, s93, s98
s_add_u32 m0, s96, 16384
s_lshl_b32 s81, s93, 16
DM 213,32,81,0
DM 214,32,81,1024
s_bfe_u32 s93, s78, 0x40000
s_lshl1_add_u32 s93, s93, s98
s_xor_b32 s93, s93, 1
s_add_u32 m0, s96, 24576
s_lshl_b32 s81, s93, 16
DM 213,32,81,0
DM 214,32,81,1024
s_bfe_u32 s93, s78, 0x40004
s_lshl1_add_u32 s93, s93, s98
s_add_u32 m0, s96, 81920
s_lshl_b32 s81, s93, 16
DM 213,32,81,0
DM 214,32,81,1024
s_bfe_u32 s93, s78, 0x40004
s_lshl1_add_u32 s93, s93, s98
s_xor_b32 s93, s93, 1
s_add_u32 m0, s96, 90112
s_lshl_b32 s81, s93, 16
DM 213,32,81,0
DM 214,32,81,1024
s_bfe_u32 s93, s78, 0x40010
s_lshl1_add_u32 s93, s93, s99
s_add_u32 m0, s97, 40960
s_lshl_b32 s81, s93, 16
DM 216,32,81,0
DM 217,32,81,1024
DM 218,32,81,2048
DM 219,32,81,3072
s_bfe_u32 s93, s78, 0x40010
s_lshl1_add_u32 s93, s93, s99
s_xor_b32 s93, s93, 1
s_add_u32 m0, s97, 49152
s_lshl_b32 s81, s93, 16
DM 216,32,81,0
DM 217,32,81,1024
DM 218,32,81,2048
DM 219,32,81,3072
s_bfe_u32 s93, s78, 0x40014
s_lshl1_add_u32 s93, s93, s99
s_add_u32 m0, s97, 57344
s_lshl_b32 s81, s93, 16
DM 216,32,81,0
DM 217,32,81,1024
DM 218,32,81,2048
DM 219,32,81,3072
s_bfe_u32 s93, s78, 0x40014
s_lshl1_add_u32 s93, s93, s99
s_xor_b32 s93, s93, 1
s_add_u32 m0, s97, 65536
s_lshl_b32 s81, s93, 16
DM 216,32,81,0
DM 217,32,81,1024
DM 218,32,81,2048
DM 219,32,81,3072
s_waitcnt vmcnt(24)
s_barrier
RT 0,200,0
RT 2,200,4096
RT 4,200,8192
RT 6,200,12288
RT 8,201,0
RT 10,201,4096
RT 12,201,8192
RT 14,201,12288
RT 16,202,0
RT 18,202,4096
RT 20,202,8192
RT 22,202,12288
s_waitcnt lgkmcnt(4)
RT 24,203,0
RT 26,203,4096
RT 28,203,8192
RT 30,203,12288
RT 32,204,0
RT 34,204,4096
RT 36,204,8192
RT 38,204,12288
s_waitcnt lgkmcnt(4)
RT 40,205,0
RT 42,205,4096
RT 44,205,8192
RT 46,205,12288
RT 48,206,0
RT 50,206,4096
RT 52,206,8192
RT 54,206,12288
s_waitcnt lgkmcnt(4)
RT 56,207,0
RT 58,207,4096
RT 60,207,8192
RT 62,207,12288
s_cmp_lg_u32 s70, 0
s_cbranch_scc1 L_pd
L_pl: ; ---- wave 0 waits for the group
buffer_load_dword v194, off, s[40:43], 0 sc1
s_waitcnt vmcnt(0)
v_cmp_eq_u32 vcc, 64, v194
s_cbranch_vccnz L_pd
s_add_u32 s92, s92, 1
s_cmp_gt_u32 s92, 0x40000
s_cbranch_scc1 L_pd
)ASM"
R"ASM(s_and_b32 s95, s92, 15
s_or_b32 s95, s95, s77
s_cbranch_scc1 L_pn
v_lshlrev_b32 v193, 2, v192
buffer_load_dword v194, v193, s[40:43], 0 offen offset:2560 sc1
s_waitcnt vmcnt(0)
buffer_store_dword v194, v193, s[40:43], 0 offen offset:2816 sc1
s_waitcnt vmcnt(0)
L_pn:
s_sleep 2
s_branch L_pl
L_pd: ; ---- output projection
s_barrier
s_bfe_u32 s93, s78, 0x4001c
s_lshl1_add_u32 s93, s93, s98
s_mul_i32 s94, s93, 6144
s_add_u32 s94, s94, s100
buffer_load_dwordx4 a[64:67], v237, s[28:31], s94 offen offset:0 sc1
buffer_load_dwordx4 a[68:71], v237, s[28:31], s94 offen offset:1024 sc1
buffer_load_dwordx4 a[72:75], v237, s[28:31], s94 offen offset:2048 sc1
s_bfe_u32 s93, s78, 0x4001c
s_lshl1_add_u32 s93, s93, s98
s_xor_b32 s93, s93, 1
s_mul_i32 s94, s93, 6144
s_add_u32 s94, s94, s100
buffer_load_dwordx4 a[76:79], v237, s[28:31], s94 offen offset:0 sc1
buffer_load_dwordx4 a[80:83], v237, s[28:31], s94 offen offset:1024 sc1
buffer_load_dwordx4 a[84:87], v237, s[28:31], s94 offen offset:2048 sc1
s_bfe_u32 s93, s78, 0x40000
s_lshl1_add_u32 s93, s93, s98
s_mul_i32 s94, s93, 6144
s_add_u32 s94, s94, s100
buffer_load_dwordx4 a[88:91], v237, s[28:31], s94 offen offset:0 sc1
buffer_load_dwordx4 a[92:95], v237, s[28:31], s94 offen offset:1024 sc1
buffer_load_dwordx4 a[96:99], v237, s[28:31], s94 offen offset:2048 sc1
s_bfe_u32 s93, s78, 0x40000
s_lshl1_add_u32 s93, s93, s98
s_xor_b32 s93, s93, 1
s_mul_i32 s94, s93, 6144
s_add_u32 s94, s94, s100
buffer_load_dwordx4 a[100:103], v237, s[28:31], s94 offen offset:0 sc1
buffer_load_dwordx4 a[104:107], v237, s[28:31], s94 offen offset:1024 sc1
buffer_load_dwordx4 a[108:111], v237, s[28:31], s94 offen offset:2048 sc1
s_waitcnt vmcnt(6)
v_mfma_f32_16x16x32_bf16 v[0:3], a[0:3], a[64:67], 0
s_bfe_u32 s93, s78, 0x40004
s_lshl1_add_u32 s93, s93, s98
s_mul_i32 s94, s93, 6144
MM 0,4,76
s_add_u32 s94, s94, s100
v_mfma_f32_16x16x32_bf16 v[4:7], a[4:7], a[80:83], 0
buffer_load_dwordx4 a[112:115], v237, s[28:31], s94 offen offset:0 sc1
MM 4,0,68
v_mfma_f32_16x16x32_bf16 v[8:11], a[0:3], a[72:75], 0
s_waitcnt vmcnt(33)
s_barrier
MM 8,4,84
buffer_load_dwordx4 a[116:119], v237, s[28:31], s94 offen offset:1024 sc1
v_mfma_f32_16x16x32_bf16 v[20:23], a[12:15], a[84:87], 0
RT 0,200,16384
RT 2,200,20480
MM 20,8,72
buffer_load_dwordx4 a[120:123], v237, s[28:31], s94 offen offset:2048 sc1
v_mfma_f32_16x16x32_bf16 v[16:19], a[8:11], a[68:71], 0
s_bfe_u32 s93, s78, 0x40004
s_lshl1_add_u32 s93, s93, s98
s_xor_b32 s93, s93, 1
MM 16,12,80
s_mul_i32 s94, s93, 6144
s_add_u32 s94, s94, s100
RT 4,200,24576
v_mfma_f32_16x16x32_bf16 v[12:15], a[12:15], a[76:79], 0
buffer_load_dwordx4 a[124:127], v237, s[28:31], s94 offen offset:0 sc1
MM 12,8,64
RT 6,200,28672
RT 8,201,16384
v_mfma_f32_16x16x32_bf16 v[24:27], a[16:19], a[64:67], 0
RT 10,201,20480
RT 12,201,24576
MM 24,20,76
buffer_load_dwordx4 a[128:131], v237, s[28:31], s94 offen offset:1024 sc1
v_mfma_f32_16x16x32_bf16 v[28:31], a[20:23], a[80:83], 0
RT 14,201,28672
MM 28,16,68
buffer_load_dwordx4 a[132:135], v237, s[28:31], s94 offen offset:2048 sc1
v_mfma_f32_16x16x32_bf16 v[32:35], a[16:19], a[72:75], 0
s_bfe_u32 s93, s78, 0x40008
s_lshl1_add_u32 s93, s93, s98
s_add_u32 m0, s96, 98304
MM 32,20,84
s_lshl_b32 s81, s93, 16
RT 16,202,16384
RT 18,202,20480
v_mfma_f32_16x16x32_bf16 v[44:47], a[28:31], a[84:87], 0
DM 213,32,81,0
MM 44,24,72
RT 20,202,24576
RT 22,202,28672
v_mfma_f32_16x16x32_bf16 v[40:43], a[24:27], a[68:71], 0
MM 40,28,80
DM 214,32,81,1024
v_mfma_f32_16x16x32_bf16 v[36:39], a[28:31], a[76:79], 0
s_bfe_u32 s93, s78, 0x40008
s_lshl1_add_u32 s93, s93, s98
s_mul_i32 s94, s93, 6144
MM 36,24,64
s_add_u32 s94, s94, s100
RT 24,203,16384
RT 26,203,20480
v_mfma_f32_16x16x32_bf16 v[48:51], a[32:35], a[64:67], 0
RT 28,203,24576
RT 30,203,28672
MM 48,36,76
v_mfma_f32_16x16x32_bf16 v[52:55], a[36:39], a[80:83], 0
buffer_load_dwordx4 a[136:139], v237, s[28:31], s94 offen offset:0 sc1
MM 52,32,68
v_mfma_f32_16x16x32_bf16 v[56:59], a[32:35], a[72:75], 0
buffer_load_dwordx4 a[140:143], v237, s[28:31], s94 offen offset:1024 sc1
MM 56,36,84
RT 32,204,16384
RT 34,204,20480
v_mfma_f32_16x16x32_bf16 v[68:71], a[44:47], a[84:87], 0
RT 36,204,24576
RT 38,204,28672
MM 68,40,72
buffer_load_dwordx4 a[144:147], v237, s[28:31], s94 offen offset:2048 sc1
v_mfma_f32_16x16x32_bf16 v[64:67], a[40:43], a[68:71], 0
s_bfe_u32 s93, s78, 0x40008
s_lshl1_add_u32 s93, s93, s98
s_xor_b32 s93, s93, 1
MM 64,44,80
s_mul_i32 s94, s93, 6144
s_add_u32 s94, s94, s100
s_waitcnt lgkmcnt(15)
v_mfma_f32_16x16x32_bf16 v[60:63], a[44:47], a[76:79], 0
buffer_load_dwordx4 a[148:151], v237, s[28:31], s94 offen offset:0 sc1
MM 60,40,64
RT 40,205,16384
RT 42,205,20480
v_mfma_f32_16x16x32_bf16 v[72:75], a[48:51], a[64:67], 0
RT 44,205,24576
RT 46,205,28672
MM 72,52,76
buffer_load_dwordx4 a[152:155], v237, s[28:31], s94 offen offset:1024 sc1
v_mfma_f32_16x16x32_bf16 v[76:79], a[52:55], a[80:83], 0
MM 76,48,68
buffer_load_dwordx4 a[156:159], v237, s[28:31], s94 offen offset:2048 sc1
v_mfma_f32_16x16x32_bf16 v[80:83], a[48:51], a[72:75], 0
s_add_u32 m0, s96, 106496
s_lshl_b32 s81, s93, 16
MM 80,52,84
RT 48,206,16384
RT 50,206,20480
v_mfma_f32_16x16x32_bf16 v[92:95], a[60:63], a[84:87], 0
DM 213,32,81,0
MM 92,56,72
RT 52,206,24576
RT 54,206,28672
v_mfma_f32_16x16x32_bf16 v[88:91], a[56:59], a[68:71], 0
MM 88,60,80
DM 214,32,81,1024
v_mfma_f32_16x16x32_bf16 v[84:87], a[60:63], a[76:79], 0
s_waitcnt vmcnt(16)
s_waitcnt lgkmcnt(15)
s_bfe_u32 s93, s78, 0x4000c
MM 84,56,64
s_lshl1_add_u32 s93, s93, s98
s_mul_i32 s94, s93, 6144
s_add_u32 s94, s94, s100
MM 0,0,88
RT 56,207,16384
RT 58,207,20480
MM 0,4,100
RT 60,207,24576
RT 62,207,28672
MM 4,4,104
buffer_load_dwordx4 a[64:67], v237, s[28:31], s94 offen offset:0 sc1
MM 4,0,92
MM 8,0,96
s_waitcnt vmcnt(45)
s_barrier
MM 8,4,108
RT 0,220,0
RT 2,220,4096
MM 20,12,108
buffer_load_dwordx4 a[68:71], v237, s[28:31], s94 offen offset:1024 sc1
MM 20,8,96
RT 4,220,8192
RT 6,220,12288
MM 16,8,92
MM 16,12,104
s_waitcnt lgkmcnt(15)
MM 12,12,100
buffer_load_dwordx4 a[72:75], v237, s[28:31], s94 offen offset:2048 sc1
MM 12,8,88
s_add_u32 m0, s96, 0
s_lshl_b32 s81, s93, 16
RT 8,221,0
MM 24,16,88
RT 10,221,4096
RT 12,221,8192
MM 24,20,100
RT 14,221,12288
MM 28,20,104
MM 28,16,92
DM 213,32,81,0
MM 32,16,96
MM 32,20,108
RT 16,222,0
RT 18,222,4096
MM 44,28,108
RT 20,222,8192
RT 22,222,12288
MM 44,24,96
DM 214,32,81,1024
MM 40,24,92
s_bfe_u32 s93, s78, 0x4000c
s_lshl1_add_u32 s93, s93, s98
s_xor_b32 s93, s93, 1
MM 40,28,104
s_waitcnt lgkmcnt(15)
s_mul_i32 s94, s93, 6144
s_add_u32 s94, s94, s100
MM 36,28,100
MM 36,24,88
RT 24,223,0
RT 26,223,4096
MM 48,32,88
RT 28,223,8192
RT 30,223,12288
MM 48,36,100
MM 52,36,104
buffer_load_dwordx4 a[76:79], v237, s[28:31], s94 offen offset:0 sc1
MM 52,32,92
MM 56,32,96
MM 56,36,108
RT 32,224,0
RT 34,224,4096
MM 68,44,108
buffer_load_dwordx4 a[80:83], v237, s[28:31], s94 offen offset:1024 sc1
MM 68,40,96
RT 36,224,8192
RT 38,224,12288
MM 64,40,92
MM 64,44,104
s_waitcnt lgkmcnt(15)
MM 60,44,100
buffer_load_dwordx4 a[84:87], v237, s[28:31], s94 offen offset:2048 sc1
MM 60,40,88
s_add_u32 m0, s96, 8192
s_lshl_b32 s81, s93, 16
RT 40,225,0
MM 72,48,88
RT 42,225,4096
RT 44,225,8192
MM 72,52,100
RT 46,225,12288
MM 76,52,104
MM 76,48,92
DM 213,32,81,0
MM 80,48,96
MM 80,52,108
RT 48,226,0
RT 50,226,4096
MM 92,60,108
RT 52,226,8192
RT 54,226,12288
MM 92,56,96
DM 214,32,81,1024
MM 88,56,92
s_bfe_u32 s93, s78, 0x40000
s_lshl1_add_u32 s93, s93, s98
s_add_u32 s93, s93, 8
MM 88,60,104
s_waitcnt vmcnt(20)
s_waitcnt lgkmcnt(15)
s_mul_i32 s94, s93, 6144
MM 84,60,100
s_add_u32 s94, s94, s100
MM 84,56,88
RT 56,227,0
RT 58,227,4096
MM 0,0,112
RT 60,227,8192
RT 62,227,12288
MM 0,4,124
MM 4,4,128
buffer_load_dwordx4 a[88:91], v237, s[28:31], s94 offen offset:0 sc1
MM 4,0,116
MM 8,0,120
s_waitcnt vmcnt(11)
s_barrier
MM 8,4,132
RT 0,220,16384
RT 2,220,20480
MM 20,12,132
buffer_load_dwordx4 a[92:95], v237, s[28:31], s94 offen offset:1024 sc1
MM 20,8,120
RT 4,220,24576
RT 6,220,28672
MM 16,8,116
MM 16,12,128
s_waitcnt lgkmcnt(15)
MM 12,12,124
buffer_load_dwordx4 a[96:99], v237, s[28:31], s94 offen offset:2048 sc1
MM 12,8,112
s_add_u32 m0, s96, 16384
s_lshl_b32 s81, s93, 16
RT 8,221,16384
MM 24,16,112
RT 10,221,20480
RT 12,221,24576
MM 24,20,124
RT 14,221,28672
MM 28,20,128
MM 28,16,116
DM 213,32,81,0
MM 32,16,120
MM 32,20,132
RT 16,222,16384
RT 18,222,20480
MM 44,28,132
RT 20,222,24576
RT 22,222,28672
MM 44,24,120
DM 214,32,81,1024
MM 40,24,116
s_bfe_u32 s93, s78, 0x40000
s_lshl1_add_u32 s93, s93, s98
s_add_u32 s93, s93, 8
MM 40,28,128
s_waitcnt lgkmcnt(15)
s_xor_b32 s93, s93, 1
s_mul_i32 s94, s93, 6144
MM 36,28,124
s_add_u32 s94, s94, s100
MM 36,24,112
RT 24,223,16384
RT 26,223,20480
MM 48,32,112
RT 28,223,24576
RT 30,223,28672
MM 48,36,124
MM 52,36,128
buffer_load_dwordx4 a[100:103], v237, s[28:31], s94 offen offset:0 sc1
MM 52,32,116
MM 56,32,120
MM 56,36,132
RT 32,224,16384
RT 34,224,20480
MM 68,44,132
buffer_load_dwordx4 a[104:107], v237, s[28:31], s94 offen offset:1024 sc1
MM 68,40,120
RT 36,224,24576
RT 38,224,28672
MM 64,40,116
MM 64,44,128
s_waitcnt lgkmcnt(15)
MM 60,44,124
buffer_load_dwordx4 a[108:111], v237, s[28:31], s94 offen offset:2048 sc1
MM 60,40,112
s_add_u32 m0, s96, 24576
s_lshl_b32 s81, s93, 16
RT 40,225,16384
MM 72,48,112
RT 42,225,20480
RT 44,225,24576
MM 72,52,124
RT 46,225,28672
MM 76,52,128
MM 76,48,116
DM 213,32,81,0
MM 80,48,120
MM 80,52,132
RT 48,226,16384
RT 50,226,20480
MM 92,60,132
RT 52,226,24576
RT 54,226,28672
MM 92,56,120
DM 214,32,81,1024
MM 88,56,116
s_bfe_u32 s93, s78, 0x40004
s_lshl1_add_u32 s93, s93, s98
s_add_u32 s93, s93, 8
MM 88,60,128
s_waitcnt vmcnt(22)
s_waitcnt lgkmcnt(15)
s_mul_i32 s94, s93, 6144
MM 84,60,124
s_add_u32 s94, s94, s100
MM 84,56,112
RT 56,227,16384
RT 58,227,20480
MM 0,0,136
RT 60,227,24576
RT 62,227,28672
MM 0,4,148
MM 4,4,152
buffer_load_dwordx4 a[112:115], v237, s[28:31], s94 offen offset:0 sc1
MM 4,0,140
MM 8,0,144
s_waitcnt vmcnt(11)
s_barrier
MM 8,4,156
RT 0,200,0
RT 2,200,4096
MM 20,12,156
buffer_load_dwordx4 a[116:119], v237, s[28:31], s94 offen offset:1024 sc1
MM 20,8,144
RT 4,200,8192
RT 6,200,12288
MM 16,8,140
MM 16,12,152
s_waitcnt lgkmcnt(15)
MM 12,12,148
buffer_load_dwordx4 a[120:123], v237, s[28:31], s94 offen offset:2048 sc1
MM 12,8,136
s_add_u32 m0, s96, 81920
s_lshl_b32 s81, s93, 16
RT 8,201,0
MM 24,16,136
s_getpc_b64 s[62:63]
L_w6:
RT 10,201,4096
RT 12,201,8192
MM 24,20,148
s_add_u32 s62, s62, L_o16-L_w6
s_addc_u32 s63, s63, 0
RT 14,201,12288
MM 28,20,152
v_min_u32 v196, L_o32-L_o16, v211
MM 28,16,140
DM 213,32,81,0
MM 32,16,144
MM 32,20,156
RT 16,202,0
RT 18,202,4096
MM 44,28,156
RT 20,202,8192
RT 22,202,12288
MM 44,24,144
DM 214,32,81,1024
MM 40,24,140
s_bfe_u32 s93, s78, 0x40004
s_lshl1_add_u32 s93, s93, s98
s_add_u32 s93, s93, 8
MM 40,28,152
s_mov_b64 exec, 3
global_load_dword v215, v196, s[62:63]
s_mov_b64 exec, -1
MM 36,28,148
s_waitcnt lgkmcnt(15)
s_xor_b32 s93, s93, 1
s_mul_i32 s94, s93, 6144
MM 36,24,136
s_add_u32 s94, s94, s100
RT 24,203,0
RT 26,203,4096
MM 48,32,136
RT 28,203,8192
RT 30,203,12288
MM 48,36,148
MM 52,36,152
buffer_load_dwordx4 a[124:127], v237, s[28:31], s94 offen offset:0 sc1
MM 52,32,140
MM 56,32,144
MM 56,36,156
RT 32,204,0
RT 34,204,4096
MM 68,44,156
buffer_load_dwordx4 a[128:131], v237, s[28:31], s94 offen offset:1024 sc1
MM 68,40,144
RT 36,204,8192
RT 38,204,12288
MM 64,40,140
MM 64,44,152
s_waitcnt lgkmcnt(15)
MM 60,44,148
buffer_load_dwordx4 a[132:135], v237, s[28:31], s94 offen offset:2048 sc1
MM 60,40,136
s_add_u32 m0, s96, 90112
s_lshl_b32 s81, s93, 16
RT 40,205,0
MM 72,48,136
RT 42,205,4096
RT 44,205,8192
MM 72,52,148
RT 46,205,12288
MM 76,52,152
MM 76,48,140
DM 213,32,81,0
MM 80,48,144
MM 80,52,156
RT 48,206,0
RT 50,206,4096
MM 92,60,156
RT 52,206,8192
RT 54,206,12288
MM 92,56,144
DM 214,32,81,1024
MM 88,56,140
s_bfe_u32 s93, s78, 0x40008
s_lshl1_add_u32 s93, s93, s98
s_add_u32 s93, s93, 8
MM 88,60,152
s_waitcnt vmcnt(23)
s_waitcnt lgkmcnt(15)
s_mul_i32 s94, s93, 6144
MM 84,60,148
s_add_u32 s94, s94, s100
MM 84,56,136
RT 56,207,0
RT 58,207,4096
MM 0,0,64
RT 60,207,8192
RT 62,207,12288
MM 0,4,76
MM 4,4,80
buffer_load_dwordx4 a[136:139], v237, s[28:31], s94 offen offset:0 sc1
MM 4,0,68
MM 8,0,72
s_waitcnt vmcnt(12)
s_barrier
MM 8,4,84
RT 0,200,16384
RT 2,200,20480
MM 20,12,84
buffer_load_dwordx4 a[140:143], v237, s[28:31], s94 offen offset:1024 sc1
MM 20,8,72
RT 4,200,24576
RT 6,200,28672
MM 16,8,68
MM 16,12,80
s_waitcnt lgkmcnt(15)
MM 12,12,76
buffer_load_dwordx4 a[144:147], v237, s[28:31], s94 offen offset:2048 sc1
MM 12,8,64
s_add_u32 m0, s96, 98304
s_lshl_b32 s81, s93, 16
RT 8,201,16384
MM 24,16,64
RT 10,201,20480
RT 12,201,24576
MM 24,20,76
RT 14,201,28672
MM 28,20,80
MM 28,16,68
DM 213,32,81,0
MM 32,16,72
MM 32,20,84
RT 16,202,16384
RT 18,202,20480
MM 44,28,84
RT 20,202,24576
RT 22,202,28672
MM 44,24,72
DM 214,32,81,1024
MM 40,24,68
s_bfe_u32 s93, s78, 0x40008
s_lshl1_add_u32 s93, s93, s98
s_add_u32 s93, s93, 8
MM 40,28,80
s_waitcnt lgkmcnt(15)
s_xor_b32 s93, s93, 1
s_mul_i32 s94, s93, 6144
MM 36,28,76
s_add_u32 s94, s94, s100
MM 36,24,64
RT 24,203,16384
RT 26,203,20480
MM 48,32,64
RT 28,203,24576
RT 30,203,28672
MM 48,36,76
MM 52,36,80
buffer_load_dwordx4 a[148:151], v237, s[28:31], s94 offen offset:0 sc1
MM 52,32,68
MM 56,32,72
MM 56,36,84
RT 32,204,16384
RT 34,204,20480
MM 68,44,84
buffer_load_dwordx4 a[152:155], v237, s[28:31], s94 offen offset:1024 sc1
MM 68,40,72
RT 36,204,24576
RT 38,204,28672
MM 64,40,68
MM 64,44,80
s_waitcnt lgkmcnt(15)
MM 60,44,76
buffer_load_dwordx4 a[156:159], v237, s[28:31], s94 offen offset:2048 sc1
MM 60,40,64
s_add_u32 m0, s96, 106496
s_lshl_b32 s81, s93, 16
RT 40,205,16384
MM 72,48,64
RT 42,205,20480
RT 44,205,24576
MM 72,52,76
RT 46,205,28672
MM 76,52,80
MM 76,48,68
DM 213,32,81,0
MM 80,48,72
MM 80,52,84
RT 48,206,16384
RT 50,206,20480
MM 92,60,84
RT 52,206,24576
RT 54,206,28672
MM 92,56,72
DM 214,32,81,1024
MM 88,56,68
s_bfe_u32 s93, s78, 0x4000c
s_lshl1_add_u32 s93, s93, s98
s_add_u32 s93, s93, 8
MM 88,60,80
s_waitcnt vmcnt(23)
s_waitcnt lgkmcnt(15)
s_mul_i32 s94, s93, 6144
MM 84,60,76
s_add_u32 s94, s94, s100
MM 84,56,64
RT 56,207,16384
RT 58,207,20480
MM 0,0,88
RT 60,207,24576
RT 62,207,28672
MM 0,4,100
MM 4,4,104
buffer_load_dwordx4 a[64:67], v237, s[28:31], s94 offen offset:0 sc1
MM 4,0,92
MM 8,0,96
s_waitcnt vmcnt(11)
s_barrier
MM 8,4,108
RT 0,220,0
RT 2,220,4096
MM 20,12,108
buffer_load_dwordx4 a[68:71], v237, s[28:31], s94 offen offset:1024 sc1
MM 20,8,96
RT 4,220,8192
RT 6,220,12288
MM 16,8,92
MM 16,12,104
s_waitcnt lgkmcnt(15)
MM 12,12,100
buffer_load_dwordx4 a[72:75], v237, s[28:31], s94 offen offset:2048 sc1
MM 12,8,88
s_add_u32 m0, s96, 0
s_lshl_b32 s81, s93, 16
RT 8,221,0
MM 24,16,88
RT 10,221,4096
RT 12,221,8192
MM 24,20,100
RT 14,221,12288
MM 28,20,104
MM 28,16,92
DM 213,32,81,0
MM 32,16,96
MM 32,20,108
RT 16,222,0
RT 18,222,4096
MM 44,28,108
RT 20,222,8192
RT 22,222,12288
MM 44,24,96
DM 214,32,81,1024
MM 40,24,92
s_bfe_u32 s93, s78, 0x4000c
s_lshl1_add_u32 s93, s93, s98
s_add_u32 s93, s93, 8
MM 40,28,104
s_waitcnt lgkmcnt(15)
s_xor_b32 s93, s93, 1
s_mul_i32 s94, s93, 6144
MM 36,28,100
s_add_u32 s94, s94, s100
MM 36,24,88
RT 24,223,0
RT 26,223,4096
MM 48,32,88
RT 28,223,8192
RT 30,223,12288
MM 48,36,100
MM 52,36,104
buffer_load_dwordx4 a[76:79], v237, s[28:31], s94 offen offset:0 sc1
MM 52,32,92
MM 56,32,96
MM 56,36,108
RT 32,224,0
RT 34,224,4096
MM 68,44,108
buffer_load_dwordx4 a[80:83], v237, s[28:31], s94 offen offset:1024 sc1
MM 68,40,96
RT 36,224,8192
RT 38,224,12288
MM 64,40,92
MM 64,44,104
s_waitcnt lgkmcnt(15)
MM 60,44,100
buffer_load_dwordx4 a[84:87], v237, s[28:31], s94 offen offset:2048 sc1
MM 60,40,88
s_add_u32 m0, s96, 8192
s_lshl_b32 s81, s93, 16
RT 40,225,0
MM 72,48,88
RT 42,225,4096
RT 44,225,8192
MM 72,52,100
RT 46,225,12288
MM 76,52,104
MM 76,48,92
DM 213,32,81,0
MM 80,48,96
MM 80,52,108
RT 48,226,0
RT 50,226,4096
MM 92,60,108
RT 52,226,8192
RT 54,226,12288
MM 92,56,96
DM 214,32,81,1024
MM 88,56,92
s_bfe_u32 s93, s78, 0x40000
s_lshl1_add_u32 s93, s93, s98
s_add_u32 s93, s93, 16
MM 88,60,104
s_waitcnt vmcnt(22)
s_waitcnt lgkmcnt(15)
s_mul_i32 s94, s93, 6144
MM 84,60,100
s_add_u32 s94, s94, s100
MM 84,56,88
RT 56,227,0
RT 58,227,4096
MM 0,0,112
RT 60,227,8192
RT 62,227,12288
MM 0,4,124
MM 4,4,128
buffer_load_dwordx4 a[88:91], v237, s[28:31], s94 offen offset:0 sc1
MM 4,0,116
MM 8,0,120
s_waitcnt vmcnt(11)
s_barrier
MM 8,4,132
RT 0,220,16384
RT 2,220,20480
MM 20,12,132
buffer_load_dwordx4 a[92:95], v237, s[28:31], s94 offen offset:1024 sc1
MM 20,8,120
RT 4,220,24576
RT 6,220,28672
MM 16,8,116
MM 16,12,128
s_waitcnt lgkmcnt(15)
MM 12,12,124
buffer_load_dwordx4 a[96:99], v237, s[28:31], s94 offen offset:2048 sc1
MM 12,8,112
s_add_u32 m0, s96, 16384
s_lshl_b32 s81, s93, 16
RT 8,221,16384
MM 24,16,112
RT 10,221,20480
RT 12,221,24576
MM 24,20,124
RT 14,221,28672
MM 28,20,128
MM 28,16,116
DM 213,32,81,0
MM 32,16,120
MM 32,20,132
RT 16,222,16384
RT 18,222,20480
MM 44,28,132
RT 20,222,24576
RT 22,222,28672
MM 44,24,120
DM 214,32,81,1024
MM 40,24,116
s_bfe_u32 s93, s78, 0x40000
s_lshl1_add_u32 s93, s93, s98
s_add_u32 s93, s93, 16
MM 40,28,128
s_waitcnt lgkmcnt(15)
s_xor_b32 s93, s93, 1
s_mul_i32 s94, s93, 6144
MM 36,28,124
s_add_u32 s94, s94, s100
MM 36,24,112
RT 24,223,16384
RT 26,223,20480
MM 48,32,112
RT 28,223,24576
RT 30,223,28672
MM 48,36,124
MM 52,36,128
buffer_load_dwordx4 a[100:103], v237, s[28:31], s94 offen offset:0 sc1
MM 52,32,116
MM 56,32,120
MM 56,36,132
RT 32,224,16384
RT 34,224,20480
MM 68,44,132
buffer_load_dwordx4 a[104:107], v237, s[28:31], s94 offen offset:1024 sc1
MM 68,40,120
RT 36,224,24576
RT 38,224,28672
MM 64,40,116
MM 64,44,128
s_waitcnt lgkmcnt(15)
MM 60,44,124
buffer_load_dwordx4 a[108:111], v237, s[28:31], s94 offen offset:2048 sc1
MM 60,40,112
s_add_u32 m0, s96, 24576
s_lshl_b32 s81, s93, 16
RT 40,225,16384
MM 72,48,112
RT 42,225,20480
RT 44,225,24576
MM 72,52,124
RT 46,225,28672
MM 76,52,128
MM 76,48,116
DM 213,32,81,0
MM 80,48,120
MM 80,52,132
RT 48,226,16384
RT 50,226,20480
MM 92,60,132
RT 52,226,24576
RT 54,226,28672
MM 92,56,120
DM 214,32,81,1024
MM 88,56,116
s_bfe_u32 s93, s78, 0x40004
s_lshl1_add_u32 s93, s93, s98
s_add_u32 s93, s93, 16
MM 88,60,128
s_waitcnt vmcnt(22)
s_waitcnt lgkmcnt(15)
s_mul_i32 s94, s93, 6144
MM 84,60,124
s_add_u32 s94, s94, s100
MM 84,56,112
RT 56,227,16384
RT 58,227,20480
MM 0,0,136
RT 60,227,24576
RT 62,227,28672
MM 0,4,148
MM 4,4,152
buffer_load_dwordx4 a[112:115], v237, s[28:31], s94 offen offset:0 sc1
MM 4,0,140
MM 8,0,144
s_waitcnt vmcnt(11)
s_barrier
MM 8,4,156
RT 0,200,0
RT 2,200,4096
MM 20,12,156
buffer_load_dwordx4 a[116:119], v237, s[28:31], s94 offen offset:1024 sc1
MM 20,8,144
RT 4,200,8192
RT 6,200,12288
MM 16,8,140
MM 16,12,152
s_waitcnt lgkmcnt(15)
MM 12,12,148
buffer_load_dwordx4 a[120:123], v237, s[28:31], s94 offen offset:2048 sc1
MM 12,8,136
s_add_u32 m0, s96, 81920
s_lshl_b32 s81, s93, 16
RT 8,201,0
MM 24,16,136
RT 10,201,4096
RT 12,201,8192
MM 24,20,148
RT 14,201,12288
MM 28,20,152
MM 28,16,140
DM 213,32,81,0
MM 32,16,144
MM 32,20,156
RT 16,202,0
RT 18,202,4096
MM 44,28,156
RT 20,202,8192
RT 22,202,12288
MM 44,24,144
DM 214,32,81,1024
MM 40,24,140
s_bfe_u32 s93, s78, 0x40004
s_lshl1_add_u32 s93, s93, s98
s_add_u32 s93, s93, 16
MM 40,28,152
s_waitcnt lgkmcnt(15)
s_xor_b32 s93, s93, 1
s_mul_i32 s94, s93, 6144
MM 36,28,148
s_add_u32 s94, s94, s100
MM 36,24,136
RT 24,203,0
RT 26,203,4096
MM 48,32,136
RT 28,203,8192
RT 30,203,12288
MM 48,36,148
MM 52,36,152
buffer_load_dwordx4 a[124:127], v237, s[28:31], s94 offen offset:0 sc1
MM 52,32,140
MM 56,32,144
MM 56,36,156
RT 32,204,0
RT 34,204,4096
MM 68,44,156
buffer_load_dwordx4 a[128:131], v237, s[28:31], s94 offen offset:1024 sc1
MM 68,40,144
RT 36,204,8192
RT 38,204,12288
MM 64,40,140
MM 64,44,152
s_waitcnt lgkmcnt(15)
MM 60,44,148
buffer_load_dwordx4 a[132:135], v237, s[28:31], s94 offen offset:2048 sc1
MM 60,40,136
s_add_u32 m0, s96, 90112
s_lshl_b32 s81, s93, 16
RT 40,205,0
MM 72,48,136
RT 42,205,4096
RT 44,205,8192
MM 72,52,148
RT 46,205,12288
MM 76,52,152
MM 76,48,140
DM 213,32,81,0
MM 80,48,144
MM 80,52,156
RT 48,206,0
RT 50,206,4096
MM 92,60,156
RT 52,206,8192
RT 54,206,12288
MM 92,56,144
DM 214,32,81,1024
MM 88,56,140
s_bfe_u32 s93, s78, 0x40008
s_lshl1_add_u32 s93, s93, s98
s_add_u32 s93, s93, 16
MM 88,60,152
s_waitcnt vmcnt(22)
s_waitcnt lgkmcnt(15)
s_mul_i32 s94, s93, 6144
MM 84,60,148
s_add_u32 s94, s94, s100
MM 84,56,136
RT 56,207,0
RT 58,207,4096
L_c3:
L_o16:
MM 0,0,64
RT 60,207,8192
RT 62,207,12288
MM 0,4,76
MM 4,4,80
buffer_load_dwordx4 a[136:139], v237, s[28:31], s94 offen offset:0 sc1
MM 4,0,68
MM 8,0,72
s_waitcnt vmcnt(11)
s_barrier
MM 8,4,84
RT 0,200,16384
RT 2,200,20480
MM 20,12,84
buffer_load_dwordx4 a[140:143], v237, s[28:31], s94 offen offset:1024 sc1
MM 20,8,72
RT 4,200,24576
RT 6,200,28672
MM 16,8,68
MM 16,12,80
s_waitcnt lgkmcnt(15)
MM 12,12,76
buffer_load_dwordx4 a[144:147], v237, s[28:31], s94 offen offset:2048 sc1
MM 12,8,64
s_add_u32 m0, s96, 98304
s_lshl_b32 s81, s93, 16
RT 8,201,16384
MM 24,16,64
RT 10,201,20480
RT 12,201,24576
MM 24,20,76
RT 14,201,28672
MM 28,20,80
MM 28,16,68
DM 213,32,81,0
MM 32,16,72
)ASM"
R"ASM(MM 32,20,84
RT 16,202,16384
RT 18,202,20480
MM 44,28,84
RT 20,202,24576
RT 22,202,28672
MM 44,24,72
DM 214,32,81,1024
MM 40,24,68
s_bfe_u32 s93, s78, 0x40008
s_lshl1_add_u32 s93, s93, s98
s_add_u32 s93, s93, 16
MM 40,28,80
s_waitcnt lgkmcnt(15)
s_xor_b32 s93, s93, 1
s_mul_i32 s94, s93, 6144
MM 36,28,76
s_add_u32 s94, s94, s100
MM 36,24,64
RT 24,203,16384
RT 26,203,20480
MM 48,32,64
RT 28,203,24576
RT 30,203,28672
MM 48,36,76
MM 52,36,80
buffer_load_dwordx4 a[148:151], v237, s[28:31], s94 offen offset:0 sc1
MM 52,32,68
MM 56,32,72
MM 56,36,84
RT 32,204,16384
RT 34,204,20480
MM 68,44,84
buffer_load_dwordx4 a[152:155], v237, s[28:31], s94 offen offset:1024 sc1
MM 68,40,72
RT 36,204,24576
RT 38,204,28672
MM 64,40,68
MM 64,44,80
s_waitcnt lgkmcnt(15)
MM 60,44,76
buffer_load_dwordx4 a[156:159], v237, s[28:31], s94 offen offset:2048 sc1
MM 60,40,64
s_add_u32 m0, s96, 106496
s_lshl_b32 s81, s93, 16
RT 40,205,16384
MM 72,48,64
RT 42,205,20480
RT 44,205,24576
MM 72,52,76
RT 46,205,28672
MM 76,52,80
MM 76,48,68
DM 213,32,81,0
MM 80,48,72
MM 80,52,84
RT 48,206,16384
RT 50,206,20480
MM 92,60,84
RT 52,206,24576
RT 54,206,28672
MM 92,56,72
DM 214,32,81,1024
MM 88,56,68
s_bfe_u32 s93, s78, 0x4000c
s_lshl1_add_u32 s93, s93, s98
s_add_u32 s93, s93, 16
MM 88,60,80
s_waitcnt vmcnt(22)
s_waitcnt lgkmcnt(15)
s_mul_i32 s94, s93, 6144
MM 84,60,76
s_add_u32 s94, s94, s100
MM 84,56,64
RT 56,207,16384
RT 58,207,20480
MM 0,0,88
RT 60,207,24576
RT 62,207,28672
MM 0,4,100
MM 4,4,104
buffer_load_dwordx4 a[64:67], v237, s[28:31], s94 offen offset:0 sc1
MM 4,0,92
MM 8,0,96
s_waitcnt vmcnt(11)
s_barrier
MM 8,4,108
RT 0,220,0
RT 2,220,4096
MM 20,12,108
buffer_load_dwordx4 a[68:71], v237, s[28:31], s94 offen offset:1024 sc1
MM 20,8,96
RT 4,220,8192
RT 6,220,12288
MM 16,8,92
MM 16,12,104
s_waitcnt lgkmcnt(15)
MM 12,12,100
buffer_load_dwordx4 a[72:75], v237, s[28:31], s94 offen offset:2048 sc1
MM 12,8,88
s_add_u32 m0, s96, 0
s_lshl_b32 s81, s93, 16
RT 8,221,0
MM 24,16,88
RT 10,221,4096
RT 12,221,8192
MM 24,20,100
RT 14,221,12288
MM 28,20,104
MM 28,16,92
DM 213,32,81,0
MM 32,16,96
MM 32,20,108
RT 16,222,0
RT 18,222,4096
MM 44,28,108
RT 20,222,8192
RT 22,222,12288
MM 44,24,96
DM 214,32,81,1024
MM 40,24,92
s_bfe_u32 s93, s78, 0x4000c
s_lshl1_add_u32 s93, s93, s98
s_add_u32 s93, s93, 16
MM 40,28,104
s_waitcnt lgkmcnt(15)
s_xor_b32 s93, s93, 1
s_mul_i32 s94, s93, 6144
MM 36,28,100
s_add_u32 s94, s94, s100
MM 36,24,88
RT 24,223,0
RT 26,223,4096
MM 48,32,88
RT 28,223,8192
RT 30,223,12288
MM 48,36,100
MM 52,36,104
buffer_load_dwordx4 a[76:79], v237, s[28:31], s94 offen offset:0 sc1
MM 52,32,92
MM 56,32,96
MM 56,36,108
RT 32,224,0
RT 34,224,4096
MM 68,44,108
buffer_load_dwordx4 a[80:83], v237, s[28:31], s94 offen offset:1024 sc1
MM 68,40,96
RT 36,224,8192
RT 38,224,12288
MM 64,40,92
MM 64,44,104
s_waitcnt lgkmcnt(15)
MM 60,44,100
buffer_load_dwordx4 a[84:87], v237, s[28:31], s94 offen offset:2048 sc1
MM 60,40,88
s_add_u32 m0, s96, 8192
s_lshl_b32 s81, s93, 16
RT 40,225,0
MM 72,48,88
RT 42,225,4096
RT 44,225,8192
MM 72,52,100
RT 46,225,12288
MM 76,52,104
MM 76,48,92
DM 213,32,81,0
MM 80,48,96
MM 80,52,108
RT 48,226,0
RT 50,226,4096
MM 92,60,108
RT 52,226,8192
RT 54,226,12288
MM 92,56,96
DM 214,32,81,1024
MM 88,56,92
s_bfe_u32 s93, s78, 0x40000
s_lshl1_add_u32 s93, s93, s98
s_add_u32 s93, s93, 24
MM 88,60,104
s_waitcnt vmcnt(22)
s_waitcnt lgkmcnt(15)
s_mul_i32 s94, s93, 6144
MM 84,60,100
s_add_u32 s94, s94, s100
MM 84,56,88
RT 56,227,0
RT 58,227,4096
MM 0,0,112
RT 60,227,8192
RT 62,227,12288
MM 0,4,124
MM 4,4,128
buffer_load_dwordx4 a[88:91], v237, s[28:31], s94 offen offset:0 sc1
MM 4,0,116
MM 8,0,120
s_waitcnt vmcnt(11)
s_barrier
MM 8,4,132
RT 0,220,16384
RT 2,220,20480
MM 20,12,132
buffer_load_dwordx4 a[92:95], v237, s[28:31], s94 offen offset:1024 sc1
MM 20,8,120
RT 4,220,24576
RT 6,220,28672
MM 16,8,116
MM 16,12,128
s_waitcnt lgkmcnt(15)
MM 12,12,124
buffer_load_dwordx4 a[96:99], v237, s[28:31], s94 offen offset:2048 sc1
MM 12,8,112
s_add_u32 m0, s96, 16384
s_lshl_b32 s81, s93, 16
RT 8,221,16384
MM 24,16,112
RT 10,221,20480
RT 12,221,24576
MM 24,20,124
RT 14,221,28672
MM 28,20,128
MM 28,16,116
DM 213,32,81,0
MM 32,16,120
MM 32,20,132
RT 16,222,16384
RT 18,222,20480
MM 44,28,132
RT 20,222,24576
RT 22,222,28672
MM 44,24,120
DM 214,32,81,1024
MM 40,24,116
s_bfe_u32 s93, s78, 0x40000
s_lshl1_add_u32 s93, s93, s98
s_add_u32 s93, s93, 24
MM 40,28,128
s_waitcnt lgkmcnt(15)
s_xor_b32 s93, s93, 1
s_mul_i32 s94, s93, 6144
MM 36,28,124
s_add_u32 s94, s94, s100
MM 36,24,112
RT 24,223,16384
RT 26,223,20480
MM 48,32,112
RT 28,223,24576
RT 30,223,28672
MM 48,36,124
MM 52,36,128
buffer_load_dwordx4 a[100:103], v237, s[28:31], s94 offen offset:0 sc1
MM 52,32,116
MM 56,32,120
MM 56,36,132
RT 32,224,16384
RT 34,224,20480
MM 68,44,132
buffer_load_dwordx4 a[104:107], v237, s[28:31], s94 offen offset:1024 sc1
MM 68,40,120
RT 36,224,24576
RT 38,224,28672
MM 64,40,116
MM 64,44,128
s_waitcnt lgkmcnt(15)
MM 60,44,124
buffer_load_dwordx4 a[108:111], v237, s[28:31], s94 offen offset:2048 sc1
MM 60,40,112
s_add_u32 m0, s96, 24576
s_lshl_b32 s81, s93, 16
RT 40,225,16384
MM 72,48,112
RT 42,225,20480
RT 44,225,24576
MM 72,52,124
RT 46,225,28672
MM 76,52,128
MM 76,48,116
DM 213,32,81,0
MM 80,48,120
MM 80,52,132
RT 48,226,16384
RT 50,226,20480
MM 92,60,132
RT 52,226,24576
RT 54,226,28672
MM 92,56,120
DM 214,32,81,1024
MM 88,56,116
s_bfe_u32 s93, s78, 0x40004
s_lshl1_add_u32 s93, s93, s98
s_add_u32 s93, s93, 24
MM 88,60,128
s_waitcnt vmcnt(22)
s_waitcnt lgkmcnt(15)
s_mul_i32 s94, s93, 6144
MM 84,60,124
s_add_u32 s94, s94, s100
MM 84,56,112
RT 56,227,16384
RT 58,227,20480
MM 0,0,136
RT 60,227,24576
RT 62,227,28672
MM 0,4,148
MM 4,4,152
buffer_load_dwordx4 a[112:115], v237, s[28:31], s94 offen offset:0 sc1
MM 4,0,140
MM 8,0,144
s_waitcnt vmcnt(11)
s_barrier
MM 8,4,156
RT 0,200,0
RT 2,200,4096
MM 20,12,156
buffer_load_dwordx4 a[116:119], v237, s[28:31], s94 offen offset:1024 sc1
MM 20,8,144
RT 4,200,8192
RT 6,200,12288
MM 16,8,140
MM 16,12,152
s_waitcnt lgkmcnt(15)
MM 12,12,148
buffer_load_dwordx4 a[120:123], v237, s[28:31], s94 offen offset:2048 sc1
MM 12,8,136
s_add_u32 m0, s96, 81920
s_lshl_b32 s81, s93, 16
RT 8,201,0
MM 24,16,136
s_getpc_b64 s[62:63]
L_w7:
RT 10,201,4096
RT 12,201,8192
MM 24,20,148
s_add_u32 s62, s62, L_o32-L_w7
s_addc_u32 s63, s63, 0
RT 14,201,12288
MM 28,20,152
v_min_u32 v196, L_o48-L_o32, v211
MM 28,16,140
DM 213,32,81,0
MM 32,16,144
MM 32,20,156
RT 16,202,0
RT 18,202,4096
MM 44,28,156
RT 20,202,8192
RT 22,202,12288
MM 44,24,144
DM 214,32,81,1024
MM 40,24,140
s_bfe_u32 s93, s78, 0x40004
s_lshl1_add_u32 s93, s93, s98
s_add_u32 s93, s93, 24
MM 40,28,152
s_mov_b64 exec, 3
global_load_dword v215, v196, s[62:63]
s_mov_b64 exec, -1
MM 36,28,148
s_waitcnt lgkmcnt(15)
s_xor_b32 s93, s93, 1
s_mul_i32 s94, s93, 6144
MM 36,24,136
s_add_u32 s94, s94, s100
RT 24,203,0
RT 26,203,4096
MM 48,32,136
RT 28,203,8192
RT 30,203,12288
MM 48,36,148
MM 52,36,152
buffer_load_dwordx4 a[124:127], v237, s[28:31], s94 offen offset:0 sc1
MM 52,32,140
MM 56,32,144
MM 56,36,156
RT 32,204,0
RT 34,204,4096
MM 68,44,156
buffer_load_dwordx4 a[128:131], v237, s[28:31], s94 offen offset:1024 sc1
MM 68,40,144
RT 36,204,8192
RT 38,204,12288
MM 64,40,140
MM 64,44,152
s_waitcnt lgkmcnt(15)
MM 60,44,148
buffer_load_dwordx4 a[132:135], v237, s[28:31], s94 offen offset:2048 sc1
MM 60,40,136
s_add_u32 m0, s96, 90112
s_lshl_b32 s81, s93, 16
RT 40,205,0
MM 72,48,136
RT 42,205,4096
RT 44,205,8192
MM 72,52,148
RT 46,205,12288
MM 76,52,152
MM 76,48,140
DM 213,32,81,0
MM 80,48,144
MM 80,52,156
RT 48,206,0
RT 50,206,4096
MM 92,60,156
RT 52,206,8192
RT 54,206,12288
MM 92,56,144
DM 214,32,81,1024
MM 88,56,140
s_bfe_u32 s93, s78, 0x40008
s_lshl1_add_u32 s93, s93, s98
s_add_u32 s93, s93, 24
MM 88,60,152
s_waitcnt vmcnt(23)
s_waitcnt lgkmcnt(15)
s_mul_i32 s94, s93, 6144
MM 84,60,148
s_add_u32 s94, s94, s100
MM 84,56,136
RT 56,207,0
RT 58,207,4096
MM 0,0,64
RT 60,207,8192
RT 62,207,12288
MM 0,4,76
MM 4,4,80
buffer_load_dwordx4 a[136:139], v237, s[28:31], s94 offen offset:0 sc1
MM 4,0,68
MM 8,0,72
s_waitcnt vmcnt(12)
s_barrier
MM 8,4,84
RT 0,200,16384
RT 2,200,20480
MM 20,12,84
buffer_load_dwordx4 a[140:143], v237, s[28:31], s94 offen offset:1024 sc1
MM 20,8,72
RT 4,200,24576
RT 6,200,28672
MM 16,8,68
MM 16,12,80
s_waitcnt lgkmcnt(15)
MM 12,12,76
buffer_load_dwordx4 a[144:147], v237, s[28:31], s94 offen offset:2048 sc1
MM 12,8,64
s_add_u32 m0, s96, 98304
s_lshl_b32 s81, s93, 16
RT 8,201,16384
MM 24,16,64
RT 10,201,20480
RT 12,201,24576
MM 24,20,76
RT 14,201,28672
MM 28,20,80
MM 28,16,68
DM 213,32,81,0
MM 32,16,72
MM 32,20,84
RT 16,202,16384
RT 18,202,20480
MM 44,28,84
RT 20,202,24576
RT 22,202,28672
MM 44,24,72
DM 214,32,81,1024
MM 40,24,68
s_bfe_u32 s93, s78, 0x40008
s_lshl1_add_u32 s93, s93, s98
s_add_u32 s93, s93, 24
MM 40,28,80
s_waitcnt lgkmcnt(15)
s_xor_b32 s93, s93, 1
s_mul_i32 s94, s93, 6144
MM 36,28,76
s_add_u32 s94, s94, s100
MM 36,24,64
RT 24,203,16384
RT 26,203,20480
MM 48,32,64
RT 28,203,24576
RT 30,203,28672
MM 48,36,76
MM 52,36,80
buffer_load_dwordx4 a[148:151], v237, s[28:31], s94 offen offset:0 sc1
MM 52,32,68
MM 56,32,72
MM 56,36,84
RT 32,204,16384
RT 34,204,20480
MM 68,44,84
buffer_load_dwordx4 a[152:155], v237, s[28:31], s94 offen offset:1024 sc1
MM 68,40,72
RT 36,204,24576
RT 38,204,28672
MM 64,40,68
MM 64,44,80
s_waitcnt lgkmcnt(15)
MM 60,44,76
buffer_load_dwordx4 a[156:159], v237, s[28:31], s94 offen offset:2048 sc1
MM 60,40,64
s_add_u32 m0, s96, 106496
s_lshl_b32 s81, s93, 16
RT 40,205,16384
MM 72,48,64
RT 42,205,20480
RT 44,205,24576
MM 72,52,76
RT 46,205,28672
MM 76,52,80
MM 76,48,68
DM 213,32,81,0
MM 80,48,72
MM 80,52,84
RT 48,206,16384
RT 50,206,20480
MM 92,60,84
RT 52,206,24576
RT 54,206,28672
MM 92,56,72
DM 214,32,81,1024
MM 88,56,68
s_bfe_u32 s93, s78, 0x4000c
s_lshl1_add_u32 s93, s93, s98
s_add_u32 s93, s93, 24
MM 88,60,80
s_waitcnt vmcnt(23)
s_waitcnt lgkmcnt(15)
s_mul_i32 s94, s93, 6144
MM 84,60,76
s_add_u32 s94, s94, s100
MM 84,56,64
RT 56,207,16384
RT 58,207,20480
MM 0,0,88
RT 60,207,24576
RT 62,207,28672
MM 0,4,100
MM 4,4,104
buffer_load_dwordx4 a[64:67], v237, s[28:31], s94 offen offset:0 sc1
MM 4,0,92
MM 8,0,96
s_waitcnt vmcnt(11)
s_barrier
MM 8,4,108
RT 0,220,0
RT 2,220,4096
MM 20,12,108
buffer_load_dwordx4 a[68:71], v237, s[28:31], s94 offen offset:1024 sc1
MM 20,8,96
RT 4,220,8192
RT 6,220,12288
MM 16,8,92
MM 16,12,104
s_waitcnt lgkmcnt(15)
MM 12,12,100
buffer_load_dwordx4 a[72:75], v237, s[28:31], s94 offen offset:2048 sc1
MM 12,8,88
s_add_u32 m0, s96, 0
s_lshl_b32 s81, s93, 16
RT 8,221,0
MM 24,16,88
RT 10,221,4096
RT 12,221,8192
MM 24,20,100
RT 14,221,12288
MM 28,20,104
MM 28,16,92
DM 213,32,81,0
MM 32,16,96
MM 32,20,108
RT 16,222,0
RT 18,222,4096
MM 44,28,108
RT 20,222,8192
RT 22,222,12288
MM 44,24,96
DM 214,32,81,1024
MM 40,24,92
s_bfe_u32 s93, s78, 0x4000c
s_lshl1_add_u32 s93, s93, s98
s_add_u32 s93, s93, 24
MM 40,28,104
s_waitcnt lgkmcnt(15)
s_xor_b32 s93, s93, 1
s_mul_i32 s94, s93, 6144
MM 36,28,100
s_add_u32 s94, s94, s100
MM 36,24,88
RT 24,223,0
RT 26,223,4096
MM 48,32,88
RT 28,223,8192
RT 30,223,12288
MM 48,36,100
MM 52,36,104
buffer_load_dwordx4 a[76:79], v237, s[28:31], s94 offen offset:0 sc1
MM 52,32,92
MM 56,32,96
MM 56,36,108
RT 32,224,0
RT 34,224,4096
MM 68,44,108
buffer_load_dwordx4 a[80:83], v237, s[28:31], s94 offen offset:1024 sc1
MM 68,40,96
RT 36,224,8192
RT 38,224,12288
MM 64,40,92
MM 64,44,104
s_waitcnt lgkmcnt(15)
MM 60,44,100
buffer_load_dwordx4 a[84:87], v237, s[28:31], s94 offen offset:2048 sc1
MM 60,40,88
s_add_u32 m0, s96, 8192
s_lshl_b32 s81, s93, 16
RT 40,225,0
MM 72,48,88
RT 42,225,4096
RT 44,225,8192
MM 72,52,100
RT 46,225,12288
MM 76,52,104
MM 76,48,92
DM 213,32,81,0
MM 80,48,96
MM 80,52,108
RT 48,226,0
RT 50,226,4096
MM 92,60,108
RT 52,226,8192
RT 54,226,12288
MM 92,56,96
DM 214,32,81,1024
MM 88,56,92
s_bfe_u32 s93, s78, 0x40000
s_lshl1_add_u32 s93, s93, s98
s_add_u32 s93, s93, 32
MM 88,60,104
s_waitcnt vmcnt(22)
s_waitcnt lgkmcnt(15)
s_mul_i32 s94, s93, 6144
MM 84,60,100
s_add_u32 s94, s94, s100
MM 84,56,88
RT 56,227,0
RT 58,227,4096
MM 0,0,112
RT 60,227,8192
RT 62,227,12288
MM 0,4,124
MM 4,4,128
buffer_load_dwordx4 a[88:91], v237, s[28:31], s94 offen offset:0 sc1
MM 4,0,116
MM 8,0,120
s_waitcnt vmcnt(11)
s_barrier
MM 8,4,132
RT 0,220,16384
RT 2,220,20480
MM 20,12,132
buffer_load_dwordx4 a[92:95], v237, s[28:31], s94 offen offset:1024 sc1
MM 20,8,120
RT 4,220,24576
RT 6,220,28672
MM 16,8,116
MM 16,12,128
s_waitcnt lgkmcnt(15)
MM 12,12,124
buffer_load_dwordx4 a[96:99], v237, s[28:31], s94 offen offset:2048 sc1
MM 12,8,112
s_add_u32 m0, s96, 16384
s_lshl_b32 s81, s93, 16
RT 8,221,16384
MM 24,16,112
RT 10,221,20480
RT 12,221,24576
MM 24,20,124
RT 14,221,28672
MM 28,20,128
MM 28,16,116
DM 213,32,81,0
MM 32,16,120
MM 32,20,132
RT 16,222,16384
RT 18,222,20480
MM 44,28,132
RT 20,222,24576
RT 22,222,28672
MM 44,24,120
DM 214,32,81,1024
MM 40,24,116
s_bfe_u32 s93, s78, 0x40000
s_lshl1_add_u32 s93, s93, s98
s_add_u32 s93, s93, 32
MM 40,28,128
s_waitcnt lgkmcnt(15)
s_xor_b32 s93, s93, 1
s_mul_i32 s94, s93, 6144
MM 36,28,124
s_add_u32 s94, s94, s100
MM 36,24,112
RT 24,223,16384
RT 26,223,20480
MM 48,32,112
RT 28,223,24576
RT 30,223,28672
MM 48,36,124
MM 52,36,128
buffer_load_dwordx4 a[100:103], v237, s[28:31], s94 offen offset:0 sc1
MM 52,32,116
MM 56,32,120
MM 56,36,132
RT 32,224,16384
RT 34,224,20480
MM 68,44,132
buffer_load_dwordx4 a[104:107], v237, s[28:31], s94 offen offset:1024 sc1
MM 68,40,120
RT 36,224,24576
RT 38,224,28672
MM 64,40,116
MM 64,44,128
s_waitcnt lgkmcnt(15)
MM 60,44,124
buffer_load_dwordx4 a[108:111], v237, s[28:31], s94 offen offset:2048 sc1
MM 60,40,112
s_add_u32 m0, s96, 24576
s_lshl_b32 s81, s93, 16
RT 40,225,16384
MM 72,48,112
RT 42,225,20480
RT 44,225,24576
MM 72,52,124
RT 46,225,28672
MM 76,52,128
MM 76,48,116
DM 213,32,81,0
MM 80,48,120
MM 80,52,132
RT 48,226,16384
RT 50,226,20480
MM 92,60,132
RT 52,226,24576
RT 54,226,28672
MM 92,56,120
DM 214,32,81,1024
MM 88,56,116
s_bfe_u32 s93, s78, 0x40004
s_lshl1_add_u32 s93, s93, s98
s_add_u32 s93, s93, 32
MM 88,60,128
s_waitcnt vmcnt(22)
s_waitcnt lgkmcnt(15)
s_mul_i32 s94, s93, 6144
MM 84,60,124
s_add_u32 s94, s94, s100
MM 84,56,112
RT 56,227,16384
RT 58,227,20480
MM 0,0,136
RT 60,227,24576
RT 62,227,28672
MM 0,4,148
MM 4,4,152
buffer_load_dwordx4 a[112:115], v237, s[28:31], s94 offen offset:0 sc1
MM 4,0,140
MM 8,0,144
s_waitcnt vmcnt(11)
s_barrier
MM 8,4,156
RT 0,200,0
RT 2,200,4096
MM 20,12,156
buffer_load_dwordx4 a[116:119], v237, s[28:31], s94 offen offset:1024 sc1
MM 20,8,144
RT 4,200,8192
RT 6,200,12288
MM 16,8,140
MM 16,12,152
s_waitcnt lgkmcnt(15)
MM 12,12,148
buffer_load_dwordx4 a[120:123], v237, s[28:31], s94 offen offset:2048 sc1
MM 12,8,136
s_add_u32 m0, s96, 81920
s_lshl_b32 s81, s93, 16
RT 8,201,0
MM 24,16,136
RT 10,201,4096
RT 12,201,8192
MM 24,20,148
RT 14,201,12288
MM 28,20,152
MM 28,16,140
DM 213,32,81,0
MM 32,16,144
MM 32,20,156
RT 16,202,0
RT 18,202,4096
MM 44,28,156
RT 20,202,8192
RT 22,202,12288
MM 44,24,144
DM 214,32,81,1024
MM 40,24,140
s_bfe_u32 s93, s78, 0x40004
s_lshl1_add_u32 s93, s93, s98
s_add_u32 s93, s93, 32
MM 40,28,152
s_waitcnt lgkmcnt(15)
s_xor_b32 s93, s93, 1
s_mul_i32 s94, s93, 6144
MM 36,28,148
s_add_u32 s94, s94, s100
MM 36,24,136
RT 24,203,0
RT 26,203,4096
MM 48,32,136
RT 28,203,8192
RT 30,203,12288
MM 48,36,148
MM 52,36,152
buffer_load_dwordx4 a[124:127], v237, s[28:31], s94 offen offset:0 sc1
MM 52,32,140
MM 56,32,144
MM 56,36,156
RT 32,204,0
RT 34,204,4096
MM 68,44,156
buffer_load_dwordx4 a[128:131], v237, s[28:31], s94 offen offset:1024 sc1
MM 68,40,144
RT 36,204,8192
RT 38,204,12288
MM 64,40,140
MM 64,44,152
s_waitcnt lgkmcnt(15)
MM 60,44,148
buffer_load_dwordx4 a[132:135], v237, s[28:31], s94 offen offset:2048 sc1
MM 60,40,136
s_add_u32 m0, s96, 90112
s_lshl_b32 s81, s93, 16
RT 40,205,0
MM 72,48,136
RT 42,205,4096
RT 44,205,8192
MM 72,52,148
RT 46,205,12288
MM 76,52,152
MM 76,48,140
DM 213,32,81,0
MM 80,48,144
MM 80,52,156
RT 48,206,0
RT 50,206,4096
MM 92,60,156
RT 52,206,8192
RT 54,206,12288
MM 92,56,144
DM 214,32,81,1024
MM 88,56,140
s_bfe_u32 s93, s78, 0x40008
s_lshl1_add_u32 s93, s93, s98
s_add_u32 s93, s93, 32
MM 88,60,152
s_waitcnt vmcnt(22)
s_waitcnt lgkmcnt(15)
s_mul_i32 s94, s93, 6144
MM 84,60,148
s_add_u32 s94, s94, s100
MM 84,56,136
RT 56,207,0
RT 58,207,4096
L_o32:
MM 0,0,64
RT 60,207,8192
RT 62,207,12288
MM 0,4,76
MM 4,4,80
buffer_load_dwordx4 a[136:139], v237, s[28:31], s94 offen offset:0 sc1
MM 4,0,68
MM 8,0,72
s_waitcnt vmcnt(11)
s_barrier
MM 8,4,84
RT 0,200,16384
RT 2,200,20480
MM 20,12,84
buffer_load_dwordx4 a[140:143], v237, s[28:31], s94 offen offset:1024 sc1
MM 20,8,72
RT 4,200,24576
RT 6,200,28672
MM 16,8,68
MM 16,12,80
s_waitcnt lgkmcnt(15)
MM 12,12,76
buffer_load_dwordx4 a[144:147], v237, s[28:31], s94 offen offset:2048 sc1
MM 12,8,64
s_add_u32 m0, s96, 98304
s_lshl_b32 s81, s93, 16
RT 8,201,16384
MM 24,16,64
RT 10,201,20480
RT 12,201,24576
MM 24,20,76
RT 14,201,28672
MM 28,20,80
MM 28,16,68
DM 213,32,81,0
MM 32,16,72
MM 32,20,84
RT 16,202,16384
RT 18,202,20480
MM 44,28,84
RT 20,202,24576
RT 22,202,28672
MM 44,24,72
DM 214,32,81,1024
MM 40,24,68
s_bfe_u32 s93, s78, 0x40008
s_lshl1_add_u32 s93, s93, s98
s_add_u32 s93, s93, 32
MM 40,28,80
s_waitcnt lgkmcnt(15)
s_xor_b32 s93, s93, 1
s_mul_i32 s94, s93, 6144
MM 36,28,76
s_add_u32 s94, s94, s100
MM 36,24,64
RT 24,203,16384
RT 26,203,20480
MM 48,32,64
RT 28,203,24576
RT 30,203,28672
MM 48,36,76
MM 52,36,80
buffer_load_dwordx4 a[148:151], v237, s[28:31], s94 offen offset:0 sc1
MM 52,32,68
MM 56,32,72
MM 56,36,84
RT 32,204,16384
RT 34,204,20480
MM 68,44,84
buffer_load_dwordx4 a[152:155], v237, s[28:31], s94 offen offset:1024 sc1
MM 68,40,72
RT 36,204,24576
RT 38,204,28672
MM 64,40,68
MM 64,44,80
s_waitcnt lgkmcnt(15)
MM 60,44,76
buffer_load_dwordx4 a[156:159], v237, s[28:31], s94 offen offset:2048 sc1
MM 60,40,64
s_add_u32 m0, s96, 106496
s_lshl_b32 s81, s93, 16
RT 40,205,16384
MM 72,48,64
RT 42,205,20480
RT 44,205,24576
MM 72,52,76
RT 46,205,28672
MM 76,52,80
MM 76,48,68
DM 213,32,81,0
MM 80,48,72
MM 80,52,84
RT 48,206,16384
RT 50,206,20480
MM 92,60,84
RT 52,206,24576
RT 54,206,28672
MM 92,56,72
DM 214,32,81,1024
MM 88,56,68
s_bfe_u32 s93, s78, 0x4000c
s_lshl1_add_u32 s93, s93, s98
s_add_u32 s93, s93, 32
MM 88,60,80
s_waitcnt vmcnt(22)
s_waitcnt lgkmcnt(15)
s_mul_i32 s94, s93, 6144
MM 84,60,76
s_add_u32 s94, s94, s100
MM 84,56,64
RT 56,207,16384
RT 58,207,20480
MM 0,0,88
RT 60,207,24576
RT 62,207,28672
MM 0,4,100
MM 4,4,104
buffer_load_dwordx4 a[64:67], v237, s[28:31], s94 offen offset:0 sc1
MM 4,0,92
MM 8,0,96
s_waitcnt vmcnt(11)
s_barrier
MM 8,4,108
RT 0,220,0
RT 2,220,4096
MM 20,12,108
buffer_load_dwordx4 a[68:71], v237, s[28:31], s94 offen offset:1024 sc1
MM 20,8,96
RT 4,220,8192
RT 6,220,12288
MM 16,8,92
)ASM"
R"ASM(MM 16,12,104
s_waitcnt lgkmcnt(15)
MM 12,12,100
buffer_load_dwordx4 a[72:75], v237, s[28:31], s94 offen offset:2048 sc1
MM 12,8,88
s_add_u32 m0, s96, 0
s_lshl_b32 s81, s93, 16
RT 8,221,0
MM 24,16,88
RT 10,221,4096
RT 12,221,8192
MM 24,20,100
RT 14,221,12288
MM 28,20,104
MM 28,16,92
DM 213,32,81,0
MM 32,16,96
MM 32,20,108
RT 16,222,0
RT 18,222,4096
MM 44,28,108
RT 20,222,8192
RT 22,222,12288
MM 44,24,96
DM 214,32,81,1024
MM 40,24,92
s_bfe_u32 s93, s78, 0x4000c
s_lshl1_add_u32 s93, s93, s98
s_add_u32 s93, s93, 32
MM 40,28,104
s_waitcnt lgkmcnt(15)
s_xor_b32 s93, s93, 1
s_mul_i32 s94, s93, 6144
MM 36,28,100
s_add_u32 s94, s94, s100
MM 36,24,88
RT 24,223,0
RT 26,223,4096
MM 48,32,88
RT 28,223,8192
RT 30,223,12288
MM 48,36,100
MM 52,36,104
buffer_load_dwordx4 a[76:79], v237, s[28:31], s94 offen offset:0 sc1
MM 52,32,92
MM 56,32,96
MM 56,36,108
RT 32,224,0
RT 34,224,4096
MM 68,44,108
buffer_load_dwordx4 a[80:83], v237, s[28:31], s94 offen offset:1024 sc1
MM 68,40,96
RT 36,224,8192
RT 38,224,12288
MM 64,40,92
MM 64,44,104
s_waitcnt lgkmcnt(15)
MM 60,44,100
buffer_load_dwordx4 a[84:87], v237, s[28:31], s94 offen offset:2048 sc1
MM 60,40,88
s_add_u32 m0, s96, 8192
s_lshl_b32 s81, s93, 16
RT 40,225,0
MM 72,48,88
RT 42,225,4096
RT 44,225,8192
MM 72,52,100
RT 46,225,12288
MM 76,52,104
MM 76,48,92
DM 213,32,81,0
MM 80,48,96
MM 80,52,108
RT 48,226,0
RT 50,226,4096
MM 92,60,108
RT 52,226,8192
RT 54,226,12288
MM 92,56,96
DM 214,32,81,1024
MM 88,56,92
s_bfe_u32 s93, s78, 0x40000
s_lshl1_add_u32 s93, s93, s98
s_add_u32 s93, s93, 40
MM 88,60,104
s_waitcnt vmcnt(22)
s_waitcnt lgkmcnt(15)
s_mul_i32 s94, s93, 6144
MM 84,60,100
s_add_u32 s94, s94, s100
MM 84,56,88
RT 56,227,0
RT 58,227,4096
MM 0,0,112
RT 60,227,8192
RT 62,227,12288
MM 0,4,124
MM 4,4,128
buffer_load_dwordx4 a[88:91], v237, s[28:31], s94 offen offset:0 sc1
MM 4,0,116
MM 8,0,120
s_waitcnt vmcnt(11)
s_barrier
MM 8,4,132
RT 0,220,16384
RT 2,220,20480
MM 20,12,132
buffer_load_dwordx4 a[92:95], v237, s[28:31], s94 offen offset:1024 sc1
MM 20,8,120
RT 4,220,24576
RT 6,220,28672
MM 16,8,116
MM 16,12,128
s_waitcnt lgkmcnt(15)
MM 12,12,124
buffer_load_dwordx4 a[96:99], v237, s[28:31], s94 offen offset:2048 sc1
MM 12,8,112
s_add_u32 m0, s96, 16384
s_lshl_b32 s81, s93, 16
RT 8,221,16384
MM 24,16,112
RT 10,221,20480
RT 12,221,24576
MM 24,20,124
RT 14,221,28672
MM 28,20,128
MM 28,16,116
DM 213,32,81,0
MM 32,16,120
MM 32,20,132
RT 16,222,16384
RT 18,222,20480
MM 44,28,132
RT 20,222,24576
RT 22,222,28672
MM 44,24,120
DM 214,32,81,1024
MM 40,24,116
s_bfe_u32 s93, s78, 0x40000
s_lshl1_add_u32 s93, s93, s98
s_add_u32 s93, s93, 40
MM 40,28,128
s_waitcnt lgkmcnt(15)
s_xor_b32 s93, s93, 1
s_mul_i32 s94, s93, 6144
MM 36,28,124
s_add_u32 s94, s94, s100
MM 36,24,112
RT 24,223,16384
RT 26,223,20480
MM 48,32,112
RT 28,223,24576
RT 30,223,28672
MM 48,36,124
MM 52,36,128
buffer_load_dwordx4 a[100:103], v237, s[28:31], s94 offen offset:0 sc1
MM 52,32,116
MM 56,32,120
MM 56,36,132
RT 32,224,16384
RT 34,224,20480
MM 68,44,132
buffer_load_dwordx4 a[104:107], v237, s[28:31], s94 offen offset:1024 sc1
MM 68,40,120
RT 36,224,24576
RT 38,224,28672
MM 64,40,116
MM 64,44,128
s_waitcnt lgkmcnt(15)
MM 60,44,124
buffer_load_dwordx4 a[108:111], v237, s[28:31], s94 offen offset:2048 sc1
MM 60,40,112
s_add_u32 m0, s96, 24576
s_lshl_b32 s81, s93, 16
RT 40,225,16384
MM 72,48,112
RT 42,225,20480
RT 44,225,24576
MM 72,52,124
RT 46,225,28672
MM 76,52,128
MM 76,48,116
DM 213,32,81,0
MM 80,48,120
MM 80,52,132
RT 48,226,16384
RT 50,226,20480
MM 92,60,132
RT 52,226,24576
RT 54,226,28672
MM 92,56,120
DM 214,32,81,1024
MM 88,56,116
s_bfe_u32 s93, s78, 0x40004
s_lshl1_add_u32 s93, s93, s98
s_add_u32 s93, s93, 40
MM 88,60,128
s_waitcnt vmcnt(22)
s_waitcnt lgkmcnt(15)
s_mul_i32 s94, s93, 6144
MM 84,60,124
s_add_u32 s94, s94, s100
MM 84,56,112
RT 56,227,16384
RT 58,227,20480
MM 0,0,136
RT 60,227,24576
RT 62,227,28672
MM 0,4,148
MM 4,4,152
buffer_load_dwordx4 a[112:115], v237, s[28:31], s94 offen offset:0 sc1
MM 4,0,140
MM 8,0,144
s_waitcnt vmcnt(11)
s_barrier
MM 8,4,156
RT 0,200,0
RT 2,200,4096
MM 20,12,156
buffer_load_dwordx4 a[116:119], v237, s[28:31], s94 offen offset:1024 sc1
MM 20,8,144
RT 4,200,8192
RT 6,200,12288
MM 16,8,140
MM 16,12,152
s_waitcnt lgkmcnt(15)
MM 12,12,148
buffer_load_dwordx4 a[120:123], v237, s[28:31], s94 offen offset:2048 sc1
MM 12,8,136
s_add_u32 m0, s96, 81920
s_lshl_b32 s81, s93, 16
RT 8,201,0
MM 24,16,136
s_getpc_b64 s[62:63]
L_w8:
RT 10,201,4096
RT 12,201,8192
MM 24,20,148
s_add_u32 s62, s62, L_o48-L_w8
s_addc_u32 s63, s63, 0
RT 14,201,12288
MM 28,20,152
v_min_u32 v196, L_o64-L_o48, v211
MM 28,16,140
DM 213,32,81,0
MM 32,16,144
MM 32,20,156
RT 16,202,0
RT 18,202,4096
MM 44,28,156
RT 20,202,8192
RT 22,202,12288
MM 44,24,144
DM 214,32,81,1024
MM 40,24,140
s_bfe_u32 s93, s78, 0x40004
s_lshl1_add_u32 s93, s93, s98
s_add_u32 s93, s93, 40
MM 40,28,152
s_mov_b64 exec, 3
global_load_dword v215, v196, s[62:63]
s_mov_b64 exec, -1
MM 36,28,148
s_waitcnt lgkmcnt(15)
s_xor_b32 s93, s93, 1
s_mul_i32 s94, s93, 6144
MM 36,24,136
s_add_u32 s94, s94, s100
RT 24,203,0
RT 26,203,4096
MM 48,32,136
RT 28,203,8192
RT 30,203,12288
MM 48,36,148
MM 52,36,152
buffer_load_dwordx4 a[124:127], v237, s[28:31], s94 offen offset:0 sc1
MM 52,32,140
MM 56,32,144
MM 56,36,156
RT 32,204,0
RT 34,204,4096
MM 68,44,156
buffer_load_dwordx4 a[128:131], v237, s[28:31], s94 offen offset:1024 sc1
MM 68,40,144
RT 36,204,8192
RT 38,204,12288
MM 64,40,140
MM 64,44,152
s_waitcnt lgkmcnt(15)
MM 60,44,148
buffer_load_dwordx4 a[132:135], v237, s[28:31], s94 offen offset:2048 sc1
MM 60,40,136
s_add_u32 m0, s96, 90112
s_lshl_b32 s81, s93, 16
RT 40,205,0
MM 72,48,136
RT 42,205,4096
RT 44,205,8192
MM 72,52,148
RT 46,205,12288
MM 76,52,152
MM 76,48,140
DM 213,32,81,0
MM 80,48,144
MM 80,52,156
RT 48,206,0
RT 50,206,4096
MM 92,60,156
RT 52,206,8192
RT 54,206,12288
MM 92,56,144
DM 214,32,81,1024
MM 88,56,140
s_bfe_u32 s93, s78, 0x40008
s_lshl1_add_u32 s93, s93, s98
s_add_u32 s93, s93, 40
MM 88,60,152
s_waitcnt vmcnt(23)
s_waitcnt lgkmcnt(15)
s_mul_i32 s94, s93, 6144
MM 84,60,148
s_add_u32 s94, s94, s100
MM 84,56,136
RT 56,207,0
RT 58,207,4096
MM 0,0,64
RT 60,207,8192
RT 62,207,12288
MM 0,4,76
MM 4,4,80
buffer_load_dwordx4 a[136:139], v237, s[28:31], s94 offen offset:0 sc1
MM 4,0,68
MM 8,0,72
s_waitcnt vmcnt(12)
s_barrier
MM 8,4,84
RT 0,200,16384
RT 2,200,20480
MM 20,12,84
buffer_load_dwordx4 a[140:143], v237, s[28:31], s94 offen offset:1024 sc1
MM 20,8,72
RT 4,200,24576
RT 6,200,28672
MM 16,8,68
MM 16,12,80
s_waitcnt lgkmcnt(15)
MM 12,12,76
buffer_load_dwordx4 a[144:147], v237, s[28:31], s94 offen offset:2048 sc1
MM 12,8,64
s_add_u32 m0, s96, 98304
s_lshl_b32 s81, s93, 16
RT 8,201,16384
MM 24,16,64
RT 10,201,20480
RT 12,201,24576
MM 24,20,76
RT 14,201,28672
MM 28,20,80
MM 28,16,68
DM 213,32,81,0
MM 32,16,72
MM 32,20,84
RT 16,202,16384
RT 18,202,20480
MM 44,28,84
RT 20,202,24576
RT 22,202,28672
MM 44,24,72
DM 214,32,81,1024
MM 40,24,68
s_bfe_u32 s93, s78, 0x40008
s_lshl1_add_u32 s93, s93, s98
s_add_u32 s93, s93, 40
MM 40,28,80
s_waitcnt lgkmcnt(15)
s_xor_b32 s93, s93, 1
s_mul_i32 s94, s93, 6144
MM 36,28,76
s_add_u32 s94, s94, s100
MM 36,24,64
RT 24,203,16384
RT 26,203,20480
MM 48,32,64
RT 28,203,24576
RT 30,203,28672
MM 48,36,76
MM 52,36,80
buffer_load_dwordx4 a[148:151], v237, s[28:31], s94 offen offset:0 sc1
MM 52,32,68
MM 56,32,72
MM 56,36,84
RT 32,204,16384
RT 34,204,20480
MM 68,44,84
buffer_load_dwordx4 a[152:155], v237, s[28:31], s94 offen offset:1024 sc1
MM 68,40,72
RT 36,204,24576
RT 38,204,28672
MM 64,40,68
MM 64,44,80
s_waitcnt lgkmcnt(15)
MM 60,44,76
buffer_load_dwordx4 a[156:159], v237, s[28:31], s94 offen offset:2048 sc1
MM 60,40,64
s_add_u32 m0, s96, 106496
s_lshl_b32 s81, s93, 16
RT 40,205,16384
MM 72,48,64
RT 42,205,20480
RT 44,205,24576
MM 72,52,76
RT 46,205,28672
MM 76,52,80
MM 76,48,68
DM 213,32,81,0
MM 80,48,72
MM 80,52,84
RT 48,206,16384
RT 50,206,20480
MM 92,60,84
RT 52,206,24576
RT 54,206,28672
MM 92,56,72
DM 214,32,81,1024
MM 88,56,68
s_bfe_u32 s93, s78, 0x4000c
s_lshl1_add_u32 s93, s93, s98
s_add_u32 s93, s93, 40
MM 88,60,80
s_waitcnt vmcnt(23)
s_waitcnt lgkmcnt(15)
s_mul_i32 s94, s93, 6144
MM 84,60,76
s_add_u32 s94, s94, s100
MM 84,56,64
RT 56,207,16384
RT 58,207,20480
MM 0,0,88
RT 60,207,24576
RT 62,207,28672
MM 0,4,100
MM 4,4,104
buffer_load_dwordx4 a[64:67], v237, s[28:31], s94 offen offset:0 sc1
MM 4,0,92
MM 8,0,96
s_waitcnt vmcnt(11)
s_barrier
MM 8,4,108
RT 0,220,0
RT 2,220,4096
MM 20,12,108
buffer_load_dwordx4 a[68:71], v237, s[28:31], s94 offen offset:1024 sc1
MM 20,8,96
RT 4,220,8192
RT 6,220,12288
MM 16,8,92
MM 16,12,104
s_waitcnt lgkmcnt(15)
MM 12,12,100
buffer_load_dwordx4 a[72:75], v237, s[28:31], s94 offen offset:2048 sc1
MM 12,8,88
s_add_u32 m0, s96, 0
s_lshl_b32 s81, s93, 16
RT 8,221,0
MM 24,16,88
RT 10,221,4096
RT 12,221,8192
MM 24,20,100
RT 14,221,12288
MM 28,20,104
MM 28,16,92
DM 213,32,81,0
MM 32,16,96
MM 32,20,108
RT 16,222,0
RT 18,222,4096
MM 44,28,108
RT 20,222,8192
RT 22,222,12288
MM 44,24,96
DM 214,32,81,1024
MM 40,24,92
s_bfe_u32 s93, s78, 0x4000c
s_lshl1_add_u32 s93, s93, s98
s_add_u32 s93, s93, 40
MM 40,28,104
s_waitcnt lgkmcnt(15)
s_xor_b32 s93, s93, 1
s_mul_i32 s94, s93, 6144
MM 36,28,100
s_add_u32 s94, s94, s100
MM 36,24,88
RT 24,223,0
RT 26,223,4096
MM 48,32,88
RT 28,223,8192
RT 30,223,12288
MM 48,36,100
MM 52,36,104
buffer_load_dwordx4 a[76:79], v237, s[28:31], s94 offen offset:0 sc1
MM 52,32,92
MM 56,32,96
MM 56,36,108
RT 32,224,0
RT 34,224,4096
MM 68,44,108
buffer_load_dwordx4 a[80:83], v237, s[28:31], s94 offen offset:1024 sc1
MM 68,40,96
RT 36,224,8192
RT 38,224,12288
MM 64,40,92
MM 64,44,104
s_waitcnt lgkmcnt(15)
MM 60,44,100
buffer_load_dwordx4 a[84:87], v237, s[28:31], s94 offen offset:2048 sc1
MM 60,40,88
s_add_u32 m0, s96, 8192
s_lshl_b32 s81, s93, 16
RT 40,225,0
MM 72,48,88
RT 42,225,4096
RT 44,225,8192
MM 72,52,100
RT 46,225,12288
MM 76,52,104
MM 76,48,92
DM 213,32,81,0
MM 80,48,96
MM 80,52,108
RT 48,226,0
RT 50,226,4096
MM 92,60,108
RT 52,226,8192
RT 54,226,12288
MM 92,56,96
DM 214,32,81,1024
MM 88,56,92
s_bfe_u32 s93, s78, 0x40010
s_lshl1_add_u32 s93, s93, s98
s_add_u32 s93, s93, 48
MM 88,60,104
s_waitcnt vmcnt(22)
s_waitcnt lgkmcnt(15)
s_mul_i32 s94, s93, 6144
MM 84,60,100
s_add_u32 s94, s94, s100
MM 84,56,88
RT 56,227,0
RT 58,227,4096
MM 0,0,112
RT 60,227,8192
RT 62,227,12288
MM 0,4,124
MM 4,4,128
buffer_load_dwordx4 a[88:91], v237, s[28:31], s94 offen offset:0 sc1
MM 4,0,116
MM 8,0,120
s_waitcnt vmcnt(11)
s_barrier
MM 8,4,132
RT 0,220,16384
RT 2,220,20480
MM 20,12,132
buffer_load_dwordx4 a[92:95], v237, s[28:31], s94 offen offset:1024 sc1
MM 20,8,120
RT 4,220,24576
RT 6,220,28672
MM 16,8,116
MM 16,12,128
s_waitcnt lgkmcnt(15)
MM 12,12,124
buffer_load_dwordx4 a[96:99], v237, s[28:31], s94 offen offset:2048 sc1
MM 12,8,112
s_add_u32 m0, s96, 16384
s_lshl_b32 s81, s93, 16
RT 8,221,16384
MM 24,16,112
RT 10,221,20480
RT 12,221,24576
MM 24,20,124
RT 14,221,28672
MM 28,20,128
MM 28,16,116
DM 213,32,81,0
MM 32,16,120
MM 32,20,132
RT 16,222,16384
RT 18,222,20480
MM 44,28,132
RT 20,222,24576
RT 22,222,28672
MM 44,24,120
DM 214,32,81,1024
MM 40,24,116
s_bfe_u32 s93, s78, 0x40010
s_lshl1_add_u32 s93, s93, s98
s_add_u32 s93, s93, 48
MM 40,28,128
s_waitcnt lgkmcnt(15)
s_xor_b32 s93, s93, 1
s_mul_i32 s94, s93, 6144
MM 36,28,124
s_add_u32 s94, s94, s100
MM 36,24,112
RT 24,223,16384
RT 26,223,20480
MM 48,32,112
RT 28,223,24576
RT 30,223,28672
MM 48,36,124
MM 52,36,128
buffer_load_dwordx4 a[100:103], v237, s[28:31], s94 offen offset:0 sc1
MM 52,32,116
MM 56,32,120
MM 56,36,132
RT 32,224,16384
RT 34,224,20480
MM 68,44,132
buffer_load_dwordx4 a[104:107], v237, s[28:31], s94 offen offset:1024 sc1
MM 68,40,120
RT 36,224,24576
RT 38,224,28672
MM 64,40,116
MM 64,44,128
s_waitcnt lgkmcnt(15)
MM 60,44,124
buffer_load_dwordx4 a[108:111], v237, s[28:31], s94 offen offset:2048 sc1
MM 60,40,112
s_add_u32 m0, s96, 24576
s_lshl_b32 s81, s93, 16
RT 40,225,16384
MM 72,48,112
RT 42,225,20480
RT 44,225,24576
MM 72,52,124
RT 46,225,28672
MM 76,52,128
MM 76,48,116
DM 213,32,81,0
MM 80,48,120
MM 80,52,132
RT 48,226,16384
RT 50,226,20480
MM 92,60,132
RT 52,226,24576
RT 54,226,28672
MM 92,56,120
DM 214,32,81,1024
MM 88,56,116
s_bfe_u32 s93, s78, 0x40014
s_lshl1_add_u32 s93, s93, s98
s_add_u32 s93, s93, 48
MM 88,60,128
s_waitcnt vmcnt(22)
s_waitcnt lgkmcnt(15)
s_mul_i32 s94, s93, 6144
MM 84,60,124
s_add_u32 s94, s94, s100
MM 84,56,112
RT 56,227,16384
RT 58,227,20480
MM 0,0,136
RT 60,227,24576
RT 62,227,28672
MM 0,4,148
MM 4,4,152
buffer_load_dwordx4 a[112:115], v237, s[28:31], s94 offen offset:0 sc1
MM 4,0,140
MM 8,0,144
s_waitcnt vmcnt(11)
s_barrier
MM 8,4,156
RT 0,200,0
RT 2,200,4096
MM 20,12,156
buffer_load_dwordx4 a[116:119], v237, s[28:31], s94 offen offset:1024 sc1
MM 20,8,144
RT 4,200,8192
RT 6,200,12288
MM 16,8,140
MM 16,12,152
s_waitcnt lgkmcnt(15)
MM 12,12,148
buffer_load_dwordx4 a[120:123], v237, s[28:31], s94 offen offset:2048 sc1
MM 12,8,136
s_add_u32 m0, s96, 81920
s_lshl_b32 s81, s93, 16
RT 8,201,0
MM 24,16,136
RT 10,201,4096
RT 12,201,8192
MM 24,20,148
RT 14,201,12288
MM 28,20,152
MM 28,16,140
DM 213,32,81,0
MM 32,16,144
MM 32,20,156
RT 16,202,0
RT 18,202,4096
MM 44,28,156
RT 20,202,8192
RT 22,202,12288
MM 44,24,144
DM 214,32,81,1024
MM 40,24,140
s_bfe_u32 s93, s78, 0x40014
s_lshl1_add_u32 s93, s93, s98
s_add_u32 s93, s93, 48
MM 40,28,152
s_waitcnt lgkmcnt(15)
s_xor_b32 s93, s93, 1
s_mul_i32 s94, s93, 6144
MM 36,28,148
s_add_u32 s94, s94, s100
MM 36,24,136
RT 24,203,0
RT 26,203,4096
MM 48,32,136
RT 28,203,8192
RT 30,203,12288
MM 48,36,148
MM 52,36,152
buffer_load_dwordx4 a[124:127], v237, s[28:31], s94 offen offset:0 sc1
MM 52,32,140
MM 56,32,144
MM 56,36,156
RT 32,204,0
RT 34,204,4096
MM 68,44,156
buffer_load_dwordx4 a[128:131], v237, s[28:31], s94 offen offset:1024 sc1
MM 68,40,144
RT 36,204,8192
RT 38,204,12288
MM 64,40,140
MM 64,44,152
s_waitcnt lgkmcnt(15)
MM 60,44,148
buffer_load_dwordx4 a[132:135], v237, s[28:31], s94 offen offset:2048 sc1
MM 60,40,136
s_add_u32 m0, s96, 90112
s_lshl_b32 s81, s93, 16
RT 40,205,0
MM 72,48,136
RT 42,205,4096
RT 44,205,8192
MM 72,52,148
RT 46,205,12288
MM 76,52,152
MM 76,48,140
DM 213,32,81,0
MM 80,48,144
MM 80,52,156
RT 48,206,0
RT 50,206,4096
MM 92,60,156
RT 52,206,8192
RT 54,206,12288
MM 92,56,144
DM 214,32,81,1024
MM 88,56,140
s_bfe_u32 s93, s78, 0x40018
s_lshl1_add_u32 s93, s93, s98
s_add_u32 s93, s93, 48
MM 88,60,152
s_waitcnt vmcnt(22)
s_waitcnt lgkmcnt(15)
s_mul_i32 s94, s93, 6144
MM 84,60,148
s_add_u32 s94, s94, s100
MM 84,56,136
RT 56,207,0
RT 58,207,4096
L_o48:
MM 0,0,64
RT 60,207,8192
RT 62,207,12288
MM 0,4,76
MM 4,4,80
buffer_load_dwordx4 a[136:139], v237, s[28:31], s94 offen offset:0 sc1
MM 4,0,68
MM 8,0,72
s_waitcnt vmcnt(11)
s_barrier
MM 8,4,84
RT 0,200,16384
RT 2,200,20480
MM 20,12,84
buffer_load_dwordx4 a[140:143], v237, s[28:31], s94 offen offset:1024 sc1
MM 20,8,72
RT 4,200,24576
RT 6,200,28672
MM 16,8,68
MM 16,12,80
s_waitcnt lgkmcnt(15)
MM 12,12,76
buffer_load_dwordx4 a[144:147], v237, s[28:31], s94 offen offset:2048 sc1
MM 12,8,64
s_add_u32 m0, s96, 98304
s_lshl_b32 s81, s93, 16
RT 8,201,16384
MM 24,16,64
RT 10,201,20480
RT 12,201,24576
MM 24,20,76
RT 14,201,28672
MM 28,20,80
MM 28,16,68
DM 213,32,81,0
MM 32,16,72
MM 32,20,84
RT 16,202,16384
RT 18,202,20480
MM 44,28,84
RT 20,202,24576
RT 22,202,28672
MM 44,24,72
DM 214,32,81,1024
MM 40,24,68
s_bfe_u32 s93, s78, 0x40018
s_lshl1_add_u32 s93, s93, s98
s_add_u32 s93, s93, 48
MM 40,28,80
s_waitcnt lgkmcnt(15)
s_xor_b32 s93, s93, 1
s_mul_i32 s94, s93, 6144
MM 36,28,76
s_add_u32 s94, s94, s100
MM 36,24,64
RT 24,203,16384
RT 26,203,20480
MM 48,32,64
RT 28,203,24576
RT 30,203,28672
MM 48,36,76
MM 52,36,80
buffer_load_dwordx4 a[148:151], v237, s[28:31], s94 offen offset:0 sc1
MM 52,32,68
MM 56,32,72
MM 56,36,84
RT 32,204,16384
RT 34,204,20480
MM 68,44,84
buffer_load_dwordx4 a[152:155], v237, s[28:31], s94 offen offset:1024 sc1
MM 68,40,72
RT 36,204,24576
RT 38,204,28672
MM 64,40,68
MM 64,44,80
s_waitcnt lgkmcnt(15)
MM 60,44,76
buffer_load_dwordx4 a[156:159], v237, s[28:31], s94 offen offset:2048 sc1
MM 60,40,64
s_add_u32 m0, s96, 106496
s_lshl_b32 s81, s93, 16
RT 40,205,16384
MM 72,48,64
RT 42,205,20480
RT 44,205,24576
MM 72,52,76
RT 46,205,28672
MM 76,52,80
MM 76,48,68
DM 213,32,81,0
MM 80,48,72
MM 80,52,84
RT 48,206,16384
RT 50,206,20480
MM 92,60,84
RT 52,206,24576
RT 54,206,28672
MM 92,56,72
DM 214,32,81,1024
MM 88,56,68
s_bfe_u32 s93, s78, 0x4001c
s_lshl1_add_u32 s93, s93, s99
s_mul_i32 s94, s93, 6144
MM 88,60,80
s_waitcnt vmcnt(22)
s_waitcnt lgkmcnt(15)
s_add_u32 s94, s94, s101
MM 84,60,76
MM 84,56,64
RT 56,207,16384
RT 58,207,20480
MM 0,0,88
RT 60,207,24576
RT 62,207,28672
MM 0,4,100
MM 4,4,104
buffer_load_dwordx4 a[64:67], v237, s[28:31], s94 offen offset:0 sc1
MM 4,0,92
MM 8,0,96
s_waitcnt vmcnt(11)
s_barrier
MM 8,4,108
RT 0,220,0
RT 2,220,4096
MM 20,12,108
RT 4,220,8192
RT 6,220,12288
MM 20,8,96
MM 16,8,92
MM 16,12,104
buffer_load_dwordx4 a[68:71], v237, s[28:31], s94 offen offset:1024 sc1
MM 12,12,100
s_waitcnt lgkmcnt(15)
MM 12,8,88
RT 8,221,0
RT 10,221,4096
MM 24,16,88
RT 12,221,8192
RT 14,221,12288
MM 24,20,100
MM 28,20,104
MM 28,16,92
MM 32,16,96
buffer_load_dwordx4 a[72:75], v237, s[28:31], s94 offen offset:2048 sc1
MM 32,20,108
RT 16,222,0
RT 18,222,4096
MM 44,28,108
s_bfe_u32 s93, s78, 0x4001c
s_lshl1_add_u32 s93, s93, s99
s_xor_b32 s93, s93, 1
MM 44,24,96
s_mul_i32 s94, s93, 6144
s_add_u32 s94, s94, s101
RT 20,222,8192
MM 40,24,92
RT 22,222,12288
MM 40,28,104
s_waitcnt lgkmcnt(15)
MM 36,28,100
MM 36,24,88
RT 24,223,0
RT 26,223,4096
MM 48,32,88
RT 28,223,8192
RT 30,223,12288
MM 48,36,100
MM 52,36,104
buffer_load_dwordx4 a[76:79], v237, s[28:31], s94 offen offset:0 sc1
MM 52,32,92
MM 56,32,96
MM 56,36,108
RT 32,224,0
RT 34,224,4096
MM 68,44,108
RT 36,224,8192
RT 38,224,12288
MM 68,40,96
MM 64,40,92
MM 64,44,104
buffer_load_dwordx4 a[80:83], v237, s[28:31], s94 offen offset:1024 sc1
MM 60,44,100
s_waitcnt lgkmcnt(15)
MM 60,40,88
RT 40,225,0
RT 42,225,4096
MM 72,48,88
RT 44,225,8192
RT 46,225,12288
MM 72,52,100
MM 76,52,104
MM 76,48,92
MM 80,48,96
buffer_load_dwordx4 a[84:87], v237, s[28:31], s94 offen offset:2048 sc1
MM 80,52,108
RT 48,226,0
RT 50,226,4096
MM 92,60,108
s_bfe_u32 s93, s78, 0x40010
s_lshl1_add_u32 s93, s93, s99
s_mul_i32 s94, s93, 6144
MM 92,56,96
s_add_u32 s94, s94, s101
RT 52,226,8192
RT 54,226,12288
MM 88,56,92
MM 88,60,104
s_waitcnt vmcnt(18)
s_waitcnt lgkmcnt(15)
MM 84,60,100
MM 84,56,88
RT 56,227,0
RT 58,227,4096
MM 0,0,112
RT 60,227,8192
RT 62,227,12288
MM 0,4,124
MM 4,4,128
buffer_load_dwordx4 a[88:91], v237, s[28:31], s94 offen offset:0 sc1
MM 4,0,116
MM 8,0,120
s_waitcnt vmcnt(7)
s_barrier
MM 8,4,132
RT 0,220,16384
RT 2,220,20480
)ASM"
R"ASM(MM 20,12,132
RT 4,220,24576
RT 6,220,28672
MM 20,8,120
MM 16,8,116
MM 16,12,128
buffer_load_dwordx4 a[92:95], v237, s[28:31], s94 offen offset:1024 sc1
MM 12,12,124
s_waitcnt lgkmcnt(15)
MM 12,8,112
RT 8,221,16384
RT 10,221,20480
MM 24,16,112
RT 12,221,24576
RT 14,221,28672
MM 24,20,124
MM 28,20,128
MM 28,16,116
MM 32,16,120
buffer_load_dwordx4 a[96:99], v237, s[28:31], s94 offen offset:2048 sc1
MM 32,20,132
RT 16,222,16384
RT 18,222,20480
MM 44,28,132
s_bfe_u32 s93, s78, 0x40010
s_lshl1_add_u32 s93, s93, s99
s_xor_b32 s93, s93, 1
MM 44,24,120
s_mul_i32 s94, s93, 6144
s_add_u32 s94, s94, s101
RT 20,222,24576
MM 40,24,116
RT 22,222,28672
MM 40,28,128
s_waitcnt lgkmcnt(15)
MM 36,28,124
MM 36,24,112
RT 24,223,16384
RT 26,223,20480
MM 48,32,112
RT 28,223,24576
RT 30,223,28672
MM 48,36,124
MM 52,36,128
buffer_load_dwordx4 a[100:103], v237, s[28:31], s94 offen offset:0 sc1
MM 52,32,116
MM 56,32,120
MM 56,36,132
RT 32,224,16384
RT 34,224,20480
MM 68,44,132
RT 36,224,24576
RT 38,224,28672
MM 68,40,120
MM 64,40,116
MM 64,44,128
buffer_load_dwordx4 a[104:107], v237, s[28:31], s94 offen offset:1024 sc1
MM 60,44,124
s_waitcnt lgkmcnt(15)
MM 60,40,112
RT 40,225,16384
RT 42,225,20480
MM 72,48,112
RT 44,225,24576
RT 46,225,28672
MM 72,52,124
MM 76,52,128
MM 76,48,116
MM 80,48,120
buffer_load_dwordx4 a[108:111], v237, s[28:31], s94 offen offset:2048 sc1
MM 80,52,132
RT 48,226,16384
RT 50,226,20480
MM 92,60,132
s_bfe_u32 s93, s78, 0x40014
s_lshl1_add_u32 s93, s93, s99
s_mul_i32 s94, s93, 6144
MM 92,56,120
s_add_u32 s94, s94, s101
RT 52,226,24576
RT 54,226,28672
MM 88,56,116
MM 88,60,128
s_waitcnt vmcnt(14)
s_waitcnt lgkmcnt(15)
MM 84,60,124
MM 84,56,112
RT 56,227,16384
RT 58,227,20480
MM 0,0,136
RT 60,227,24576
RT 62,227,28672
MM 0,4,148
MM 4,4,152
buffer_load_dwordx4 a[112:115], v237, s[28:31], s94 offen offset:0 sc1
MM 4,0,140
MM 8,0,144
s_waitcnt vmcnt(63)
s_barrier
MM 8,4,156
RT 0,228,16384
RT 2,228,20480
MM 20,12,156
RT 4,228,57344
RT 6,228,61440
MM 20,8,144
MM 16,8,140
MM 16,12,152
buffer_load_dwordx4 a[116:119], v237, s[28:31], s94 offen offset:1024 sc1
MM 12,12,148
s_waitcnt lgkmcnt(15)
MM 12,8,136
RT 8,229,16384
RT 10,229,20480
MM 24,16,136
RT 12,229,57344
RT 14,229,61440
MM 24,20,148
MM 28,20,152
MM 28,16,140
MM 32,16,144
buffer_load_dwordx4 a[120:123], v237, s[28:31], s94 offen offset:2048 sc1
MM 32,20,156
RT 16,230,16384
RT 18,230,20480
MM 44,28,156
s_mov_b32 s80, 0
RT 20,230,57344
RT 22,230,61440
MM 44,24,144
CV 240,0,1
CV 241,2,3
MM 40,24,140
s_bfe_u32 s93, s78, 0x40014
CV 242,4,5
CV 243,6,7
MM 40,28,152
s_waitcnt lgkmcnt(15)
s_lshl1_add_u32 s93, s93, s99
s_xor_b32 s93, s93, 1
MM 36,28,148
s_mul_i32 s94, s93, 6144
s_add_u32 s94, s94, s101
MM 36,24,136
CV 244,8,9
CV 245,10,11
MM 48,32,136
CV 246,12,13
CV 247,14,15
MM 48,36,148
RT 24,231,16384
RT 26,231,20480
MM 52,36,152
buffer_store_dwordx4 v[240:243], v237, s[44:47], s80 offen offset:0 sc1
MM 52,32,140
RT 28,231,57344
RT 30,231,61440
MM 56,32,144
buffer_load_dwordx4 a[124:127], v237, s[28:31], s94 offen offset:0 sc1
MM 56,36,156
CV 248,16,17
CV 249,18,19
MM 68,44,156
buffer_store_dwordx4 v[244:247], v237, s[44:47], s80 offen offset:1024 sc1
MM 68,40,144
CV 250,20,21
CV 251,22,23
MM 64,40,140
CV 252,24,25
CV 253,26,27
MM 64,44,152
buffer_store_dwordx4 v[248:251], v237, s[44:47], s80 offen offset:2048 sc1
MM 60,44,148
s_waitcnt lgkmcnt(15)
CV 254,28,29
CV 255,30,31
MM 60,40,136
buffer_load_dwordx4 a[128:131], v237, s[28:31], s94 offen offset:1024 sc1
MM 72,48,136
RT 32,232,16384
RT 34,232,20480
MM 72,52,148
RT 36,232,57344
RT 38,232,61440
MM 76,52,152
buffer_store_dwordx4 v[252:255], v237, s[44:47], s80 offen offset:3072 sc1
MM 76,48,140
s_mov_b32 s80, 4096
CV 240,32,33
CV 241,34,35
CV 242,36,37
CV 243,38,39
MM 80,48,144
buffer_store_dwordx4 v[240:243], v237, s[44:47], s80 offen offset:0 sc1
MM 80,52,156
RT 40,233,16384
RT 42,233,20480
MM 92,60,156
RT 44,233,57344
RT 46,233,61440
MM 92,56,144
buffer_load_dwordx4 a[132:135], v237, s[28:31], s94 offen offset:2048 sc1
MM 88,56,140
s_bfe_u32 s93, s78, 0x40018
CV 244,40,41
CV 245,42,43
CV 246,44,45
CV 247,46,47
MM 88,60,152
buffer_store_dwordx4 v[244:247], v237, s[44:47], s80 offen offset:1024 sc1
MM 84,60,148
s_waitcnt vmcnt(18)
s_waitcnt lgkmcnt(15)
s_lshl1_add_u32 s93, s93, s99
MM 84,56,136
s_mul_i32 s94, s93, 6144
s_add_u32 s94, s94, s101
RT 48,234,16384
s_cmp_eq_u32 s56, 0
s_cbranch_scc1 L_v0
v_mfma_f32_16x16x32_bf16 v[96:99], a[0:3], a[64:67], 0
RT 50,234,20480
RT 52,234,57344
MM 96,4,76
RT 54,234,61440
CV 248,48,49
v_mfma_f32_16x16x32_bf16 v[100:103], a[4:7], a[80:83], 0
buffer_load_dwordx4 a[136:139], v237, s[28:31], s94 offen offset:0 sc1
CV 249,50,51
CV 250,52,53
CV 251,54,55
MM 100,0,68
buffer_store_dwordx4 v[248:251], v237, s[44:47], s80 offen offset:2048 sc1
v_mfma_f32_16x16x32_bf16 v[104:107], a[0:3], a[72:75], 0
s_waitcnt vmcnt(63)
s_barrier
CV 252,56,57
MM 104,4,84
buffer_load_dwordx4 a[140:143], v237, s[28:31], s94 offen offset:1024 sc1
v_mfma_f32_16x16x32_bf16 v[116:119], a[12:15], a[84:87], 0
CV 253,58,59
CV 254,60,61
CV 255,62,63
MM 116,8,72
buffer_store_dwordx4 v[252:255], v237, s[44:47], s80 offen offset:3072 sc1
RT 56,235,16384
RT 58,235,20480
RT 60,235,57344
RT 62,235,61440
v_mfma_f32_16x16x32_bf16 v[112:115], a[8:11], a[68:71], 0
buffer_load_dwordx4 a[144:147], v237, s[28:31], s94 offen offset:2048 sc1
MM 112,12,80
s_mov_b32 s80, 8192
CV 240,64,65
CV 241,66,67
CV 242,68,69
CV 243,70,71
v_mfma_f32_16x16x32_bf16 v[108:111], a[12:15], a[76:79], 0
buffer_store_dwordx4 v[240:243], v237, s[44:47], s80 offen offset:0 sc1
MM 108,8,64
s_add_u32 m0, s97, 16384
s_lshl_b32 s81, s93, 16
s_waitcnt lgkmcnt(15)
v_mfma_f32_16x16x32_bf16 v[120:123], a[16:19], a[64:67], 0
DM 216,32,81,0
CV 244,72,73
CV 245,74,75
CV 246,76,77
CV 247,78,79
MM 120,20,76
buffer_store_dwordx4 v[244:247], v237, s[44:47], s80 offen offset:1024 sc1
RT 0,228,24576
RT 2,228,28672
RT 4,228,32768
RT 6,228,36864
v_mfma_f32_16x16x32_bf16 v[124:127], a[20:23], a[80:83], 0
CV 248,80,81
CV 249,82,83
MM 124,16,68
DM 217,32,81,1024
v_mfma_f32_16x16x32_bf16 v[128:131], a[16:19], a[72:75], 0
CV 250,84,85
CV 251,86,87
MM 128,20,84
buffer_store_dwordx4 v[248:251], v237, s[44:47], s80 offen offset:2048 sc1
v_mfma_f32_16x16x32_bf16 v[140:143], a[28:31], a[84:87], 0
DM 218,32,81,2048
MM 140,24,72
CV 252,88,89
CV 253,90,91
CV 254,92,93
CV 255,94,95
RT 8,229,24576
RT 10,229,28672
RT 12,229,32768
RT 14,229,36864
v_mfma_f32_16x16x32_bf16 v[136:139], a[24:27], a[68:71], 0
buffer_store_dwordx4 v[252:255], v237, s[44:47], s80 offen offset:3072 sc1
MM 136,28,80
s_waitcnt lgkmcnt(15)
RT 16,230,24576
RT 18,230,28672
v_mfma_f32_16x16x32_bf16 v[132:135], a[28:31], a[76:79], 0
DM 219,32,81,3072
MM 132,24,64
s_bfe_u32 s93, s78, 0x40018
s_lshl1_add_u32 s93, s93, s99
s_xor_b32 s93, s93, 1
v_mfma_f32_16x16x32_bf16 v[144:147], a[32:35], a[64:67], 0
s_mul_i32 s94, s93, 6144
s_add_u32 s94, s94, s101
RT 20,230,32768
MM 144,36,76
RT 22,230,36864
RT 24,231,24576
v_mfma_f32_16x16x32_bf16 v[148:151], a[36:39], a[80:83], 0
buffer_load_dwordx4 a[148:151], v237, s[28:31], s94 offen offset:0 sc1
MM 148,32,68
RT 26,231,28672
RT 28,231,32768
v_mfma_f32_16x16x32_bf16 v[152:155], a[32:35], a[72:75], 0
RT 30,231,36864
MM 152,36,84
buffer_load_dwordx4 a[152:155], v237, s[28:31], s94 offen offset:1024 sc1
v_mfma_f32_16x16x32_bf16 v[164:167], a[44:47], a[84:87], 0
RT 32,232,24576
RT 34,232,28672
MM 164,40,72
RT 36,232,32768
RT 38,232,36864
v_mfma_f32_16x16x32_bf16 v[160:163], a[40:43], a[68:71], 0
buffer_load_dwordx4 a[156:159], v237, s[28:31], s94 offen offset:2048 sc1
MM 160,44,80
s_add_u32 m0, s97, 24576
s_lshl_b32 s81, s93, 16
s_waitcnt lgkmcnt(15)
v_mfma_f32_16x16x32_bf16 v[156:159], a[44:47], a[76:79], 0
MM 156,40,64
DM 216,32,81,0
v_mfma_f32_16x16x32_bf16 v[168:171], a[48:51], a[64:67], 0
RT 40,233,24576
RT 42,233,28672
MM 168,52,76
RT 44,233,32768
RT 46,233,36864
v_mfma_f32_16x16x32_bf16 v[172:175], a[52:55], a[80:83], 0
DM 217,32,81,1024
MM 172,48,68
v_mfma_f32_16x16x32_bf16 v[176:179], a[48:51], a[72:75], 0
MM 176,52,84
DM 218,32,81,2048
v_mfma_f32_16x16x32_bf16 v[188:191], a[60:63], a[84:87], 0
RT 48,234,24576
RT 50,234,28672
MM 188,56,72
RT 52,234,32768
RT 54,234,36864
v_mfma_f32_16x16x32_bf16 v[184:187], a[56:59], a[68:71], 0
DM 219,32,81,3072
MM 184,60,80
s_waitcnt vmcnt(32)
s_waitcnt lgkmcnt(15)
v_mfma_f32_16x16x32_bf16 v[180:183], a[60:63], a[76:79], 0
MM 180,56,64
RT 56,235,24576
RT 58,235,28672
s_branch L_v1
L_v0:
v_mfma_f32_16x16x32_bf16 v[96:99], a[0:3], a[64:67], v[0:3]
RT 50,234,20480
RT 52,234,57344
MM 96,4,76
RT 54,234,61440
CV 248,48,49
v_mfma_f32_16x16x32_bf16 v[100:103], a[4:7], a[80:83], v[4:7]
buffer_load_dwordx4 a[136:139], v237, s[28:31], s94 offen offset:0 sc1
CV 249,50,51
CV 250,52,53
CV 251,54,55
MM 100,0,68
buffer_store_dwordx4 v[248:251], v237, s[44:47], s80 offen offset:2048 sc1
v_mfma_f32_16x16x32_bf16 v[104:107], a[0:3], a[72:75], v[8:11]
s_waitcnt vmcnt(63)
s_barrier
CV 252,56,57
MM 104,4,84
buffer_load_dwordx4 a[140:143], v237, s[28:31], s94 offen offset:1024 sc1
v_mfma_f32_16x16x32_bf16 v[116:119], a[12:15], a[84:87], v[20:23]
CV 253,58,59
CV 254,60,61
CV 255,62,63
MM 116,8,72
buffer_store_dwordx4 v[252:255], v237, s[44:47], s80 offen offset:3072 sc1
RT 56,235,16384
RT 58,235,20480
RT 60,235,57344
RT 62,235,61440
v_mfma_f32_16x16x32_bf16 v[112:115], a[8:11], a[68:71], v[16:19]
buffer_load_dwordx4 a[144:147], v237, s[28:31], s94 offen offset:2048 sc1
MM 112,12,80
s_mov_b32 s80, 8192
CV 240,64,65
CV 241,66,67
CV 242,68,69
CV 243,70,71
v_mfma_f32_16x16x32_bf16 v[108:111], a[12:15], a[76:79], v[12:15]
buffer_store_dwordx4 v[240:243], v237, s[44:47], s80 offen offset:0 sc1
MM 108,8,64
s_add_u32 m0, s97, 16384
s_lshl_b32 s81, s93, 16
s_waitcnt lgkmcnt(15)
v_mfma_f32_16x16x32_bf16 v[120:123], a[16:19], a[64:67], v[24:27]
DM 216,32,81,0
CV 244,72,73
CV 245,74,75
CV 246,76,77
CV 247,78,79
MM 120,20,76
buffer_store_dwordx4 v[244:247], v237, s[44:47], s80 offen offset:1024 sc1
RT 0,228,24576
RT 2,228,28672
RT 4,228,32768
RT 6,228,36864
v_mfma_f32_16x16x32_bf16 v[124:127], a[20:23], a[80:83], v[28:31]
CV 248,80,81
CV 249,82,83
MM 124,16,68
DM 217,32,81,1024
v_mfma_f32_16x16x32_bf16 v[128:131], a[16:19], a[72:75], v[32:35]
CV 250,84,85
CV 251,86,87
MM 128,20,84
buffer_store_dwordx4 v[248:251], v237, s[44:47], s80 offen offset:2048 sc1
v_mfma_f32_16x16x32_bf16 v[140:143], a[28:31], a[84:87], v[44:47]
DM 218,32,81,2048
MM 140,24,72
CV 252,88,89
CV 253,90,91
CV 254,92,93
CV 255,94,95
RT 8,229,24576
RT 10,229,28672
RT 12,229,32768
RT 14,229,36864
v_mfma_f32_16x16x32_bf16 v[136:139], a[24:27], a[68:71], v[40:43]
buffer_store_dwordx4 v[252:255], v237, s[44:47], s80 offen offset:3072 sc1
MM 136,28,80
s_waitcnt lgkmcnt(15)
RT 16,230,24576
RT 18,230,28672
v_mfma_f32_16x16x32_bf16 v[132:135], a[28:31], a[76:79], v[36:39]
DM 219,32,81,3072
MM 132,24,64
s_bfe_u32 s93, s78, 0x40018
s_lshl1_add_u32 s93, s93, s99
s_xor_b32 s93, s93, 1
v_mfma_f32_16x16x32_bf16 v[144:147], a[32:35], a[64:67], v[48:51]
s_mul_i32 s94, s93, 6144
s_add_u32 s94, s94, s101
RT 20,230,32768
MM 144,36,76
RT 22,230,36864
RT 24,231,24576
v_mfma_f32_16x16x32_bf16 v[148:151], a[36:39], a[80:83], v[52:55]
buffer_load_dwordx4 a[148:151], v237, s[28:31], s94 offen offset:0 sc1
MM 148,32,68
RT 26,231,28672
RT 28,231,32768
v_mfma_f32_16x16x32_bf16 v[152:155], a[32:35], a[72:75], v[56:59]
RT 30,231,36864
MM 152,36,84
buffer_load_dwordx4 a[152:155], v237, s[28:31], s94 offen offset:1024 sc1
v_mfma_f32_16x16x32_bf16 v[164:167], a[44:47], a[84:87], v[68:71]
RT 32,232,24576
RT 34,232,28672
MM 164,40,72
RT 36,232,32768
RT 38,232,36864
v_mfma_f32_16x16x32_bf16 v[160:163], a[40:43], a[68:71], v[64:67]
buffer_load_dwordx4 a[156:159], v237, s[28:31], s94 offen offset:2048 sc1
MM 160,44,80
s_add_u32 m0, s97, 24576
s_lshl_b32 s81, s93, 16
s_waitcnt lgkmcnt(15)
v_mfma_f32_16x16x32_bf16 v[156:159], a[44:47], a[76:79], v[60:63]
MM 156,40,64
DM 216,32,81,0
v_mfma_f32_16x16x32_bf16 v[168:171], a[48:51], a[64:67], v[72:75]
RT 40,233,24576
RT 42,233,28672
MM 168,52,76
RT 44,233,32768
RT 46,233,36864
v_mfma_f32_16x16x32_bf16 v[172:175], a[52:55], a[80:83], v[76:79]
DM 217,32,81,1024
MM 172,48,68
v_mfma_f32_16x16x32_bf16 v[176:179], a[48:51], a[72:75], v[80:83]
MM 176,52,84
DM 218,32,81,2048
v_mfma_f32_16x16x32_bf16 v[188:191], a[60:63], a[84:87], v[92:95]
RT 48,234,24576
RT 50,234,28672
MM 188,56,72
RT 52,234,32768
RT 54,234,36864
v_mfma_f32_16x16x32_bf16 v[184:187], a[56:59], a[68:71], v[88:91]
DM 219,32,81,3072
MM 184,60,80
s_waitcnt vmcnt(32)
s_waitcnt lgkmcnt(15)
v_mfma_f32_16x16x32_bf16 v[180:183], a[60:63], a[76:79], v[84:87]
MM 180,56,64
RT 56,235,24576
RT 58,235,28672
L_v1:
MM 96,0,88
RT 60,235,32768
RT 62,235,36864
MM 96,4,100
MM 100,4,104
MM 100,0,92
MM 104,0,96
s_waitcnt vmcnt(63)
s_barrier
MM 104,4,108
RT 0,228,40960
RT 2,228,45056
MM 116,12,108
RT 4,228,49152
RT 6,228,53248
MM 116,8,96
MM 112,8,92
MM 112,12,104
s_waitcnt lgkmcnt(15)
MM 108,12,100
MM 108,8,88
RT 8,229,40960
RT 10,229,45056
MM 120,16,88
s_waitcnt vmcnt(8)
s_mov_b64 exec, 1
buffer_atomic_add v195, off, s[40:43], s58
s_mov_b64 exec, -1
MM 120,20,100
RT 12,229,49152
RT 14,229,53248
MM 124,20,104
MM 124,16,92
MM 128,16,96
MM 128,20,108
RT 16,230,40960
RT 18,230,45056
MM 140,28,108
RT 20,230,49152
RT 22,230,53248
MM 140,24,96
MM 136,24,92
MM 136,28,104
s_waitcnt lgkmcnt(15)
MM 132,28,100
MM 132,24,88
RT 24,231,40960
RT 26,231,45056
MM 144,32,88
RT 28,231,49152
RT 30,231,53248
MM 144,36,100
MM 148,36,104
MM 148,32,92
MM 152,32,96
MM 152,36,108
RT 32,232,40960
RT 34,232,45056
MM 164,44,108
RT 36,232,49152
RT 38,232,53248
MM 164,40,96
MM 160,40,92
MM 160,44,104
s_waitcnt lgkmcnt(15)
MM 156,44,100
MM 156,40,88
RT 40,233,40960
RT 42,233,45056
MM 168,48,88
RT 44,233,49152
RT 46,233,53248
MM 168,52,100
MM 172,52,104
MM 172,48,92
MM 176,48,96
MM 176,52,108
RT 48,234,40960
RT 50,234,45056
MM 188,60,108
RT 52,234,49152
RT 54,234,53248
MM 188,56,96
MM 184,56,92
MM 184,60,104
s_waitcnt vmcnt(22)
s_waitcnt lgkmcnt(15)
MM 180,60,100
MM 180,56,88
RT 56,235,40960
RT 58,235,45056
MM 96,0,112
RT 60,235,49152
RT 62,235,53248
MM 96,4,124
MM 100,4,128
buffer_load_dword v215, off, s[40:43], s57 sc1
MM 100,0,116
MM 104,0,120
s_waitcnt vmcnt(2)
s_barrier
MM 104,4,132
RT 0,228,0
RT 2,228,4096
MM 116,12,132
RT 4,228,8192
RT 6,228,12288
MM 116,8,120
MM 112,8,116
MM 112,12,128
s_waitcnt lgkmcnt(15)
MM 108,12,124
MM 108,8,112
RT 8,229,0
RT 10,229,4096
MM 120,16,112
RT 12,229,8192
RT 14,229,12288
MM 120,20,124
MM 124,20,128
MM 124,16,116
MM 128,16,120
MM 128,20,132
RT 16,230,0
RT 18,230,4096
MM 140,28,132
RT 20,230,8192
RT 22,230,12288
MM 140,24,120
MM 136,24,116
MM 136,28,128
s_waitcnt lgkmcnt(15)
MM 132,28,124
MM 132,24,112
RT 24,231,0
RT 26,231,4096
MM 144,32,112
RT 28,231,8192
RT 30,231,12288
MM 144,36,124
MM 148,36,128
MM 148,32,116
MM 152,32,120
MM 152,36,132
RT 32,232,0
RT 34,232,4096
MM 164,44,132
RT 36,232,8192
RT 38,232,12288
MM 164,40,120
MM 160,40,116
MM 160,44,128
s_waitcnt lgkmcnt(15)
MM 156,44,124
MM 156,40,112
RT 40,233,0
RT 42,233,4096
MM 168,48,112
RT 44,233,8192
RT 46,233,12288
MM 168,52,124
MM 172,52,128
MM 172,48,116
MM 176,48,120
MM 176,52,132
RT 48,234,0
RT 50,234,4096
MM 188,60,132
s_mov_b32 s80, 0
RT 52,234,8192
RT 54,234,12288
MM 188,56,120
MM 184,56,116
MM 184,60,128
s_waitcnt vmcnt(6)
s_waitcnt lgkmcnt(15)
MM 180,60,124
MM 180,56,112
RT 56,235,0
RT 58,235,4096
MM 96,0,136
RT 60,235,8192
RT 62,235,12288
MM 96,4,148
s_waitcnt vmcnt(0)
L_pc: ; ---- partner partial sum ready?
v_cmp_eq_u32 vcc, 4, v215
s_cbranch_vccnz L_pc_ok
s_add_u32 s92, s92, 1
s_cmp_gt_u32 s92, 0x40000
s_cbranch_scc1 L_pc_ok
buffer_load_dword v215, off, s[40:43], s57 sc1
s_waitcnt vmcnt(0)
s_branch L_pc
L_pc_ok:
s_barrier
MM 100,4,152
buffer_load_dwordx4 v[0:3], v237, s[48:51], s80 offen offset:0 sc1
MM 100,0,140
MM 104,0,144
MM 104,4,156
buffer_load_dwordx4 v[4:7], v237, s[48:51], s80 offen offset:1024 sc1
MM 116,12,156
MM 116,8,144
MM 112,8,140
MM 112,12,152
buffer_load_dwordx4 v[8:11], v237, s[48:51], s80 offen offset:2048 sc1
MM 108,12,148
s_waitcnt lgkmcnt(15)
MM 108,8,136
MM 120,16,136
MM 120,20,148
buffer_load_dwordx4 v[12:15], v237, s[48:51], s80 offen offset:3072 sc1
MM 124,20,152
s_mov_b32 s80, 4096
ds_write_b128 v208, v[96:99] offset:0
MM 124,16,140
MM 128,16,144
buffer_load_dwordx4 v[16:19], v237, s[48:51], s80 offen offset:0 sc1
MM 128,20,156
MM 140,28,156
ds_write_b128 v208, v[100:103] offset:1024
MM 140,24,144
MM 136,24,140
buffer_load_dwordx4 v[20:23], v237, s[48:51], s80 offen offset:1024 sc1
MM 136,28,152
s_waitcnt lgkmcnt(10)
MM 132,28,148
ds_write_b128 v208, v[108:111] offset:3072
MM 132,24,136
MM 144,32,136
MM 144,36,148
MM 148,36,152
ds_write_b128 v208, v[112:115] offset:4096
MM 148,32,140
MM 152,32,144
MM 152,36,156
MM 164,44,156
MM 164,40,144
ds_write_b128 v208, v[120:123] offset:6144
MM 160,40,140
MM 160,44,152
s_waitcnt lgkmcnt(5)
MM 156,44,148
MM 156,40,136
ds_write_b128 v208, v[124:127] offset:7168
MM 168,48,136
MM 168,52,148
MM 172,52,152
MM 172,48,140
ds_write_b128 v208, v[132:135] offset:9216
MM 176,48,144
MM 176,52,156
MM 188,60,156
MM 188,56,144
ds_write_b128 v208, v[136:139] offset:10240
MM 184,56,140
MM 184,60,152
MM 180,60,148
MM 180,56,136
s_waitcnt lgkmcnt(0)
s_barrier
ds_read_b128 v[48:51], v209 offset:0
ds_read_b128 v[52:55], v209 offset:3072
ds_read_b128 v[56:59], v209 offset:6144
ds_read_b128 v[60:63], v209 offset:9216
s_waitcnt vmcnt(0)
ds_write_b128 v208, v[104:107] offset:2048
v_lshlrev_b32 v64, 16, v0
v_and_b32 v65, 0xffff0000, v0
v_pk_add_f32 v[144:145], v[144:145], v[64:65]
v_lshlrev_b32 v66, 16, v1
v_and_b32 v67, 0xffff0000, v1
v_pk_add_f32 v[146:147], v[146:147], v[66:67]
ds_write_b128 v208, v[116:119] offset:5120
v_lshlrev_b32 v64, 16, v6
v_and_b32 v65, 0xffff0000, v6
v_pk_add_f32 v[156:157], v[156:157], v[64:65]
v_lshlrev_b32 v66, 16, v7
v_and_b32 v67, 0xffff0000, v7
v_pk_add_f32 v[158:159], v[158:159], v[66:67]
s_waitcnt lgkmcnt(4)
v_pk_add_f32 v[144:145], v[144:145], v[48:49]
v_pk_add_f32 v[146:147], v[146:147], v[50:51]
CV 24,144,145
CV 25,146,147
v_pk_add_f32 v[156:157], v[156:157], v[52:53]
v_pk_add_f32 v[158:159], v[158:159], v[54:55]
CV 26,156,157
CV 27,158,159
s_mov_b32 s80, 0
s_nop 0
v_permlane16_swap_b32 v24, v26
v_permlane16_swap_b32 v25, v27
buffer_store_dwordx4 v[24:27], v212, s[36:39], s80 offen offset:0
ds_write_b128 v208, v[128:131] offset:8192
v_lshlrev_b32 v64, 16, v12
v_and_b32 v65, 0xffff0000, v12
v_pk_add_f32 v[168:169], v[168:169], v[64:65]
v_lshlrev_b32 v66, 16, v13
v_and_b32 v67, 0xffff0000, v13
v_pk_add_f32 v[170:171], v[170:171], v[66:67]
ds_write_b128 v208, v[140:143] offset:11264
v_lshlrev_b32 v64, 16, v18
v_and_b32 v65, 0xffff0000, v18
v_pk_add_f32 v[180:181], v[180:181], v[64:65]
v_lshlrev_b32 v66, 16, v19
v_and_b32 v67, 0xffff0000, v19
v_pk_add_f32 v[182:183], v[182:183], v[66:67]
s_waitcnt lgkmcnt(4)
v_pk_add_f32 v[168:169], v[168:169], v[56:57]
v_pk_add_f32 v[170:171], v[170:171], v[58:59]
CV 28,168,169
CV 29,170,171
v_pk_add_f32 v[180:181], v[180:181], v[60:61]
v_pk_add_f32 v[182:183], v[182:183], v[62:63]
CV 30,180,181
CV 31,182,183
s_mov_b32 s80, 0
s_nop 0
v_permlane16_swap_b32 v28, v30
v_permlane16_swap_b32 v29, v31
buffer_store_dwordx4 v[28:31], v212, s[36:39], s80 offen offset:64
ds_read_b128 v[48:51], v209 offset:1024
ds_read_b128 v[52:55], v209 offset:4096
ds_read_b128 v[56:59], v209 offset:7168
ds_read_b128 v[60:63], v209 offset:10240
v_lshlrev_b32 v64, 16, v2
v_and_b32 v65, 0xffff0000, v2
v_pk_add_f32 v[148:149], v[148:149], v[64:65]
v_lshlrev_b32 v66, 16, v3
v_and_b32 v67, 0xffff0000, v3
v_pk_add_f32 v[150:151], v[150:151], v[66:67]
v_lshlrev_b32 v64, 16, v8
v_and_b32 v65, 0xffff0000, v8
v_pk_add_f32 v[160:161], v[160:161], v[64:65]
v_lshlrev_b32 v66, 16, v9
v_and_b32 v67, 0xffff0000, v9
v_pk_add_f32 v[162:163], v[162:163], v[66:67]
s_waitcnt lgkmcnt(2)
v_pk_add_f32 v[148:149], v[148:149], v[48:49]
v_pk_add_f32 v[150:151], v[150:151], v[50:51]
CV 32,148,149
CV 33,150,151
v_pk_add_f32 v[160:161], v[160:161], v[52:53]
v_pk_add_f32 v[162:163], v[162:163], v[54:55]
CV 34,160,161
CV 35,162,163
s_mov_b32 s80, 65536
s_nop 0
v_permlane16_swap_b32 v32, v34
v_permlane16_swap_b32 v33, v35
buffer_store_dwordx4 v[32:35], v212, s[36:39], s80 offen offset:0
v_lshlrev_b32 v64, 16, v14
v_and_b32 v65, 0xffff0000, v14
v_pk_add_f32 v[172:173], v[172:173], v[64:65]
v_lshlrev_b32 v66, 16, v15
v_and_b32 v67, 0xffff0000, v15
v_pk_add_f32 v[174:175], v[174:175], v[66:67]
v_lshlrev_b32 v64, 16, v20
v_and_b32 v65, 0xffff0000, v20
v_pk_add_f32 v[184:185], v[184:185], v[64:65]
v_lshlrev_b32 v66, 16, v21
v_and_b32 v67, 0xffff0000, v21
v_pk_add_f32 v[186:187], v[186:187], v[66:67]
s_waitcnt lgkmcnt(0)
v_pk_add_f32 v[172:173], v[172:173], v[56:57]
v_pk_add_f32 v[174:175], v[174:175], v[58:59]
CV 36,172,173
CV 37,174,175
v_pk_add_f32 v[184:185], v[184:185], v[60:61]
v_pk_add_f32 v[186:187], v[186:187], v[62:63]
CV 38,184,185
CV 39,186,187
s_mov_b32 s80, 65536
s_nop 0
v_permlane16_swap_b32 v36, v38
v_permlane16_swap_b32 v37, v39
buffer_store_dwordx4 v[36:39], v212, s[36:39], s80 offen offset:64
s_waitcnt lgkmcnt(0)
s_barrier
ds_read_b128 v[48:51], v209 offset:2048
ds_read_b128 v[52:55], v209 offset:5120
ds_read_b128 v[56:59], v209 offset:8192
ds_read_b128 v[60:63], v209 offset:11264
v_lshlrev_b32 v64, 16, v4
v_and_b32 v65, 0xffff0000, v4
v_pk_add_f32 v[152:153], v[152:153], v[64:65]
v_lshlrev_b32 v66, 16, v5
v_and_b32 v67, 0xffff0000, v5
v_pk_add_f32 v[154:155], v[154:155], v[66:67]
v_lshlrev_b32 v64, 16, v10
v_and_b32 v65, 0xffff0000, v10
v_pk_add_f32 v[164:165], v[164:165], v[64:65]
v_lshlrev_b32 v66, 16, v11
v_and_b32 v67, 0xffff0000, v11
v_pk_add_f32 v[166:167], v[166:167], v[66:67]
s_waitcnt lgkmcnt(2)
v_pk_add_f32 v[152:153], v[152:153], v[48:49]
v_pk_add_f32 v[154:155], v[154:155], v[50:51]
CV 40,152,153
CV 41,154,155
v_pk_add_f32 v[164:165], v[164:165], v[52:53]
v_pk_add_f32 v[166:167], v[166:167], v[54:55]
CV 42,164,165
CV 43,166,167
s_mov_b32 s80, 131072
s_nop 0
v_permlane16_swap_b32 v40, v42
v_permlane16_swap_b32 v41, v43
buffer_store_dwordx4 v[40:43], v212, s[36:39], s80 offen offset:0
v_lshlrev_b32 v64, 16, v16
v_and_b32 v65, 0xffff0000, v16
v_pk_add_f32 v[176:177], v[176:177], v[64:65]
v_lshlrev_b32 v66, 16, v17
v_and_b32 v67, 0xffff0000, v17
v_pk_add_f32 v[178:179], v[178:179], v[66:67]
v_lshlrev_b32 v64, 16, v22
v_and_b32 v65, 0xffff0000, v22
v_pk_add_f32 v[188:189], v[188:189], v[64:65]
v_lshlrev_b32 v66, 16, v23
v_and_b32 v67, 0xffff0000, v23
v_pk_add_f32 v[190:191], v[190:191], v[66:67]
s_waitcnt lgkmcnt(0)
v_pk_add_f32 v[176:177], v[176:177], v[56:57]
v_pk_add_f32 v[178:179], v[178:179], v[58:59]
CV 44,176,177
CV 45,178,179
v_pk_add_f32 v[188:189], v[188:189], v[60:61]
v_pk_add_f32 v[190:191], v[190:191], v[62:63]
CV 46,188,189
CV 47,190,191
s_mov_b32 s80, 131072
s_nop 0
v_permlane16_swap_b32 v44, v46
v_permlane16_swap_b32 v45, v47
buffer_store_dwordx4 v[44:47], v212, s[36:39], s80 offen offset:64
L_o64: ; ---- end; out-of-line paths follow (softmax rescale, slow hand-shake)
s_endpgm
L_tr1a:
s_call_b64 s[60:61], L_rare01
s_branch L_back1a
L_rare01:
v_max3_f32 v246, v160, v161, v162
v_max3_f32 v246, v246, v163, v164
v_max3_f32 v246, v246, v165, v166
v_max_f32 v246, v246, v167
v_mov_b32 v247, v246
s_nop 1
v_permlane16_swap_b32 v246, v247
v_max_f32 v246, v246, v247
v_mov_b32 v247, v246
s_nop 1
v_permlane32_swap_b32 v246, v247
v_max_f32 v246, v246, v247
v_max_f32 v246, 0, v246
v_exp_f32 v247, -v246
v_sub_f32 v160, v160, v246
v_sub_f32 v161, v161, v246
v_sub_f32 v162, v162, v246
v_sub_f32 v163, v163, v246
v_sub_f32 v164, v164, v246
v_sub_f32 v165, v165, v246
v_sub_f32 v166, v166, v246
v_sub_f32 v167, v167, v246
v_sub_f32 v96, v96, v246
v_sub_f32 v97, v97, v246
v_sub_f32 v98, v98, v246
v_sub_f32 v99, v99, v246
v_mul_f32 v240, v240, v247
v_accvgpr_read_b32 v193, a0
v_mul_f32 v193, v193, v247
v_accvgpr_write_b32 a0, v193
v_accvgpr_read_b32 v194, a1
v_mul_f32 v194, v194, v247
v_accvgpr_write_b32 a1, v194
v_accvgpr_read_b32 v193, a2
v_mul_f32 v193, v193, v247
v_accvgpr_write_b32 a2, v193
v_accvgpr_read_b32 v194, a3
v_mul_f32 v194, v194, v247
v_accvgpr_write_b32 a3, v194
v_accvgpr_read_b32 v193, a4
v_mul_f32 v193, v193, v247
v_accvgpr_write_b32 a4, v193
)ASM"
R"ASM(v_accvgpr_read_b32 v194, a5
v_mul_f32 v194, v194, v247
v_accvgpr_write_b32 a5, v194
v_accvgpr_read_b32 v193, a6
v_mul_f32 v193, v193, v247
v_accvgpr_write_b32 a6, v193
v_accvgpr_read_b32 v194, a7
v_mul_f32 v194, v194, v247
v_accvgpr_write_b32 a7, v194
v_accvgpr_read_b32 v193, a8
v_mul_f32 v193, v193, v247
v_accvgpr_write_b32 a8, v193
v_accvgpr_read_b32 v194, a9
v_mul_f32 v194, v194, v247
v_accvgpr_write_b32 a9, v194
v_accvgpr_read_b32 v193, a10
v_mul_f32 v193, v193, v247
v_accvgpr_write_b32 a10, v193
v_accvgpr_read_b32 v194, a11
v_mul_f32 v194, v194, v247
v_accvgpr_write_b32 a11, v194
v_accvgpr_read_b32 v193, a12
v_mul_f32 v193, v193, v247
v_accvgpr_write_b32 a12, v193
v_accvgpr_read_b32 v194, a13
v_mul_f32 v194, v194, v247
v_accvgpr_write_b32 a13, v194
v_accvgpr_read_b32 v193, a14
v_mul_f32 v193, v193, v247
v_accvgpr_write_b32 a14, v193
v_accvgpr_read_b32 v194, a15
v_mul_f32 v194, v194, v247
v_accvgpr_write_b32 a15, v194
v_accvgpr_read_b32 v193, a16
v_mul_f32 v193, v193, v247
v_accvgpr_write_b32 a16, v193
v_accvgpr_read_b32 v194, a17
v_mul_f32 v194, v194, v247
v_accvgpr_write_b32 a17, v194
v_accvgpr_read_b32 v193, a18
v_mul_f32 v193, v193, v247
v_accvgpr_write_b32 a18, v193
v_accvgpr_read_b32 v194, a19
v_mul_f32 v194, v194, v247
v_accvgpr_write_b32 a19, v194
v_accvgpr_read_b32 v193, a20
v_mul_f32 v193, v193, v247
v_accvgpr_write_b32 a20, v193
v_accvgpr_read_b32 v194, a21
v_mul_f32 v194, v194, v247
v_accvgpr_write_b32 a21, v194
v_accvgpr_read_b32 v193, a22
v_mul_f32 v193, v193, v247
v_accvgpr_write_b32 a22, v193
v_accvgpr_read_b32 v194, a23
v_mul_f32 v194, v194, v247
v_accvgpr_write_b32 a23, v194
v_accvgpr_read_b32 v193, a24
v_mul_f32 v193, v193, v247
v_accvgpr_write_b32 a24, v193
v_accvgpr_read_b32 v194, a25
v_mul_f32 v194, v194, v247
v_accvgpr_write_b32 a25, v194
v_accvgpr_read_b32 v193, a26
v_mul_f32 v193, v193, v247
v_accvgpr_write_b32 a26, v193
v_accvgpr_read_b32 v194, a27
v_mul_f32 v194, v194, v247
v_accvgpr_write_b32 a27, v194
v_accvgpr_read_b32 v193, a28
v_mul_f32 v193, v193, v247
v_accvgpr_write_b32 a28, v193
v_accvgpr_read_b32 v194, a29
v_mul_f32 v194, v194, v247
v_accvgpr_write_b32 a29, v194
v_accvgpr_read_b32 v193, a30
v_mul_f32 v193, v193, v247
v_accvgpr_write_b32 a30, v193
v_accvgpr_read_b32 v194, a31
v_mul_f32 v194, v194, v247
v_accvgpr_write_b32 a31, v194
v_max3_f32 v246, v168, v169, v170
v_max3_f32 v246, v246, v171, v172
v_max3_f32 v246, v246, v173, v174
v_max_f32 v246, v246, v175
v_mov_b32 v247, v246
s_nop 1
v_permlane16_swap_b32 v246, v247
v_max_f32 v246, v246, v247
v_mov_b32 v247, v246
s_nop 1
v_permlane32_swap_b32 v246, v247
v_max_f32 v246, v246, v247
v_max_f32 v246, 0, v246
v_exp_f32 v247, -v246
v_sub_f32 v168, v168, v246
v_sub_f32 v169, v169, v246
v_sub_f32 v170, v170, v246
v_sub_f32 v171, v171, v246
v_sub_f32 v172, v172, v246
v_sub_f32 v173, v173, v246
v_sub_f32 v174, v174, v246
v_sub_f32 v175, v175, v246
v_sub_f32 v100, v100, v246
v_sub_f32 v101, v101, v246
v_sub_f32 v102, v102, v246
v_sub_f32 v103, v103, v246
v_mul_f32 v241, v241, v247
v_accvgpr_read_b32 v193, a32
v_mul_f32 v193, v193, v247
v_accvgpr_write_b32 a32, v193
v_accvgpr_read_b32 v194, a33
v_mul_f32 v194, v194, v247
v_accvgpr_write_b32 a33, v194
v_accvgpr_read_b32 v193, a34
v_mul_f32 v193, v193, v247
v_accvgpr_write_b32 a34, v193
v_accvgpr_read_b32 v194, a35
v_mul_f32 v194, v194, v247
v_accvgpr_write_b32 a35, v194
v_accvgpr_read_b32 v193, a36
v_mul_f32 v193, v193, v247
v_accvgpr_write_b32 a36, v193
v_accvgpr_read_b32 v194, a37
v_mul_f32 v194, v194, v247
v_accvgpr_write_b32 a37, v194
v_accvgpr_read_b32 v193, a38
v_mul_f32 v193, v193, v247
v_accvgpr_write_b32 a38, v193
v_accvgpr_read_b32 v194, a39
v_mul_f32 v194, v194, v247
v_accvgpr_write_b32 a39, v194
v_accvgpr_read_b32 v193, a40
v_mul_f32 v193, v193, v247
v_accvgpr_write_b32 a40, v193
v_accvgpr_read_b32 v194, a41
v_mul_f32 v194, v194, v247
v_accvgpr_write_b32 a41, v194
v_accvgpr_read_b32 v193, a42
v_mul_f32 v193, v193, v247
v_accvgpr_write_b32 a42, v193
v_accvgpr_read_b32 v194, a43
v_mul_f32 v194, v194, v247
v_accvgpr_write_b32 a43, v194
v_accvgpr_read_b32 v193, a44
v_mul_f32 v193, v193, v247
v_accvgpr_write_b32 a44, v193
v_accvgpr_read_b32 v194, a45
v_mul_f32 v194, v194, v247
v_accvgpr_write_b32 a45, v194
v_accvgpr_read_b32 v193, a46
v_mul_f32 v193, v193, v247
v_accvgpr_write_b32 a46, v193
v_accvgpr_read_b32 v194, a47
v_mul_f32 v194, v194, v247
v_accvgpr_write_b32 a47, v194
v_accvgpr_read_b32 v193, a48
v_mul_f32 v193, v193, v247
v_accvgpr_write_b32 a48, v193
v_accvgpr_read_b32 v194, a49
v_mul_f32 v194, v194, v247
v_accvgpr_write_b32 a49, v194
v_accvgpr_read_b32 v193, a50
v_mul_f32 v193, v193, v247
v_accvgpr_write_b32 a50, v193
v_accvgpr_read_b32 v194, a51
v_mul_f32 v194, v194, v247
v_accvgpr_write_b32 a51, v194
v_accvgpr_read_b32 v193, a52
v_mul_f32 v193, v193, v247
v_accvgpr_write_b32 a52, v193
v_accvgpr_read_b32 v194, a53
v_mul_f32 v194, v194, v247
v_accvgpr_write_b32 a53, v194
v_accvgpr_read_b32 v193, a54
v_mul_f32 v193, v193, v247
v_accvgpr_write_b32 a54, v193
v_accvgpr_read_b32 v194, a55
v_mul_f32 v194, v194, v247
v_accvgpr_write_b32 a55, v194
v_accvgpr_read_b32 v193, a56
v_mul_f32 v193, v193, v247
v_accvgpr_write_b32 a56, v193
v_accvgpr_read_b32 v194, a57
v_mul_f32 v194, v194, v247
v_accvgpr_write_b32 a57, v194
v_accvgpr_read_b32 v193, a58
v_mul_f32 v193, v193, v247
v_accvgpr_write_b32 a58, v193
v_accvgpr_read_b32 v194, a59
v_mul_f32 v194, v194, v247
v_accvgpr_write_b32 a59, v194
v_accvgpr_read_b32 v193, a60
v_mul_f32 v193, v193, v247
v_accvgpr_write_b32 a60, v193
v_accvgpr_read_b32 v194, a61
v_mul_f32 v194, v194, v247
v_accvgpr_write_b32 a61, v194
v_accvgpr_read_b32 v193, a62
v_mul_f32 v193, v193, v247
v_accvgpr_write_b32 a62, v193
v_accvgpr_read_b32 v194, a63
v_mul_f32 v194, v194, v247
v_accvgpr_write_b32 a63, v194
s_nop 1
s_setpc_b64 s[60:61]
L_tr1b:
s_call_b64 s[60:61], L_rare10
s_branch L_back1b
L_rare10:
v_max3_f32 v246, v144, v145, v146
v_max3_f32 v246, v246, v147, v148
v_max3_f32 v246, v246, v149, v150
v_max_f32 v246, v246, v151
v_mov_b32 v247, v246
s_nop 1
v_permlane16_swap_b32 v246, v247
v_max_f32 v246, v246, v247
v_mov_b32 v247, v246
s_nop 1
v_permlane32_swap_b32 v246, v247
v_max_f32 v246, v246, v247
v_max_f32 v246, 0, v246
v_exp_f32 v247, -v246
v_sub_f32 v144, v144, v246
v_sub_f32 v145, v145, v246
v_sub_f32 v146, v146, v246
v_sub_f32 v147, v147, v246
v_sub_f32 v148, v148, v246
v_sub_f32 v149, v149, v246
v_sub_f32 v150, v150, v246
v_sub_f32 v151, v151, v246
v_sub_f32 v104, v104, v246
v_sub_f32 v105, v105, v246
v_sub_f32 v106, v106, v246
v_sub_f32 v107, v107, v246
v_mul_f32 v242, v242, v247
v_accvgpr_read_b32 v193, a64
v_mul_f32 v193, v193, v247
v_accvgpr_write_b32 a64, v193
v_accvgpr_read_b32 v194, a65
v_mul_f32 v194, v194, v247
v_accvgpr_write_b32 a65, v194
v_accvgpr_read_b32 v193, a66
v_mul_f32 v193, v193, v247
v_accvgpr_write_b32 a66, v193
v_accvgpr_read_b32 v194, a67
v_mul_f32 v194, v194, v247
v_accvgpr_write_b32 a67, v194
v_accvgpr_read_b32 v193, a68
v_mul_f32 v193, v193, v247
v_accvgpr_write_b32 a68, v193
v_accvgpr_read_b32 v194, a69
v_mul_f32 v194, v194, v247
v_accvgpr_write_b32 a69, v194
v_accvgpr_read_b32 v193, a70
v_mul_f32 v193, v193, v247
v_accvgpr_write_b32 a70, v193
v_accvgpr_read_b32 v194, a71
v_mul_f32 v194, v194, v247
v_accvgpr_write_b32 a71, v194
v_accvgpr_read_b32 v193, a72
v_mul_f32 v193, v193, v247
v_accvgpr_write_b32 a72, v193
v_accvgpr_read_b32 v194, a73
v_mul_f32 v194, v194, v247
v_accvgpr_write_b32 a73, v194
v_accvgpr_read_b32 v193, a74
v_mul_f32 v193, v193, v247
v_accvgpr_write_b32 a74, v193
v_accvgpr_read_b32 v194, a75
v_mul_f32 v194, v194, v247
v_accvgpr_write_b32 a75, v194
v_accvgpr_read_b32 v193, a76
v_mul_f32 v193, v193, v247
v_accvgpr_write_b32 a76, v193
v_accvgpr_read_b32 v194, a77
v_mul_f32 v194, v194, v247
v_accvgpr_write_b32 a77, v194
v_accvgpr_read_b32 v193, a78
v_mul_f32 v193, v193, v247
v_accvgpr_write_b32 a78, v193
v_accvgpr_read_b32 v194, a79
v_mul_f32 v194, v194, v247
v_accvgpr_write_b32 a79, v194
v_accvgpr_read_b32 v193, a80
v_mul_f32 v193, v193, v247
v_accvgpr_write_b32 a80, v193
v_accvgpr_read_b32 v194, a81
v_mul_f32 v194, v194, v247
v_accvgpr_write_b32 a81, v194
v_accvgpr_read_b32 v193, a82
v_mul_f32 v193, v193, v247
v_accvgpr_write_b32 a82, v193
v_accvgpr_read_b32 v194, a83
v_mul_f32 v194, v194, v247
v_accvgpr_write_b32 a83, v194
v_accvgpr_read_b32 v193, a84
v_mul_f32 v193, v193, v247
v_accvgpr_write_b32 a84, v193
v_accvgpr_read_b32 v194, a85
v_mul_f32 v194, v194, v247
v_accvgpr_write_b32 a85, v194
v_accvgpr_read_b32 v193, a86
v_mul_f32 v193, v193, v247
v_accvgpr_write_b32 a86, v193
v_accvgpr_read_b32 v194, a87
v_mul_f32 v194, v194, v247
v_accvgpr_write_b32 a87, v194
v_accvgpr_read_b32 v193, a88
v_mul_f32 v193, v193, v247
v_accvgpr_write_b32 a88, v193
v_accvgpr_read_b32 v194, a89
v_mul_f32 v194, v194, v247
v_accvgpr_write_b32 a89, v194
v_accvgpr_read_b32 v193, a90
v_mul_f32 v193, v193, v247
v_accvgpr_write_b32 a90, v193
v_accvgpr_read_b32 v194, a91
v_mul_f32 v194, v194, v247
v_accvgpr_write_b32 a91, v194
v_accvgpr_read_b32 v193, a92
v_mul_f32 v193, v193, v247
v_accvgpr_write_b32 a92, v193
v_accvgpr_read_b32 v194, a93
v_mul_f32 v194, v194, v247
v_accvgpr_write_b32 a93, v194
v_accvgpr_read_b32 v193, a94
v_mul_f32 v193, v193, v247
v_accvgpr_write_b32 a94, v193
v_accvgpr_read_b32 v194, a95
v_mul_f32 v194, v194, v247
v_accvgpr_write_b32 a95, v194
v_max3_f32 v246, v152, v153, v154
v_max3_f32 v246, v246, v155, v156
v_max3_f32 v246, v246, v157, v158
v_max_f32 v246, v246, v159
v_mov_b32 v247, v246
s_nop 1
v_permlane16_swap_b32 v246, v247
v_max_f32 v246, v246, v247
v_mov_b32 v247, v246
s_nop 1
v_permlane32_swap_b32 v246, v247
v_max_f32 v246, v246, v247
v_max_f32 v246, 0, v246
v_exp_f32 v247, -v246
v_sub_f32 v152, v152, v246
v_sub_f32 v153, v153, v246
v_sub_f32 v154, v154, v246
v_sub_f32 v155, v155, v246
v_sub_f32 v156, v156, v246
v_sub_f32 v157, v157, v246
v_sub_f32 v158, v158, v246
v_sub_f32 v159, v159, v246
v_sub_f32 v108, v108, v246
v_sub_f32 v109, v109, v246
v_sub_f32 v110, v110, v246
v_sub_f32 v111, v111, v246
v_mul_f32 v243, v243, v247
v_accvgpr_read_b32 v193, a96
v_mul_f32 v193, v193, v247
v_accvgpr_write_b32 a96, v193
v_accvgpr_read_b32 v194, a97
v_mul_f32 v194, v194, v247
v_accvgpr_write_b32 a97, v194
v_accvgpr_read_b32 v193, a98
v_mul_f32 v193, v193, v247
v_accvgpr_write_b32 a98, v193
v_accvgpr_read_b32 v194, a99
v_mul_f32 v194, v194, v247
v_accvgpr_write_b32 a99, v194
v_accvgpr_read_b32 v193, a100
v_mul_f32 v193, v193, v247
v_accvgpr_write_b32 a100, v193
v_accvgpr_read_b32 v194, a101
v_mul_f32 v194, v194, v247
v_accvgpr_write_b32 a101, v194
v_accvgpr_read_b32 v193, a102
v_mul_f32 v193, v193, v247
v_accvgpr_write_b32 a102, v193
v_accvgpr_read_b32 v194, a103
v_mul_f32 v194, v194, v247
v_accvgpr_write_b32 a103, v194
v_accvgpr_read_b32 v193, a104
v_mul_f32 v193, v193, v247
v_accvgpr_write_b32 a104, v193
v_accvgpr_read_b32 v194, a105
v_mul_f32 v194, v194, v247
v_accvgpr_write_b32 a105, v194
v_accvgpr_read_b32 v193, a106
v_mul_f32 v193, v193, v247
v_accvgpr_write_b32 a106, v193
v_accvgpr_read_b32 v194, a107
v_mul_f32 v194, v194, v247
v_accvgpr_write_b32 a107, v194
v_accvgpr_read_b32 v193, a108
v_mul_f32 v193, v193, v247
v_accvgpr_write_b32 a108, v193
v_accvgpr_read_b32 v194, a109
v_mul_f32 v194, v194, v247
v_accvgpr_write_b32 a109, v194
v_accvgpr_read_b32 v193, a110
v_mul_f32 v193, v193, v247
v_accvgpr_write_b32 a110, v193
v_accvgpr_read_b32 v194, a111
v_mul_f32 v194, v194, v247
v_accvgpr_write_b32 a111, v194
v_accvgpr_read_b32 v193, a112
v_mul_f32 v193, v193, v247
v_accvgpr_write_b32 a112, v193
v_accvgpr_read_b32 v194, a113
v_mul_f32 v194, v194, v247
v_accvgpr_write_b32 a113, v194
v_accvgpr_read_b32 v193, a114
v_mul_f32 v193, v193, v247
v_accvgpr_write_b32 a114, v193
v_accvgpr_read_b32 v194, a115
v_mul_f32 v194, v194, v247
v_accvgpr_write_b32 a115, v194
v_accvgpr_read_b32 v193, a116
v_mul_f32 v193, v193, v247
v_accvgpr_write_b32 a116, v193
v_accvgpr_read_b32 v194, a117
v_mul_f32 v194, v194, v247
v_accvgpr_write_b32 a117, v194
v_accvgpr_read_b32 v193, a118
v_mul_f32 v193, v193, v247
v_accvgpr_write_b32 a118, v193
v_accvgpr_read_b32 v194, a119
v_mul_f32 v194, v194, v247
v_accvgpr_write_b32 a119, v194
v_accvgpr_read_b32 v193, a120
v_mul_f32 v193, v193, v247
v_accvgpr_write_b32 a120, v193
v_accvgpr_read_b32 v194, a121
v_mul_f32 v194, v194, v247
v_accvgpr_write_b32 a121, v194
v_accvgpr_read_b32 v193, a122
v_mul_f32 v193, v193, v247
v_accvgpr_write_b32 a122, v193
v_accvgpr_read_b32 v194, a123
v_mul_f32 v194, v194, v247
v_accvgpr_write_b32 a123, v194
v_accvgpr_read_b32 v193, a124
v_mul_f32 v193, v193, v247
v_accvgpr_write_b32 a124, v193
v_accvgpr_read_b32 v194, a125
v_mul_f32 v194, v194, v247
v_accvgpr_write_b32 a125, v194
v_accvgpr_read_b32 v193, a126
v_mul_f32 v193, v193, v247
v_accvgpr_write_b32 a126, v193
v_accvgpr_read_b32 v194, a127
v_mul_f32 v194, v194, v247
v_accvgpr_write_b32 a127, v194
s_nop 1
s_setpc_b64 s[60:61]
L_tr1c:
s_call_b64 s[60:61], L_rare21
s_branch L_back1c
L_rare21:
v_max3_f32 v246, v160, v161, v162
v_max3_f32 v246, v246, v163, v164
v_max3_f32 v246, v246, v165, v166
v_max_f32 v246, v246, v167
v_mov_b32 v247, v246
s_nop 1
v_permlane16_swap_b32 v246, v247
v_max_f32 v246, v246, v247
v_mov_b32 v247, v246
s_nop 1
v_permlane32_swap_b32 v246, v247
v_max_f32 v246, v246, v247
v_max_f32 v246, 0, v246
v_exp_f32 v247, -v246
v_sub_f32 v160, v160, v246
v_sub_f32 v161, v161, v246
v_sub_f32 v162, v162, v246
v_sub_f32 v163, v163, v246
v_sub_f32 v164, v164, v246
v_sub_f32 v165, v165, v246
v_sub_f32 v166, v166, v246
v_sub_f32 v167, v167, v246
v_sub_f32 v112, v112, v246
v_sub_f32 v113, v113, v246
v_sub_f32 v114, v114, v246
v_sub_f32 v115, v115, v246
v_mul_f32 v244, v244, v247
v_accvgpr_read_b32 v193, a128
v_mul_f32 v193, v193, v247
v_accvgpr_write_b32 a128, v193
v_accvgpr_read_b32 v194, a129
v_mul_f32 v194, v194, v247
v_accvgpr_write_b32 a129, v194
v_accvgpr_read_b32 v193, a130
v_mul_f32 v193, v193, v247
v_accvgpr_write_b32 a130, v193
v_accvgpr_read_b32 v194, a131
v_mul_f32 v194, v194, v247
v_accvgpr_write_b32 a131, v194
v_accvgpr_read_b32 v193, a132
v_mul_f32 v193, v193, v247
v_accvgpr_write_b32 a132, v193
v_accvgpr_read_b32 v194, a133
v_mul_f32 v194, v194, v247
v_accvgpr_write_b32 a133, v194
v_accvgpr_read_b32 v193, a134
v_mul_f32 v193, v193, v247
v_accvgpr_write_b32 a134, v193
v_accvgpr_read_b32 v194, a135
v_mul_f32 v194, v194, v247
v_accvgpr_write_b32 a135, v194
v_accvgpr_read_b32 v193, a136
v_mul_f32 v193, v193, v247
v_accvgpr_write_b32 a136, v193
v_accvgpr_read_b32 v194, a137
v_mul_f32 v194, v194, v247
v_accvgpr_write_b32 a137, v194
v_accvgpr_read_b32 v193, a138
v_mul_f32 v193, v193, v247
v_accvgpr_write_b32 a138, v193
v_accvgpr_read_b32 v194, a139
v_mul_f32 v194, v194, v247
v_accvgpr_write_b32 a139, v194
v_accvgpr_read_b32 v193, a140
v_mul_f32 v193, v193, v247
v_accvgpr_write_b32 a140, v193
v_accvgpr_read_b32 v194, a141
v_mul_f32 v194, v194, v247
v_accvgpr_write_b32 a141, v194
v_accvgpr_read_b32 v193, a142
v_mul_f32 v193, v193, v247
v_accvgpr_write_b32 a142, v193
v_accvgpr_read_b32 v194, a143
v_mul_f32 v194, v194, v247
v_accvgpr_write_b32 a143, v194
v_accvgpr_read_b32 v193, a144
v_mul_f32 v193, v193, v247
v_accvgpr_write_b32 a144, v193
v_accvgpr_read_b32 v194, a145
v_mul_f32 v194, v194, v247
v_accvgpr_write_b32 a145, v194
v_accvgpr_read_b32 v193, a146
v_mul_f32 v193, v193, v247
v_accvgpr_write_b32 a146, v193
v_accvgpr_read_b32 v194, a147
v_mul_f32 v194, v194, v247
v_accvgpr_write_b32 a147, v194
v_accvgpr_read_b32 v193, a148
v_mul_f32 v193, v193, v247
v_accvgpr_write_b32 a148, v193
v_accvgpr_read_b32 v194, a149
v_mul_f32 v194, v194, v247
v_accvgpr_write_b32 a149, v194
v_accvgpr_read_b32 v193, a150
v_mul_f32 v193, v193, v247
v_accvgpr_write_b32 a150, v193
v_accvgpr_read_b32 v194, a151
v_mul_f32 v194, v194, v247
v_accvgpr_write_b32 a151, v194
v_accvgpr_read_b32 v193, a152
v_mul_f32 v193, v193, v247
v_accvgpr_write_b32 a152, v193
v_accvgpr_read_b32 v194, a153
v_mul_f32 v194, v194, v247
v_accvgpr_write_b32 a153, v194
v_accvgpr_read_b32 v193, a154
v_mul_f32 v193, v193, v247
v_accvgpr_write_b32 a154, v193
v_accvgpr_read_b32 v194, a155
v_mul_f32 v194, v194, v247
v_accvgpr_write_b32 a155, v194
v_accvgpr_read_b32 v193, a156
v_mul_f32 v193, v193, v247
v_accvgpr_write_b32 a156, v193
v_accvgpr_read_b32 v194, a157
v_mul_f32 v194, v194, v247
v_accvgpr_write_b32 a157, v194
v_accvgpr_read_b32 v193, a158
v_mul_f32 v193, v193, v247
v_accvgpr_write_b32 a158, v193
v_accvgpr_read_b32 v194, a159
v_mul_f32 v194, v194, v247
v_accvgpr_write_b32 a159, v194
v_max3_f32 v246, v168, v169, v170
v_max3_f32 v246, v246, v171, v172
v_max3_f32 v246, v246, v173, v174
v_max_f32 v246, v246, v175
v_mov_b32 v247, v246
s_nop 1
v_permlane16_swap_b32 v246, v247
v_max_f32 v246, v246, v247
v_mov_b32 v247, v246
s_nop 1
v_permlane32_swap_b32 v246, v247
v_max_f32 v246, v246, v247
v_max_f32 v246, 0, v246
v_exp_f32 v247, -v246
v_sub_f32 v168, v168, v246
v_sub_f32 v169, v169, v246
v_sub_f32 v170, v170, v246
v_sub_f32 v171, v171, v246
v_sub_f32 v172, v172, v246
v_sub_f32 v173, v173, v246
v_sub_f32 v174, v174, v246
v_sub_f32 v175, v175, v246
v_sub_f32 v116, v116, v246
v_sub_f32 v117, v117, v246
v_sub_f32 v118, v118, v246
v_sub_f32 v119, v119, v246
v_mul_f32 v245, v245, v247
v_accvgpr_read_b32 v193, a160
v_mul_f32 v193, v193, v247
v_accvgpr_write_b32 a160, v193
v_accvgpr_read_b32 v194, a161
v_mul_f32 v194, v194, v247
v_accvgpr_write_b32 a161, v194
v_accvgpr_read_b32 v193, a162
v_mul_f32 v193, v193, v247
v_accvgpr_write_b32 a162, v193
v_accvgpr_read_b32 v194, a163
v_mul_f32 v194, v194, v247
v_accvgpr_write_b32 a163, v194
v_accvgpr_read_b32 v193, a164
v_mul_f32 v193, v193, v247
v_accvgpr_write_b32 a164, v193
v_accvgpr_read_b32 v194, a165
v_mul_f32 v194, v194, v247
v_accvgpr_write_b32 a165, v194
v_accvgpr_read_b32 v193, a166
v_mul_f32 v193, v193, v247
v_accvgpr_write_b32 a166, v193
v_accvgpr_read_b32 v194, a167
v_mul_f32 v194, v194, v247
v_accvgpr_write_b32 a167, v194
v_accvgpr_read_b32 v193, a168
v_mul_f32 v193, v193, v247
v_accvgpr_write_b32 a168, v193
v_accvgpr_read_b32 v194, a169
v_mul_f32 v194, v194, v247
v_accvgpr_write_b32 a169, v194
v_accvgpr_read_b32 v193, a170
v_mul_f32 v193, v193, v247
v_accvgpr_write_b32 a170, v193
v_accvgpr_read_b32 v194, a171
v_mul_f32 v194, v194, v247
v_accvgpr_write_b32 a171, v194
v_accvgpr_read_b32 v193, a172
v_mul_f32 v193, v193, v247
v_accvgpr_write_b32 a172, v193
v_accvgpr_read_b32 v194, a173
v_mul_f32 v194, v194, v247
v_accvgpr_write_b32 a173, v194
v_accvgpr_read_b32 v193, a174
v_mul_f32 v193, v193, v247
v_accvgpr_write_b32 a174, v193
v_accvgpr_read_b32 v194, a175
v_mul_f32 v194, v194, v247
v_accvgpr_write_b32 a175, v194
v_accvgpr_read_b32 v193, a176
v_mul_f32 v193, v193, v247
v_accvgpr_write_b32 a176, v193
v_accvgpr_read_b32 v194, a177
v_mul_f32 v194, v194, v247
v_accvgpr_write_b32 a177, v194
v_accvgpr_read_b32 v193, a178
v_mul_f32 v193, v193, v247
v_accvgpr_write_b32 a178, v193
v_accvgpr_read_b32 v194, a179
v_mul_f32 v194, v194, v247
v_accvgpr_write_b32 a179, v194
v_accvgpr_read_b32 v193, a180
v_mul_f32 v193, v193, v247
v_accvgpr_write_b32 a180, v193
v_accvgpr_read_b32 v194, a181
v_mul_f32 v194, v194, v247
v_accvgpr_write_b32 a181, v194
v_accvgpr_read_b32 v193, a182
v_mul_f32 v193, v193, v247
v_accvgpr_write_b32 a182, v193
v_accvgpr_read_b32 v194, a183
v_mul_f32 v194, v194, v247
v_accvgpr_write_b32 a183, v194
v_accvgpr_read_b32 v193, a184
v_mul_f32 v193, v193, v247
v_accvgpr_write_b32 a184, v193
v_accvgpr_read_b32 v194, a185
v_mul_f32 v194, v194, v247
v_accvgpr_write_b32 a185, v194
v_accvgpr_read_b32 v193, a186
v_mul_f32 v193, v193, v247
v_accvgpr_write_b32 a186, v193
v_accvgpr_read_b32 v194, a187
v_mul_f32 v194, v194, v247
v_accvgpr_write_b32 a187, v194
v_accvgpr_read_b32 v193, a188
v_mul_f32 v193, v193, v247
v_accvgpr_write_b32 a188, v193
v_accvgpr_read_b32 v194, a189
v_mul_f32 v194, v194, v247
v_accvgpr_write_b32 a189, v194
v_accvgpr_read_b32 v193, a190
v_mul_f32 v193, v193, v247
v_accvgpr_write_b32 a190, v193
v_accvgpr_read_b32 v194, a191
v_mul_f32 v194, v194, v247
v_accvgpr_write_b32 a191, v194
s_nop 1
s_setpc_b64 s[60:61]
L_tr2a:
s_call_b64 s[60:61], L_rare00
s_branch L_back2a
L_rare00:
v_max3_f32 v246, v144, v145, v146
v_max3_f32 v246, v246, v147, v148
v_max3_f32 v246, v246, v149, v150
v_max_f32 v246, v246, v151
v_mov_b32 v247, v246
s_nop 1
v_permlane16_swap_b32 v246, v247
v_max_f32 v246, v246, v247
v_mov_b32 v247, v246
s_nop 1
v_permlane32_swap_b32 v246, v247
v_max_f32 v246, v246, v247
v_max_f32 v246, 0, v246
v_exp_f32 v247, -v246
v_sub_f32 v144, v144, v246
v_sub_f32 v145, v145, v246
v_sub_f32 v146, v146, v246
v_sub_f32 v147, v147, v246
v_sub_f32 v148, v148, v246
v_sub_f32 v149, v149, v246
v_sub_f32 v150, v150, v246
v_sub_f32 v151, v151, v246
v_sub_f32 v96, v96, v246
v_sub_f32 v97, v97, v246
v_sub_f32 v98, v98, v246
v_sub_f32 v99, v99, v246
v_mul_f32 v240, v240, v247
v_accvgpr_read_b32 v193, a0
v_mul_f32 v193, v193, v247
v_accvgpr_write_b32 a0, v193
v_accvgpr_read_b32 v194, a1
v_mul_f32 v194, v194, v247
v_accvgpr_write_b32 a1, v194
v_accvgpr_read_b32 v193, a2
v_mul_f32 v193, v193, v247
v_accvgpr_write_b32 a2, v193
v_accvgpr_read_b32 v194, a3
v_mul_f32 v194, v194, v247
v_accvgpr_write_b32 a3, v194
v_accvgpr_read_b32 v193, a4
v_mul_f32 v193, v193, v247
v_accvgpr_write_b32 a4, v193
v_accvgpr_read_b32 v194, a5
v_mul_f32 v194, v194, v247
v_accvgpr_write_b32 a5, v194
v_accvgpr_read_b32 v193, a6
v_mul_f32 v193, v193, v247
v_accvgpr_write_b32 a6, v193
v_accvgpr_read_b32 v194, a7
v_mul_f32 v194, v194, v247
v_accvgpr_write_b32 a7, v194
v_accvgpr_read_b32 v193, a8
v_mul_f32 v193, v193, v247
v_accvgpr_write_b32 a8, v193
v_accvgpr_read_b32 v194, a9
v_mul_f32 v194, v194, v247
v_accvgpr_write_b32 a9, v194
v_accvgpr_read_b32 v193, a10
v_mul_f32 v193, v193, v247
v_accvgpr_write_b32 a10, v193
v_accvgpr_read_b32 v194, a11
v_mul_f32 v194, v194, v247
v_accvgpr_write_b32 a11, v194
v_accvgpr_read_b32 v193, a12
v_mul_f32 v193, v193, v247
v_accvgpr_write_b32 a12, v193
v_accvgpr_read_b32 v194, a13
v_mul_f32 v194, v194, v247
v_accvgpr_write_b32 a13, v194
v_accvgpr_read_b32 v193, a14
v_mul_f32 v193, v193, v247
v_accvgpr_write_b32 a14, v193
v_accvgpr_read_b32 v194, a15
v_mul_f32 v194, v194, v247
v_accvgpr_write_b32 a15, v194
v_accvgpr_read_b32 v193, a16
v_mul_f32 v193, v193, v247
v_accvgpr_write_b32 a16, v193
v_accvgpr_read_b32 v194, a17
v_mul_f32 v194, v194, v247
v_accvgpr_write_b32 a17, v194
v_accvgpr_read_b32 v193, a18
v_mul_f32 v193, v193, v247
v_accvgpr_write_b32 a18, v193
v_accvgpr_read_b32 v194, a19
v_mul_f32 v194, v194, v247
v_accvgpr_write_b32 a19, v194
v_accvgpr_read_b32 v193, a20
v_mul_f32 v193, v193, v247
v_accvgpr_write_b32 a20, v193
v_accvgpr_read_b32 v194, a21
v_mul_f32 v194, v194, v247
v_accvgpr_write_b32 a21, v194
v_accvgpr_read_b32 v193, a22
v_mul_f32 v193, v193, v247
v_accvgpr_write_b32 a22, v193
v_accvgpr_read_b32 v194, a23
v_mul_f32 v194, v194, v247
v_accvgpr_write_b32 a23, v194
v_accvgpr_read_b32 v193, a24
v_mul_f32 v193, v193, v247
v_accvgpr_write_b32 a24, v193
v_accvgpr_read_b32 v194, a25
v_mul_f32 v194, v194, v247
v_accvgpr_write_b32 a25, v194
v_accvgpr_read_b32 v193, a26
v_mul_f32 v193, v193, v247
v_accvgpr_write_b32 a26, v193
v_accvgpr_read_b32 v194, a27
v_mul_f32 v194, v194, v247
v_accvgpr_write_b32 a27, v194
v_accvgpr_read_b32 v193, a28
v_mul_f32 v193, v193, v247
v_accvgpr_write_b32 a28, v193
v_accvgpr_read_b32 v194, a29
v_mul_f32 v194, v194, v247
v_accvgpr_write_b32 a29, v194
v_accvgpr_read_b32 v193, a30
v_mul_f32 v193, v193, v247
v_accvgpr_write_b32 a30, v193
v_accvgpr_read_b32 v194, a31
v_mul_f32 v194, v194, v247
v_accvgpr_write_b32 a31, v194
v_max3_f32 v246, v152, v153, v154
v_max3_f32 v246, v246, v155, v156
v_max3_f32 v246, v246, v157, v158
v_max_f32 v246, v246, v159
v_mov_b32 v247, v246
s_nop 1
v_permlane16_swap_b32 v246, v247
v_max_f32 v246, v246, v247
v_mov_b32 v247, v246
s_nop 1
v_permlane32_swap_b32 v246, v247
v_max_f32 v246, v246, v247
v_max_f32 v246, 0, v246
v_exp_f32 v247, -v246
v_sub_f32 v152, v152, v246
v_sub_f32 v153, v153, v246
v_sub_f32 v154, v154, v246
v_sub_f32 v155, v155, v246
v_sub_f32 v156, v156, v246
v_sub_f32 v157, v157, v246
v_sub_f32 v158, v158, v246
v_sub_f32 v159, v159, v246
v_sub_f32 v100, v100, v246
v_sub_f32 v101, v101, v246
v_sub_f32 v102, v102, v246
v_sub_f32 v103, v103, v246
v_mul_f32 v241, v241, v247
v_accvgpr_read_b32 v193, a32
v_mul_f32 v193, v193, v247
v_accvgpr_write_b32 a32, v193
v_accvgpr_read_b32 v194, a33
v_mul_f32 v194, v194, v247
v_accvgpr_write_b32 a33, v194
v_accvgpr_read_b32 v193, a34
v_mul_f32 v193, v193, v247
v_accvgpr_write_b32 a34, v193
v_accvgpr_read_b32 v194, a35
v_mul_f32 v194, v194, v247
v_accvgpr_write_b32 a35, v194
v_accvgpr_read_b32 v193, a36
v_mul_f32 v193, v193, v247
v_accvgpr_write_b32 a36, v193
v_accvgpr_read_b32 v194, a37
v_mul_f32 v194, v194, v247
v_accvgpr_write_b32 a37, v194
v_accvgpr_read_b32 v193, a38
v_mul_f32 v193, v193, v247
v_accvgpr_write_b32 a38, v193
v_accvgpr_read_b32 v194, a39
v_mul_f32 v194, v194, v247
v_accvgpr_write_b32 a39, v194
v_accvgpr_read_b32 v193, a40
v_mul_f32 v193, v193, v247
v_accvgpr_write_b32 a40, v193
v_accvgpr_read_b32 v194, a41
v_mul_f32 v194, v194, v247
v_accvgpr_write_b32 a41, v194
v_accvgpr_read_b32 v193, a42
v_mul_f32 v193, v193, v247
v_accvgpr_write_b32 a42, v193
v_accvgpr_read_b32 v194, a43
v_mul_f32 v194, v194, v247
v_accvgpr_write_b32 a43, v194
v_accvgpr_read_b32 v193, a44
v_mul_f32 v193, v193, v247
v_accvgpr_write_b32 a44, v193
v_accvgpr_read_b32 v194, a45
v_mul_f32 v194, v194, v247
v_accvgpr_write_b32 a45, v194
v_accvgpr_read_b32 v193, a46
v_mul_f32 v193, v193, v247
v_accvgpr_write_b32 a46, v193
v_accvgpr_read_b32 v194, a47
v_mul_f32 v194, v194, v247
v_accvgpr_write_b32 a47, v194
v_accvgpr_read_b32 v193, a48
v_mul_f32 v193, v193, v247
v_accvgpr_write_b32 a48, v193
v_accvgpr_read_b32 v194, a49
v_mul_f32 v194, v194, v247
v_accvgpr_write_b32 a49, v194
v_accvgpr_read_b32 v193, a50
v_mul_f32 v193, v193, v247
v_accvgpr_write_b32 a50, v193
v_accvgpr_read_b32 v194, a51
v_mul_f32 v194, v194, v247
v_accvgpr_write_b32 a51, v194
v_accvgpr_read_b32 v193, a52
v_mul_f32 v193, v193, v247
v_accvgpr_write_b32 a52, v193
v_accvgpr_read_b32 v194, a53
v_mul_f32 v194, v194, v247
v_accvgpr_write_b32 a53, v194
v_accvgpr_read_b32 v193, a54
v_mul_f32 v193, v193, v247
v_accvgpr_write_b32 a54, v193
v_accvgpr_read_b32 v194, a55
v_mul_f32 v194, v194, v247
v_accvgpr_write_b32 a55, v194
v_accvgpr_read_b32 v193, a56
v_mul_f32 v193, v193, v247
v_accvgpr_write_b32 a56, v193
v_accvgpr_read_b32 v194, a57
v_mul_f32 v194, v194, v247
v_accvgpr_write_b32 a57, v194
v_accvgpr_read_b32 v193, a58
v_mul_f32 v193, v193, v247
v_accvgpr_write_b32 a58, v193
v_accvgpr_read_b32 v194, a59
v_mul_f32 v194, v194, v247
v_accvgpr_write_b32 a59, v194
v_accvgpr_read_b32 v193, a60
v_mul_f32 v193, v193, v247
v_accvgpr_write_b32 a60, v193
v_accvgpr_read_b32 v194, a61
v_mul_f32 v194, v194, v247
v_accvgpr_write_b32 a61, v194
v_accvgpr_read_b32 v193, a62
v_mul_f32 v193, v193, v247
v_accvgpr_write_b32 a62, v193
v_accvgpr_read_b32 v194, a63
v_mul_f32 v194, v194, v247
v_accvgpr_write_b32 a63, v194
s_nop 1
s_setpc_b64 s[60:61]
L_tr2b:
s_call_b64 s[60:61], L_rare11
s_branch L_back2b
L_rare11:
v_max3_f32 v246, v160, v161, v162
v_max3_f32 v246, v246, v163, v164
v_max3_f32 v246, v246, v165, v166
v_max_f32 v246, v246, v167
v_mov_b32 v247, v246
s_nop 1
v_permlane16_swap_b32 v246, v247
v_max_f32 v246, v246, v247
v_mov_b32 v247, v246
s_nop 1
v_permlane32_swap_b32 v246, v247
v_max_f32 v246, v246, v247
v_max_f32 v246, 0, v246
v_exp_f32 v247, -v246
v_sub_f32 v160, v160, v246
v_sub_f32 v161, v161, v246
v_sub_f32 v162, v162, v246
v_sub_f32 v163, v163, v246
v_sub_f32 v164, v164, v246
v_sub_f32 v165, v165, v246
v_sub_f32 v166, v166, v246
v_sub_f32 v167, v167, v246
v_sub_f32 v104, v104, v246
v_sub_f32 v105, v105, v246
v_sub_f32 v106, v106, v246
v_sub_f32 v107, v107, v246
v_mul_f32 v242, v242, v247
v_accvgpr_read_b32 v193, a64
v_mul_f32 v193, v193, v247
v_accvgpr_write_b32 a64, v193
v_accvgpr_read_b32 v194, a65
v_mul_f32 v194, v194, v247
v_accvgpr_write_b32 a65, v194
v_accvgpr_read_b32 v193, a66
)ASM"
R"ASM(v_mul_f32 v193, v193, v247
v_accvgpr_write_b32 a66, v193
v_accvgpr_read_b32 v194, a67
v_mul_f32 v194, v194, v247
v_accvgpr_write_b32 a67, v194
v_accvgpr_read_b32 v193, a68
v_mul_f32 v193, v193, v247
v_accvgpr_write_b32 a68, v193
v_accvgpr_read_b32 v194, a69
v_mul_f32 v194, v194, v247
v_accvgpr_write_b32 a69, v194
v_accvgpr_read_b32 v193, a70
v_mul_f32 v193, v193, v247
v_accvgpr_write_b32 a70, v193
v_accvgpr_read_b32 v194, a71
v_mul_f32 v194, v194, v247
v_accvgpr_write_b32 a71, v194
v_accvgpr_read_b32 v193, a72
v_mul_f32 v193, v193, v247
v_accvgpr_write_b32 a72, v193
v_accvgpr_read_b32 v194, a73
v_mul_f32 v194, v194, v247
v_accvgpr_write_b32 a73, v194
v_accvgpr_read_b32 v193, a74
v_mul_f32 v193, v193, v247
v_accvgpr_write_b32 a74, v193
v_accvgpr_read_b32 v194, a75
v_mul_f32 v194, v194, v247
v_accvgpr_write_b32 a75, v194
v_accvgpr_read_b32 v193, a76
v_mul_f32 v193, v193, v247
v_accvgpr_write_b32 a76, v193
v_accvgpr_read_b32 v194, a77
v_mul_f32 v194, v194, v247
v_accvgpr_write_b32 a77, v194
v_accvgpr_read_b32 v193, a78
v_mul_f32 v193, v193, v247
v_accvgpr_write_b32 a78, v193
v_accvgpr_read_b32 v194, a79
v_mul_f32 v194, v194, v247
v_accvgpr_write_b32 a79, v194
v_accvgpr_read_b32 v193, a80
v_mul_f32 v193, v193, v247
v_accvgpr_write_b32 a80, v193
v_accvgpr_read_b32 v194, a81
v_mul_f32 v194, v194, v247
v_accvgpr_write_b32 a81, v194
v_accvgpr_read_b32 v193, a82
v_mul_f32 v193, v193, v247
v_accvgpr_write_b32 a82, v193
v_accvgpr_read_b32 v194, a83
v_mul_f32 v194, v194, v247
v_accvgpr_write_b32 a83, v194
v_accvgpr_read_b32 v193, a84
v_mul_f32 v193, v193, v247
v_accvgpr_write_b32 a84, v193
v_accvgpr_read_b32 v194, a85
v_mul_f32 v194, v194, v247
v_accvgpr_write_b32 a85, v194
v_accvgpr_read_b32 v193, a86
v_mul_f32 v193, v193, v247
v_accvgpr_write_b32 a86, v193
v_accvgpr_read_b32 v194, a87
v_mul_f32 v194, v194, v247
v_accvgpr_write_b32 a87, v194
v_accvgpr_read_b32 v193, a88
v_mul_f32 v193, v193, v247
v_accvgpr_write_b32 a88, v193
v_accvgpr_read_b32 v194, a89
v_mul_f32 v194, v194, v247
v_accvgpr_write_b32 a89, v194
v_accvgpr_read_b32 v193, a90
v_mul_f32 v193, v193, v247
v_accvgpr_write_b32 a90, v193
v_accvgpr_read_b32 v194, a91
v_mul_f32 v194, v194, v247
v_accvgpr_write_b32 a91, v194
v_accvgpr_read_b32 v193, a92
v_mul_f32 v193, v193, v247
v_accvgpr_write_b32 a92, v193
v_accvgpr_read_b32 v194, a93
v_mul_f32 v194, v194, v247
v_accvgpr_write_b32 a93, v194
v_accvgpr_read_b32 v193, a94
v_mul_f32 v193, v193, v247
v_accvgpr_write_b32 a94, v193
v_accvgpr_read_b32 v194, a95
v_mul_f32 v194, v194, v247
v_accvgpr_write_b32 a95, v194
v_max3_f32 v246, v168, v169, v170
v_max3_f32 v246, v246, v171, v172
v_max3_f32 v246, v246, v173, v174
v_max_f32 v246, v246, v175
v_mov_b32 v247, v246
s_nop 1
v_permlane16_swap_b32 v246, v247
v_max_f32 v246, v246, v247
v_mov_b32 v247, v246
s_nop 1
v_permlane32_swap_b32 v246, v247
v_max_f32 v246, v246, v247
v_max_f32 v246, 0, v246
v_exp_f32 v247, -v246
v_sub_f32 v168, v168, v246
v_sub_f32 v169, v169, v246
v_sub_f32 v170, v170, v246
v_sub_f32 v171, v171, v246
v_sub_f32 v172, v172, v246
v_sub_f32 v173, v173, v246
v_sub_f32 v174, v174, v246
v_sub_f32 v175, v175, v246
v_sub_f32 v108, v108, v246
v_sub_f32 v109, v109, v246
v_sub_f32 v110, v110, v246
v_sub_f32 v111, v111, v246
v_mul_f32 v243, v243, v247
v_accvgpr_read_b32 v193, a96
v_mul_f32 v193, v193, v247
v_accvgpr_write_b32 a96, v193
v_accvgpr_read_b32 v194, a97
v_mul_f32 v194, v194, v247
v_accvgpr_write_b32 a97, v194
v_accvgpr_read_b32 v193, a98
v_mul_f32 v193, v193, v247
v_accvgpr_write_b32 a98, v193
v_accvgpr_read_b32 v194, a99
v_mul_f32 v194, v194, v247
v_accvgpr_write_b32 a99, v194
v_accvgpr_read_b32 v193, a100
v_mul_f32 v193, v193, v247
v_accvgpr_write_b32 a100, v193
v_accvgpr_read_b32 v194, a101
v_mul_f32 v194, v194, v247
v_accvgpr_write_b32 a101, v194
v_accvgpr_read_b32 v193, a102
v_mul_f32 v193, v193, v247
v_accvgpr_write_b32 a102, v193
v_accvgpr_read_b32 v194, a103
v_mul_f32 v194, v194, v247
v_accvgpr_write_b32 a103, v194
v_accvgpr_read_b32 v193, a104
v_mul_f32 v193, v193, v247
v_accvgpr_write_b32 a104, v193
v_accvgpr_read_b32 v194, a105
v_mul_f32 v194, v194, v247
v_accvgpr_write_b32 a105, v194
v_accvgpr_read_b32 v193, a106
v_mul_f32 v193, v193, v247
v_accvgpr_write_b32 a106, v193
v_accvgpr_read_b32 v194, a107
v_mul_f32 v194, v194, v247
v_accvgpr_write_b32 a107, v194
v_accvgpr_read_b32 v193, a108
v_mul_f32 v193, v193, v247
v_accvgpr_write_b32 a108, v193
v_accvgpr_read_b32 v194, a109
v_mul_f32 v194, v194, v247
v_accvgpr_write_b32 a109, v194
v_accvgpr_read_b32 v193, a110
v_mul_f32 v193, v193, v247
v_accvgpr_write_b32 a110, v193
v_accvgpr_read_b32 v194, a111
v_mul_f32 v194, v194, v247
v_accvgpr_write_b32 a111, v194
v_accvgpr_read_b32 v193, a112
v_mul_f32 v193, v193, v247
v_accvgpr_write_b32 a112, v193
v_accvgpr_read_b32 v194, a113
v_mul_f32 v194, v194, v247
v_accvgpr_write_b32 a113, v194
v_accvgpr_read_b32 v193, a114
v_mul_f32 v193, v193, v247
v_accvgpr_write_b32 a114, v193
v_accvgpr_read_b32 v194, a115
v_mul_f32 v194, v194, v247
v_accvgpr_write_b32 a115, v194
v_accvgpr_read_b32 v193, a116
v_mul_f32 v193, v193, v247
v_accvgpr_write_b32 a116, v193
v_accvgpr_read_b32 v194, a117
v_mul_f32 v194, v194, v247
v_accvgpr_write_b32 a117, v194
v_accvgpr_read_b32 v193, a118
v_mul_f32 v193, v193, v247
v_accvgpr_write_b32 a118, v193
v_accvgpr_read_b32 v194, a119
v_mul_f32 v194, v194, v247
v_accvgpr_write_b32 a119, v194
v_accvgpr_read_b32 v193, a120
v_mul_f32 v193, v193, v247
v_accvgpr_write_b32 a120, v193
v_accvgpr_read_b32 v194, a121
v_mul_f32 v194, v194, v247
v_accvgpr_write_b32 a121, v194
v_accvgpr_read_b32 v193, a122
v_mul_f32 v193, v193, v247
v_accvgpr_write_b32 a122, v193
v_accvgpr_read_b32 v194, a123
v_mul_f32 v194, v194, v247
v_accvgpr_write_b32 a123, v194
v_accvgpr_read_b32 v193, a124
v_mul_f32 v193, v193, v247
v_accvgpr_write_b32 a124, v193
v_accvgpr_read_b32 v194, a125
v_mul_f32 v194, v194, v247
v_accvgpr_write_b32 a125, v194
v_accvgpr_read_b32 v193, a126
v_mul_f32 v193, v193, v247
v_accvgpr_write_b32 a126, v193
v_accvgpr_read_b32 v194, a127
v_mul_f32 v194, v194, v247
v_accvgpr_write_b32 a127, v194
s_nop 1
s_setpc_b64 s[60:61]
L_tr2c:
s_call_b64 s[60:61], L_rare20
s_branch L_back2c
L_rare20:
v_max3_f32 v246, v144, v145, v146
v_max3_f32 v246, v246, v147, v148
v_max3_f32 v246, v246, v149, v150
v_max_f32 v246, v246, v151
v_mov_b32 v247, v246
s_nop 1
v_permlane16_swap_b32 v246, v247
v_max_f32 v246, v246, v247
v_mov_b32 v247, v246
s_nop 1
v_permlane32_swap_b32 v246, v247
v_max_f32 v246, v246, v247
v_max_f32 v246, 0, v246
v_exp_f32 v247, -v246
v_sub_f32 v144, v144, v246
v_sub_f32 v145, v145, v246
v_sub_f32 v146, v146, v246
v_sub_f32 v147, v147, v246
v_sub_f32 v148, v148, v246
v_sub_f32 v149, v149, v246
v_sub_f32 v150, v150, v246
v_sub_f32 v151, v151, v246
v_sub_f32 v112, v112, v246
v_sub_f32 v113, v113, v246
v_sub_f32 v114, v114, v246
v_sub_f32 v115, v115, v246
v_mul_f32 v244, v244, v247
v_accvgpr_read_b32 v193, a128
v_mul_f32 v193, v193, v247
v_accvgpr_write_b32 a128, v193
v_accvgpr_read_b32 v194, a129
v_mul_f32 v194, v194, v247
v_accvgpr_write_b32 a129, v194
v_accvgpr_read_b32 v193, a130
v_mul_f32 v193, v193, v247
v_accvgpr_write_b32 a130, v193
v_accvgpr_read_b32 v194, a131
v_mul_f32 v194, v194, v247
v_accvgpr_write_b32 a131, v194
v_accvgpr_read_b32 v193, a132
v_mul_f32 v193, v193, v247
v_accvgpr_write_b32 a132, v193
v_accvgpr_read_b32 v194, a133
v_mul_f32 v194, v194, v247
v_accvgpr_write_b32 a133, v194
v_accvgpr_read_b32 v193, a134
v_mul_f32 v193, v193, v247
v_accvgpr_write_b32 a134, v193
v_accvgpr_read_b32 v194, a135
v_mul_f32 v194, v194, v247
v_accvgpr_write_b32 a135, v194
v_accvgpr_read_b32 v193, a136
v_mul_f32 v193, v193, v247
v_accvgpr_write_b32 a136, v193
v_accvgpr_read_b32 v194, a137
v_mul_f32 v194, v194, v247
v_accvgpr_write_b32 a137, v194
v_accvgpr_read_b32 v193, a138
v_mul_f32 v193, v193, v247
v_accvgpr_write_b32 a138, v193
v_accvgpr_read_b32 v194, a139
v_mul_f32 v194, v194, v247
v_accvgpr_write_b32 a139, v194
v_accvgpr_read_b32 v193, a140
v_mul_f32 v193, v193, v247
v_accvgpr_write_b32 a140, v193
v_accvgpr_read_b32 v194, a141
v_mul_f32 v194, v194, v247
v_accvgpr_write_b32 a141, v194
v_accvgpr_read_b32 v193, a142
v_mul_f32 v193, v193, v247
v_accvgpr_write_b32 a142, v193
v_accvgpr_read_b32 v194, a143
v_mul_f32 v194, v194, v247
v_accvgpr_write_b32 a143, v194
v_accvgpr_read_b32 v193, a144
v_mul_f32 v193, v193, v247
v_accvgpr_write_b32 a144, v193
v_accvgpr_read_b32 v194, a145
v_mul_f32 v194, v194, v247
v_accvgpr_write_b32 a145, v194
v_accvgpr_read_b32 v193, a146
v_mul_f32 v193, v193, v247
v_accvgpr_write_b32 a146, v193
v_accvgpr_read_b32 v194, a147
v_mul_f32 v194, v194, v247
v_accvgpr_write_b32 a147, v194
v_accvgpr_read_b32 v193, a148
v_mul_f32 v193, v193, v247
v_accvgpr_write_b32 a148, v193
v_accvgpr_read_b32 v194, a149
v_mul_f32 v194, v194, v247
v_accvgpr_write_b32 a149, v194
v_accvgpr_read_b32 v193, a150
v_mul_f32 v193, v193, v247
v_accvgpr_write_b32 a150, v193
v_accvgpr_read_b32 v194, a151
v_mul_f32 v194, v194, v247
v_accvgpr_write_b32 a151, v194
v_accvgpr_read_b32 v193, a152
v_mul_f32 v193, v193, v247
v_accvgpr_write_b32 a152, v193
v_accvgpr_read_b32 v194, a153
v_mul_f32 v194, v194, v247
v_accvgpr_write_b32 a153, v194
v_accvgpr_read_b32 v193, a154
v_mul_f32 v193, v193, v247
v_accvgpr_write_b32 a154, v193
v_accvgpr_read_b32 v194, a155
v_mul_f32 v194, v194, v247
v_accvgpr_write_b32 a155, v194
v_accvgpr_read_b32 v193, a156
v_mul_f32 v193, v193, v247
v_accvgpr_write_b32 a156, v193
v_accvgpr_read_b32 v194, a157
v_mul_f32 v194, v194, v247
v_accvgpr_write_b32 a157, v194
v_accvgpr_read_b32 v193, a158
v_mul_f32 v193, v193, v247
v_accvgpr_write_b32 a158, v193
v_accvgpr_read_b32 v194, a159
v_mul_f32 v194, v194, v247
v_accvgpr_write_b32 a159, v194
v_max3_f32 v246, v152, v153, v154
v_max3_f32 v246, v246, v155, v156
v_max3_f32 v246, v246, v157, v158
v_max_f32 v246, v246, v159
v_mov_b32 v247, v246
s_nop 1
v_permlane16_swap_b32 v246, v247
v_max_f32 v246, v246, v247
v_mov_b32 v247, v246
s_nop 1
v_permlane32_swap_b32 v246, v247
v_max_f32 v246, v246, v247
v_max_f32 v246, 0, v246
v_exp_f32 v247, -v246
v_sub_f32 v152, v152, v246
v_sub_f32 v153, v153, v246
v_sub_f32 v154, v154, v246
v_sub_f32 v155, v155, v246
v_sub_f32 v156, v156, v246
v_sub_f32 v157, v157, v246
v_sub_f32 v158, v158, v246
v_sub_f32 v159, v159, v246
v_sub_f32 v116, v116, v246
v_sub_f32 v117, v117, v246
v_sub_f32 v118, v118, v246
v_sub_f32 v119, v119, v246
v_mul_f32 v245, v245, v247
v_accvgpr_read_b32 v193, a160
v_mul_f32 v193, v193, v247
v_accvgpr_write_b32 a160, v193
v_accvgpr_read_b32 v194, a161
v_mul_f32 v194, v194, v247
v_accvgpr_write_b32 a161, v194
v_accvgpr_read_b32 v193, a162
v_mul_f32 v193, v193, v247
v_accvgpr_write_b32 a162, v193
v_accvgpr_read_b32 v194, a163
v_mul_f32 v194, v194, v247
v_accvgpr_write_b32 a163, v194
v_accvgpr_read_b32 v193, a164
v_mul_f32 v193, v193, v247
v_accvgpr_write_b32 a164, v193
v_accvgpr_read_b32 v194, a165
v_mul_f32 v194, v194, v247
v_accvgpr_write_b32 a165, v194
v_accvgpr_read_b32 v193, a166
v_mul_f32 v193, v193, v247
v_accvgpr_write_b32 a166, v193
v_accvgpr_read_b32 v194, a167
v_mul_f32 v194, v194, v247
v_accvgpr_write_b32 a167, v194
v_accvgpr_read_b32 v193, a168
v_mul_f32 v193, v193, v247
v_accvgpr_write_b32 a168, v193
v_accvgpr_read_b32 v194, a169
v_mul_f32 v194, v194, v247
v_accvgpr_write_b32 a169, v194
v_accvgpr_read_b32 v193, a170
v_mul_f32 v193, v193, v247
v_accvgpr_write_b32 a170, v193
v_accvgpr_read_b32 v194, a171
v_mul_f32 v194, v194, v247
v_accvgpr_write_b32 a171, v194
v_accvgpr_read_b32 v193, a172
v_mul_f32 v193, v193, v247
v_accvgpr_write_b32 a172, v193
v_accvgpr_read_b32 v194, a173
v_mul_f32 v194, v194, v247
v_accvgpr_write_b32 a173, v194
v_accvgpr_read_b32 v193, a174
v_mul_f32 v193, v193, v247
v_accvgpr_write_b32 a174, v193
v_accvgpr_read_b32 v194, a175
v_mul_f32 v194, v194, v247
v_accvgpr_write_b32 a175, v194
v_accvgpr_read_b32 v193, a176
v_mul_f32 v193, v193, v247
v_accvgpr_write_b32 a176, v193
v_accvgpr_read_b32 v194, a177
v_mul_f32 v194, v194, v247
v_accvgpr_write_b32 a177, v194
v_accvgpr_read_b32 v193, a178
v_mul_f32 v193, v193, v247
v_accvgpr_write_b32 a178, v193
v_accvgpr_read_b32 v194, a179
v_mul_f32 v194, v194, v247
v_accvgpr_write_b32 a179, v194
v_accvgpr_read_b32 v193, a180
v_mul_f32 v193, v193, v247
v_accvgpr_write_b32 a180, v193
v_accvgpr_read_b32 v194, a181
v_mul_f32 v194, v194, v247
v_accvgpr_write_b32 a181, v194
v_accvgpr_read_b32 v193, a182
v_mul_f32 v193, v193, v247
v_accvgpr_write_b32 a182, v193
v_accvgpr_read_b32 v194, a183
v_mul_f32 v194, v194, v247
v_accvgpr_write_b32 a183, v194
v_accvgpr_read_b32 v193, a184
v_mul_f32 v193, v193, v247
v_accvgpr_write_b32 a184, v193
v_accvgpr_read_b32 v194, a185
v_mul_f32 v194, v194, v247
v_accvgpr_write_b32 a185, v194
v_accvgpr_read_b32 v193, a186
v_mul_f32 v193, v193, v247
v_accvgpr_write_b32 a186, v193
v_accvgpr_read_b32 v194, a187
v_mul_f32 v194, v194, v247
v_accvgpr_write_b32 a187, v194
v_accvgpr_read_b32 v193, a188
v_mul_f32 v193, v193, v247
v_accvgpr_write_b32 a188, v193
v_accvgpr_read_b32 v194, a189
v_mul_f32 v194, v194, v247
v_accvgpr_write_b32 a189, v194
v_accvgpr_read_b32 v193, a190
v_mul_f32 v193, v193, v247
v_accvgpr_write_b32 a190, v193
v_accvgpr_read_b32 v194, a191
v_mul_f32 v194, v194, v247
v_accvgpr_write_b32 a191, v194
s_nop 1
s_setpc_b64 s[60:61]
L_tr3a:
s_call_b64 s[60:61], L_rare01
s_branch L_back3a
L_tr3b:
s_call_b64 s[60:61], L_rare10
s_branch L_back3b
L_tr3c:
s_call_b64 s[60:61], L_rare21
s_branch L_back3c
L_tr4a:
s_call_b64 s[60:61], L_rare00
s_branch L_back4a
L_tr4b:
s_call_b64 s[60:61], L_rare11
s_branch L_back4b
L_tr4c:
s_call_b64 s[60:61], L_rare20
s_branch L_back4c
L_tr5a:
s_call_b64 s[60:61], L_rare01
s_branch L_back5a
L_tr5b:
s_call_b64 s[60:61], L_rare10
s_branch L_back5b
L_tr5c:
s_call_b64 s[60:61], L_rare21
s_branch L_back5c
L_trL6a:
s_call_b64 s[60:61], L_rare00
s_branch L_backL6a
L_trL6b:
s_call_b64 s[60:61], L_rare11
s_branch L_backL6b
L_trL6c:
s_call_b64 s[60:61], L_rare20
s_branch L_backL6c
L_trL7a:
s_call_b64 s[60:61], L_rare01
s_branch L_backL7a
L_trL7b:
s_call_b64 s[60:61], L_rare10
s_branch L_backL7b
L_trL7c:
s_call_b64 s[60:61], L_rare21
s_branch L_backL7c
L_tr14a:
s_call_b64 s[60:61], L_rare00
s_branch L_back14a
L_tr14b:
s_call_b64 s[60:61], L_rare11
s_branch L_back14b
L_tr14c:
s_call_b64 s[60:61], L_rare20
s_branch L_back14c
L_tr15a:
s_call_b64 s[60:61], L_rare01
s_branch L_back15a
L_tr15b:
s_call_b64 s[60:61], L_rare10
s_branch L_back15b
L_tr15c:
s_call_b64 s[60:61], L_rare21
s_branch L_back15c
L_hs_slow:
s_add_u32 s92, s92, 1
s_cmp_gt_u32 s92, 0x40000
s_cbranch_scc1 L_hs_ok
s_sleep 4
buffer_load_dword v215, off, s[40:43], s77 offset:2816 sc1
s_waitcnt vmcnt(0)
s_branch L_hs
)ASM"
  :: [ka] "s"(ka), [bid] "s"(blockIdx.x), [w] "s"(w), [lds] "s"((unsigned)(size_t)smem)
  : "v0", "v1", "v2", "v3", "v4", "v5", "v6", "v7", "v8", "v9", "v10", "v11", "v12", "v13", "v14", "v15", "v16", "v17", "v18", "v19", "v20", "v21", "v22", "v23", "v24", "v25", "v26", "v27", "v28", "v29", "v30", "v31", "v32", "v33", "v34", "v35", "v36", "v37", "v38", "v39", "v40", "v41", "v42", "v43", "v44", "v45", "v46", "v47", "v48", "v49", "v50", "v51", "v52", "v53", "v54", "v55", "v56", "v57", "v58", "v59", "v60", "v61", "v62", "v63", "v64", "v65", "v66", "v67", "v68", "v69", "v70", "v71", "v72", "v73", "v74", "v75", "v76", "v77", "v78", "v79", "v80", "v81", "v82", "v83", "v84", "v85", "v86", "v87", "v88", "v89", "v90", "v91", "v92", "v93", "v94", "v95", "v96", "v97", "v98", "v99", "v100", "v101", "v102", "v103", "v104", "v105", "v106", "v107", "v108", "v109", "v110", "v111", "v112", "v113", "v114", "v115", "v116", "v117", "v118", "v119", "v120", "v121", "v122", "v123", "v124", "v125", "v126", "v127", "v128", "v129", "v130", "v131", "v132", "v133", "v134", "v135", "v136", "v137", "v138", "v139", "v140", "v141", "v142", "v143", "v144", "v145", "v146", "v147", "v148", "v149", "v150", "v151", "v152", "v153", "v154", "v155", "v156", "v157", "v158", "v159", "v160", "v161", "v162", "v163", "v164", "v165", "v166", "v167", "v168", "v169", "v170", "v171", "v172", "v173", "v174", "v175", "v176", "v177", "v178", "v179", "v180", "v181", "v182", "v183", "v184", "v185", "v186", "v187", "v188", "v189", "v190", "v191", "v192", "v193", "v194", "v195", "v196", "v197", "v198", "v199", "v200", "v201", "v202", "v203", "v204", "v205", "v206", "v207", "v208", "v209", "v210", "v211", "v212", "v213", "v214", "v215", "v216", "v217", "v218", "v219", "v220", "v221", "v222", "v223", "v224", "v225", "v226", "v227", "v228", "v229", "v230", "v231", "v232", "v233", "v234", "v235", "v236", "v237", "v238", "v239", "v240", "v241", "v242", "v243", "v244", "v245", "v246", "v247", "v248", "v249", "v250", "v251", "v252", "v253", "v254", "v255", "a0", "a1", "a2", "a3", "a4", "a5", "a6", "a7", "a8", "a9", "a10", "a11", "a12", "a13", "a14", "a15", "a16", "a17", "a18", "a19", "a20", "a21", "a22", "a23", "a24", "a25", "a26", "a27", "a28", "a29", "a30", "a31", "a32", "a33", "a34", "a35", "a36", "a37", "a38", "a39", "a40", "a41", "a42", "a43", "a44", "a45", "a46", "a47", "a48", "a49", "a50", "a51", "a52", "a53", "a54", "a55", "a56", "a57", "a58", "a59", "a60", "a61", "a62", "a63", "a64", "a65", "a66", "a67", "a68", "a69", "a70", "a71", "a72", "a73", "a74", "a75", "a76", "a77", "a78", "a79", "a80", "a81", "a82", "a83", "a84", "a85", "a86", "a87", "a88", "a89", "a90", "a91", "a92", "a93", "a94", "a95", "a96", "a97", "a98", "a99", "a100", "a101", "a102", "a103", "a104", "a105", "a106", "a107", "a108", "a109", "a110", "a111", "a112", "a113", "a114", "a115", "a116", "a117", "a118", "a119", "a120", "a121", "a122", "a123", "a124", "a125", "a126", "a127", "a128", "a129", "a130", "a131", "a132", "a133", "a134", "a135", "a136", "a137", "a138", "a139", "a140", "a141", "a142", "a143", "a144", "a145", "a146", "a147", "a148", "a149", "a150", "a151", "a152", "a153", "a154", "a155", "a156", "a157", "a158", "a159", "a160", "a161", "a162", "a163", "a164", "a165", "a166", "a167", "a168", "a169", "a170", "a171", "a172", "a173", "a174", "a175", "a176", "a177", "a178", "a179", "a180", "a181", "a182", "a183", "a184", "a185", "a186", "a187", "a188", "a189", "a190", "a191", "a192", "a193", "a194", "a195", "a196", "a197", "a198", "a199", "a200", "a201", "a202", "a203", "a204", "a205", "a206", "a207", "a208", "a209", "a210", "a211", "a212", "a213", "a214", "a215", "a216", "a217", "a218", "a219", "a220", "a221", "a222", "a223", "a224", "a225", "a226", "a227", "a228", "a229", "a230", "a231", "a232", "a233", "a234", "a235", "a236", "a237", "a238", "a239", "a240", "a241", "a242", "a243", "a244", "a245", "a246", "a247", "a248", "a249", "a250", "a251", "a252", "a253", "a254", "a255", "s6", "s7", "s8", "s9", "s10", "s11", "s12", "s13", "s14", "s15", "s16", "s17", "s18", "s19", "s20", "s21", "s22", "s23", "s24", "s25", "s26", "s27", "s28", "s29", "s30", "s31", "s32", "s33", "s34", "s35", "s36", "s37", "s38", "s39", "s40", "s41", "s42", "s43", "s44", "s45", "s46", "s47", "s48", "s49", "s50", "s51", "s52", "s53", "s54", "s55", "s56", "s57", "s58", "s59", "s60", "s61", "s62", "s63", "s64", "s65", "s66", "s67", "s68", "s69", "s70", "s71", "s72", "s73", "s74", "s75", "s76", "s77", "s78", "s79", "s80", "s81", "s82", "s83", "s84", "s85", "s86", "s87", "s88", "s89", "s90", "s91", "s92", "s93", "s94", "s95", "s96", "s97", "s98", "s99", "s100", "s101", "vcc", "scc", "memory");
}

extern "C" void kernel_launch(void* const* d_in, const int* in_sizes, int n_in, void* d_out, int out_size, void* d_ws, size_t ws_size, hipStream_t stream) {
  fused_kernel<<<256, 256, 0, stream>>>((cptr)d_in[0], (cptr)d_in[1], (cptr)d_in[2], (cptr)d_in[3], (cptr)d_in[4],
                                        (cptr)d_in[5], (cptr)d_in[6], (cptr)d_in[7], (cptr)d_in[8], (cptr)d_in[9], (char*)d_ws, (char*)d_out);
}
```
